```python
import torch

def _softmax(x, axis=-1):
    return torch.softmax(x, dim=axis)
def _gelu(x):
    return torch.nn.functional.gelu(x, approximate="tanh")
def _silu(x):
    return torch.nn.functional.silu(x)
def _sigmoid(x):
    return torch.sigmoid(x)
def _layernorm(x, axis=-1, eps=1e-5):
    m = x.mean(dim=axis, keepdim=True)
    v = x.var(dim=axis, keepdim=True, unbiased=False)
    return (x - m) / torch.sqrt(v + eps)
def _scatter_add(tgt, idx, src, axis=0):
    return tgt.clone().index_add_(axis, idx, src)
def _segment_sum(src, seg, n):
    out = torch.zeros((n,) + src.shape[1:], dtype=src.dtype)
    return out.index_add_(0, seg, src)
def _pad(x, pw):
    flat = tuple(v for pair in reversed(pw) for v in pair)
    return torch.nn.functional.pad(x, flat)

kernel_name = 'concat_heads__gqa_block_cached__gqa_block_cached__near_ridge'

def setup_inputs():

    _w = (lambda a: (lambda t: t.to(torch.bfloat16) if t.is_floating_point() else t)(torch.as_tensor(a)))
    def _A_setup():
        B, Sq, D = 8, 112, 448
        Hq, Hkv, Sk, Dh = 16, 8, 2048, 256
        assert Hq % Hkv == 0, (Hq, Hkv)
        return {
            "x": _w((lambda *s: torch.randn(*s, dtype=torch.bfloat16) * 0.8)(B, Sq, D)),
            "k_cache": _w((lambda *s: torch.randn(*s, dtype=torch.bfloat16) * 0.8)(B, Hkv, Sk, Dh)),
            "v_cache": _w((lambda *s: torch.randn(*s, dtype=torch.bfloat16) * 0.05)(B, Hkv, Sk, Dh)),
            "wq": _w((lambda *s: torch.randn(*s, dtype=torch.bfloat16) * 0.8)(D, Hq * Dh) * (D ** -0.5)),
            "wo": _w((lambda *s: torch.randn(*s, dtype=torch.bfloat16) * 0.05)(Hq * Dh, D) * ((Hq * Dh) ** -0.5)),
        }
    _A_in = _A_setup()

    def _B_setup():
        B, Sq, D = 8, 112, 448
        Hq, Hkv, Sk, Dh = 16, 8, 2048, 256
        assert Hq % Hkv == 0, (Hq, Hkv)
        return {
            "x": _w((lambda *s: torch.randn(*s, dtype=torch.bfloat16) * 0.8)(B, Sq, D)),
            "k_cache": _w((lambda *s: torch.randn(*s, dtype=torch.bfloat16) * 0.8)(B, Hkv, Sk, Dh)),
            "v_cache": _w((lambda *s: torch.randn(*s, dtype=torch.bfloat16) * 0.05)(B, Hkv, Sk, Dh)),
            "wq": _w((lambda *s: torch.randn(*s, dtype=torch.bfloat16) * 0.8)(D, Hq * Dh) * (D ** -0.5)),
            "wo": _w((lambda *s: torch.randn(*s, dtype=torch.bfloat16) * 0.05)(Hq * Dh, D) * ((Hq * Dh) ** -0.5)),
        }
    _B_in = _B_setup()

    return {
        "A_x": _A_in["x"],
        "A_k_cache": _A_in["k_cache"],
        "A_v_cache": _A_in["v_cache"],
        "A_wq": _A_in["wq"],
        "A_wo": _A_in["wo"],
        "B_x": _B_in["x"],
        "B_k_cache": _B_in["k_cache"],
        "B_v_cache": _B_in["v_cache"],
        "B_wq": _B_in["wq"],
        "B_wo": _B_in["wo"],
    }

def reference(A_x, A_k_cache, A_v_cache, A_wq, A_wo, B_x, B_k_cache, B_v_cache, B_wq, B_wo):
    def _A_ref(x, k_cache, v_cache, wq, wo):
        B, Sq, D = x.shape
        Hq, Dh = 16, 256
        Hkv = k_cache.shape[1]
        rep = Hq // Hkv
        q = (lambda x, *d: x.permute(*d))((x @ wq).reshape(B, Sq, Hq, Dh), 0, 2, 1, 3)
        k = (lambda x, n, axis: torch.repeat_interleave(x, n, dim=axis))(k_cache, rep, axis=1)
        v = (lambda x, n, axis: torch.repeat_interleave(x, n, dim=axis))(v_cache, rep, axis=1)
        scores = torch.einsum("bhqd,bhkd->bhqk", q, k) * (Dh ** -0.5)
        p = _softmax(scores, axis=-1)
        o = torch.einsum("bhqk,bhkd->bhqd", p, v)
        return (lambda x, *d: x.permute(*d))(o, 0, 2, 1, 3).reshape(B, Sq, Hq * Dh) @ wo

    def _B_ref(x, k_cache, v_cache, wq, wo):
        B, Sq, D = x.shape
        Hq, Dh = 16, 256
        Hkv = k_cache.shape[1]
        rep = Hq // Hkv
        q = (lambda x, *d: x.permute(*d))((x @ wq).reshape(B, Sq, Hq, Dh), 0, 2, 1, 3)
        k = (lambda x, n, axis: torch.repeat_interleave(x, n, dim=axis))(k_cache, rep, axis=1)
        v = (lambda x, n, axis: torch.repeat_interleave(x, n, dim=axis))(v_cache, rep, axis=1)
        scores = torch.einsum("bhqd,bhkd->bhqk", q, k) * (Dh ** -0.5)
        p = _softmax(scores, axis=-1)
        o = torch.einsum("bhqk,bhkd->bhqd", p, v)
        return (lambda x, *d: x.permute(*d))(o, 0, 2, 1, 3).reshape(B, Sq, Hq * Dh) @ wo

    a = _A_ref(A_x, A_k_cache, A_v_cache, A_wq, A_wo)
    b = _B_ref(B_x, B_k_cache, B_v_cache, B_wq, B_wo)
    return torch.concatenate([a, b], axis=-1)
```

```cpp
#include <hip/hip_runtime.h>
#include <hip/hip_bf16.h>
#include <stdint.h>

constexpr int B=8, SQ=112, D=448, HQ=16, HKV=8, SK=2048, DH=256;
typedef unsigned short u16;
__device__ __forceinline__ float bf2f(u16 v){ return __uint_as_float(((unsigned)v)<<16); }
__device__ __forceinline__ u16 f2bf(float f){ unsigned u=__float_as_uint(f); u += 0x7FFF + ((u>>16)&1); return (u16)(u>>16); }

__global__ void qproj(const u16* x0,const u16* w0,const u16* x1,const u16* w1,float* q){
  int br=blockIdx.z; const u16* x=br?x1:x0; const u16* w=br?w1:w0;
  int row=blockIdx.y;
  int n=blockIdx.x*256+threadIdx.x;
  float acc=0;
  for(int c=0;c<D;c++) acc += bf2f(x[row*D+c])*bf2f(w[(size_t)c*(HQ*DH)+n]);
  int b=row/SQ,s=row%SQ,h=n/DH,d=n%DH;
  q[((((size_t)br*B+b)*HQ+h)*SQ+s)*DH+d]=bf2f(f2bf(acc));
}
__global__ void attn(const float* q,const u16* k0,const u16* v0,const u16* k1,const u16* v1,float* o){
  __shared__ float qs[DH]; __shared__ float p[SK]; __shared__ float red[256];
  int s=blockIdx.x,h=blockIdx.y,bb=blockIdx.z; int br=bb/B,b=bb%B;
  const u16* k=(br?k1:k0)+((size_t)b*HKV+h/2)*SK*DH; const u16* v=(br?v1:v0)+((size_t)b*HKV+h/2)*SK*DH;
  int t=threadIdx.x;
  qs[t]=q[((((size_t)br*B+b)*HQ+h)*SQ+s)*DH+t]; __syncthreads();
  float mx=-1e30f;

#pragma unroll 1
  for(int j=t;j<SK;j+=256){ float a=0; const uint4* kr=(const uint4*)(k+(size_t)j*DH);
#pragma unroll 2
    for(int c=0;c<DH/8;c++){ uint4 w=kr[c];
      a+=qs[c*8+0]*__uint_as_float(w.x<<16)+qs[c*8+1]*__uint_as_float(w.x&0xffff0000u);
      a+=qs[c*8+2]*__uint_as_float(w.y<<16)+qs[c*8+3]*__uint_as_float(w.y&0xffff0000u);
      a+=qs[c*8+4]*__uint_as_float(w.z<<16)+qs[c*8+5]*__uint_as_float(w.z&0xffff0000u);
      a+=qs[c*8+6]*__uint_as_float(w.w<<16)+qs[c*8+7]*__uint_as_float(w.w&0xffff0000u);
    }
    a*=0.0625f; p[j]=a; mx=fmaxf(mx,a);}
  red[t]=mx; __syncthreads();
  for(int o2=128;o2>0;o2>>=1){ if(t<o2) red[t]=fmaxf(red[t],red[t+o2]); __syncthreads(); }
  mx=red[0]; __syncthreads();
  float sm=0; for(int j=t;j<SK;j+=256){ float e=__expf(p[j]-mx); p[j]=e; sm+=e; }
  red[t]=sm; __syncthreads();
  for(int o2=128;o2>0;o2>>=1){ if(t<o2) red[t]+=red[t+o2]; __syncthreads(); }
  sm=red[0];
  float acc=0; for(int j=0;j<SK;j++) acc+=p[j]*bf2f(v[(size_t)j*DH+t]);
  o[(((size_t)br*B+b)*SQ+s)*(HQ*DH)+h*DH+t]=bf2f(f2bf(acc/sm));
}
__global__ void oproj(const float* o,const u16* w0,const u16* w1,u16* out){
  int br=blockIdx.z; const u16* w=br?w1:w0; int row=blockIdx.y; int n=blockIdx.x*64+threadIdx.x;
  const float* orow=o+((size_t)br*B*SQ+row)*(HQ*DH);
  float acc=0; for(int c=0;c<HQ*DH;c++) acc+=orow[c]*bf2f(w[(size_t)c*D+n]);
  out[(size_t)row*(2*D)+br*D+n]=f2bf(acc);
}
extern "C" void kernel_launch(void* const* d_in, const int* in_sizes, int n_in, void* d_out, int out_size, void* d_ws, size_t ws_size, hipStream_t stream) {
  const u16* Ax=(const u16*)d_in[0]; const u16* Ak=(const u16*)d_in[1]; const u16* Av=(const u16*)d_in[2]; const u16* Awq=(const u16*)d_in[3]; const u16* Awo=(const u16*)d_in[4];
  const u16* Bx=(const u16*)d_in[5]; const u16* Bk=(const u16*)d_in[6]; const u16* Bv=(const u16*)d_in[7]; const u16* Bwq=(const u16*)d_in[8]; const u16* Bwo=(const u16*)d_in[9];
  float* q=(float*)d_ws; float* o=q+(size_t)2*B*HQ*SQ*DH;
  qproj<<<dim3(HQ*DH/256,B*SQ,2),256,0,stream>>>(Ax,Awq,Bx,Bwq,q);
  attn<<<dim3(SQ,HQ,2*B),256,0,stream>>>(q,Ak,Av,Bk,Bv,o);
  oproj<<<dim3(D/64,B*SQ,2),64,0,stream>>>(o,Awo,Bwo,(u16*)d_out);
}
```

```cpp
#include <hip/hip_runtime.h>
#include <hip/hip_bf16.h>
#include <stdint.h>

constexpr int B=8, SQ=112, D=448, HQ=16, HKV=8, SK=2048, DH=256;
typedef unsigned short u16;
__device__ __forceinline__ float bf2f(u16 v){ return __uint_as_float(((unsigned)v)<<16); }
__device__ __forceinline__ u16 f2bf(float f){ unsigned u=__float_as_uint(f); u += 0x7FFF + ((u>>16)&1); return (u16)(u>>16); }

__global__ void qproj(const u16* x0,const u16* w0,const u16* x1,const u16* w1,float* q){
  int br=blockIdx.z; const u16* x=br?x1:x0; const u16* w=br?w1:w0;
  int row=blockIdx.y;
  int n=blockIdx.x*256+threadIdx.x;
  float acc=0;
  for(int c=0;c<D;c++) acc += bf2f(x[row*D+c])*bf2f(w[(size_t)c*(HQ*DH)+n]);
  int b=row/SQ,s=row%SQ,h=n/DH,d=n%DH;
  q[((((size_t)br*B+b)*HQ+h)*SQ+s)*DH+d]=bf2f(f2bf(acc));
}
__global__ void attn(const float* q,const u16* k0,const u16* v0,const u16* k1,const u16* v1,float* o){
  __shared__ float qs[DH]; __shared__ float p[SK]; __shared__ float red[256];
  int s=blockIdx.x,h=blockIdx.y,bb=blockIdx.z; int br=bb/B,b=bb%B;
  const u16* k=(br?k1:k0)+((size_t)b*HKV+h/2)*SK*DH; const u16* v=(br?v1:v0)+((size_t)b*HKV+h/2)*SK*DH;
  int t=threadIdx.x;
  qs[t]=q[((((size_t)br*B+b)*HQ+h)*SQ+s)*DH+t]; __syncthreads();
  float mx=-1e30f;

#pragma unroll 1
  for(int j=t;j<SK;j+=256){ float a=0; const uint4* kr=(const uint4*)(k+(size_t)j*DH);
#pragma unroll 2
    for(int c=0;c<DH/8;c++){ uint4 w=kr[c];
      a+=qs[c*8+0]*__uint_as_float(w.x<<16)+qs[c*8+1]*__uint_as_float(w.x&0xffff0000u);
      a+=qs[c*8+2]*__uint_as_float(w.y<<16)+qs[c*8+3]*__uint_as_float(w.y&0xffff0000u);
      a+=qs[c*8+4]*__uint_as_float(w.z<<16)+qs[c*8+5]*__uint_as_float(w.z&0xffff0000u);
      a+=qs[c*8+6]*__uint_as_float(w.w<<16)+qs[c*8+7]*__uint_as_float(w.w&0xffff0000u);
    }
    a*=0.0625f; p[j]=a; mx=fmaxf(mx,a);}
  red[t]=mx; __syncthreads();
  for(int o2=128;o2>0;o2>>=1){ if(t<o2) red[t]=fmaxf(red[t],red[t+o2]); __syncthreads(); }
  mx=red[0]; __syncthreads();
  float sm=0; for(int j=t;j<SK;j+=256){ float e=__expf(p[j]-mx); p[j]=e; sm+=e; }
  red[t]=sm; __syncthreads();
  for(int o2=128;o2>0;o2>>=1){ if(t<o2) red[t]+=red[t+o2]; __syncthreads(); }
  sm=red[0];
  float acc=0; for(int j=0;j<SK;j++) acc+=p[j]*bf2f(v[(size_t)j*DH+t]);
  o[(((size_t)br*B+b)*SQ+s)*(HQ*DH)+h*DH+t]=bf2f(f2bf(acc/sm));
}
__global__ void oproj(const float* o,const u16* w0,const u16* w1,u16* out){
  int br=blockIdx.z; const u16* w=br?w1:w0; int row=blockIdx.y; int n=blockIdx.x*64+threadIdx.x;
  const float* orow=o+((size_t)br*B*SQ+row)*(HQ*DH);
  float acc=0; for(int c=0;c<HQ*DH;c++) acc+=orow[c]*bf2f(w[(size_t)c*D+n]);
  out[(size_t)row*(2*D)+br*D+n]=f2bf(acc);
}

typedef unsigned long long u64;
template<int DEPTH,int NT>
__global__ __launch_bounds__(256,1) void stream(const char* k0,const char* v0,const char* k1,const char* v1){
  extern __shared__ __attribute__((aligned(16))) char lds[];
  const int lane=threadIdx.x&63; const int w=__builtin_amdgcn_readfirstlane(threadIdx.x>>6);
  int bid=blockIdx.x;
  int xcd=bid&7, idx=bid>>3;
  int unit = xcd*16 + (idx>>1);
  int br=unit>>6, u=unit&63;
  const char* kb=(br?k1:k0)+(size_t)u*2048*512;
  const char* vb=(br?v1:v0)+(size_t)u*2048*512;
  __amdgpu_buffer_rsrc_t srdK=__builtin_amdgcn_make_buffer_rsrc((void*)kb,(short)0,2048*512,0x00020000);
  __amdgpu_buffer_rsrc_t srdV=__builtin_amdgcn_make_buffer_rsrc((void*)vb,(short)0,2048*512,0x00020000);
  unsigned voff=(lane>>3)*512+16*((lane&7)^((lane>>3)&6));
  unsigned ldsb=(unsigned)(size_t)lds + w*32768;
  for(int t=0;t<16;t++){
    unsigned tb=t*65536;
    #pragma unroll
    for(int i=0;i<16;i++){
      unsigned so, m0v=ldsb+i*1024;
      so=tb+(w*32+(i>>2)*8)*512+(i&3)*128;
      so=__builtin_amdgcn_readfirstlane(so); m0v=__builtin_amdgcn_readfirstlane(m0v);
      #define LD(P,M,S,O) asm volatile("s_mov_b32 m0,%0\n\ts_nop 0\n\tbuffer_load_dwordx4 %1,%2,%3 offen " P " lds\n\ts_waitcnt vmcnt(%c4)"::"s"(M),"v"(voff),"s"(S),"s"(O),"i"(DEPTH):"memory")
      if(NT) LD("nt",m0v,srdK,so); else LD("",m0v,srdK,so);
      unsigned so2, m0w=ldsb+16384+i*1024;
      so2=tb+i*8*512+w*128;
      so2=__builtin_amdgcn_readfirstlane(so2); m0w=__builtin_amdgcn_readfirstlane(m0w);
      if(NT) LD("nt",m0w,srdV,so2); else LD("",m0w,srdV,so2);
    }
  }
  asm volatile("s_waitcnt vmcnt(0)":::"memory");
}
extern "C" void kernel_launch(void* const* d_in, const int* in_sizes, int n_in, void* d_out, int out_size, void* d_ws, size_t ws_size, hipStream_t stream_) {
  const u16* Ax=(const u16*)d_in[0]; const u16* Ak=(const u16*)d_in[1]; const u16* Av=(const u16*)d_in[2]; const u16* Awq=(const u16*)d_in[3]; const u16* Awo=(const u16*)d_in[4];
  const u16* Bx=(const u16*)d_in[5]; const u16* Bk=(const u16*)d_in[6]; const u16* Bv=(const u16*)d_in[7]; const u16* Bwq=(const u16*)d_in[8]; const u16* Bwo=(const u16*)d_in[9];
  hipFuncSetAttribute((const void*)stream<24,0>,hipFuncAttributeMaxDynamicSharedMemorySize,163840);
  stream<24,0><<<256,256,163840,stream_>>>((const char*)Ak,(const char*)Av,(const char*)Bk,(const char*)Bv);
  float* q=(float*)d_ws; float* o=q+(size_t)2*B*HQ*SQ*DH;
  qproj<<<dim3(HQ*DH/256,B*SQ,2),256,0,stream_>>>(Ax,Awq,Bx,Bwq,q);
  attn<<<dim3(SQ,HQ,2*B),256,0,stream_>>>(q,Ak,Av,Bk,Bv,o);
  oproj<<<dim3(D/64,B*SQ,2),64,0,stream_>>>(o,Awo,Bwo,(u16*)d_out);
}
```

```cpp
#include <hip/hip_runtime.h>
#include <hip/hip_bf16.h>
#include <stdint.h>

namespace {
constexpr int kB = 8, kSQ = 112, kD = 448, kHQ = 16, kHKV = 8, kSK = 2048, kDH = 256;
typedef unsigned long long u64;

__global__ __launch_bounds__(256, 1) void gqa_block(const char* __restrict__ ax, const char* __restrict__ ak, const char* __restrict__ av,
    const char* __restrict__ awq, const char* __restrict__ awo, const char* __restrict__ bx, const char* __restrict__ bk,
    const char* __restrict__ bv, const char* __restrict__ bwq, const char* __restrict__ bwo, char* __restrict__ part) {
  __shared__ __attribute__((aligned(16))) char lds[163840];
  const unsigned bid = blockIdx.x;
  const unsigned g8 = bid & 7u, idx = bid >> 3;
  const unsigned br = g8 >> 2, kvh = 2u * (g8 & 3u) + (idx >> 4), b = (idx >> 1) & 7u, half = idx & 1u;
  const char* x = (br ? bx : ax) + (size_t)(b * kSQ + 56u * half) * kD * 2;
  const char* wq = (br ? bwq : awq) + (size_t)kvh * 2 * kDH * 2;
  const char* k = (br ? bk : ak) + (size_t)(b * kHKV + kvh) * kSK * kDH * 2;
  const char* v = (br ? bv : av) + (size_t)(b * kHKV + kvh) * kSK * kDH * 2;
  const char* wo = (br ? bwo : awo) + (size_t)kvh * 2 * kDH * kD * 2;
  char* pp = part + ((size_t)((br * kB + b) * 2 + half) * 56 * 8 + kvh) * 1024;
  const unsigned rotq = b % 7u, roto = (idx >> 1) & 7u, hsw = 0u;
  unsigned tid = threadIdx.x + (unsigned)(size_t)lds;
  asm volatile(
    "v_and_b32 v254, 63, v255\n"
    "v_lshrrev_b32 v239, 6, v255\n"
    "v_lshrrev_b32 v240, 3, v254\n"
    "v_readfirstlane_b32 s36, v239\n"
    "v_and_b32 v241, 7, v254\n"
    "v_and_b32 v242, 6, v240\n"
    "v_xor_b32 v242, v242, v241\n"
    "v_lshlrev_b32 v242, 4, v242\n"
    "v_mul_u32_u24 v243, 896, v240\n"
    "v_add_u32 v202, v242, v243\n"
    "s_mov_b32 s22, 0xc400\n"
    "s_mov_b32 s23, 0x20000\n"
    "s_mov_b32 s40, 0\n"
    "s_mov_b32 s41, 0x1800\n"
    "s_mov_b32 s42, 0x3000\n"
    "s_mov_b32 s43, 0x4800\n"
    "s_mov_b32 s44, 0x7000\n"
    "s_mov_b32 s45, 0x8800\n"
    "s_mov_b32 s46, 0xa000\n"
    "s_mov_b32 s47, 0xb800\n"
    "v_lshlrev_b32 v243, 9, v240\n"
    "v_add_u32 v234, v242, v243\n"
    "s_mov_b32 s38, s36\n"
    "s_lshl_b32 s80, s38, 13\n"
    "s_add_u32 s38, s38, s16\n"
    "s_cmp_ge_u32 s38, 7\n"
    "s_cselect_b32 s39, 7, 0\n"
    "s_sub_u32 s38, s38, s39\n"
    "s_lshl_b32 s38, s38, 7\n"
    "s_add_u32 s20, s4, s38\n"
    "s_addc_u32 s21, s5, 0\n"
    "s_and_b32 s21, s21, 0xffff\n"
    "s_mov_b32 m0, s80\n"
    "s_nop 0\n"
    "buffer_load_dwordx4 v202, s[20:23], s40 offen lds\n"
    "buffer_load_dwordx4 v202, s[20:23], s41 offen offset:1024 lds\n"
    "buffer_load_dwordx4 v202, s[20:23], s42 offen offset:2048 lds\n"
    "buffer_load_dwordx4 v202, s[20:23], s43 offen offset:3072 lds\n"
    "s_add_u32 s81, s80, 4096\n"
    "s_mov_b32 m0, s81\n"
    "s_nop 0\n"
    "buffer_load_dwordx4 v202, s[20:23], s44 offen lds\n"
    "buffer_load_dwordx4 v202, s[20:23], s45 offen offset:1024 lds\n"
    "buffer_load_dwordx4 v202, s[20:23], s46 offen offset:2048 lds\n"
    "buffer_load_dwordx4 v202, s[20:23], s47 offen offset:3072 lds\n"
    "v_and_b32 v242, 2, v240\n"
    "v_xor_b32 v242, v242, v241\n"
    "v_lshlrev_b32 v242, 4, v242\n"
    "v_lshlrev_b32 v243, 13, v240\n"
    "v_add_u32 v200, v242, v243\n"
    "v_xor_b32 v201, 64, v200\n"
    "s_lshl_b32 s38, s36, 7\n"
    "s_add_u32 s83, s6, s38\n"
    "s_addc_u32 s84, s7, 0\n"
    "s_and_b32 s84, s84, 0xffff\n"
    "s_mul_i32 s82, s16, 524288\n"
    "s_mov_b32 s26, 0x40000\n"
    "s_mov_b32 s27, 0x20000\n"
    "s_lshl_b32 s38, s99, 9\n"
    "s_xor_b32 s39, s38, 512\n"
    "s_add_u32 s56, s38, 0\n"
    "s_add_u32 s60, s39, 0\n"
    "s_add_u32 s57, s38, 64512\n"
    "s_add_u32 s61, s39, 64512\n"
    "s_add_u32 s58, s38, 129024\n"
    "s_add_u32 s62, s39, 129024\n"
    "s_add_u32 s59, s38, 193536\n"
    "s_add_u32 s63, s39, 193536\n"
    "s_lshl_b32 s38, s36, 14\n"
    "s_add_u32 s85, s38, 65536\n"
    "s_lshl_b32 s38, s36, 13\n"
    "s_add_u32 s86, s38, 131072\n"
    "s_add_u32 s24, s83, s82\n"
    "s_addc_u32 s25, s84, 0\n"
    "s_mov_b32 m0, s85\n"
    "s_nop 0\n"
    "buffer_load_dwordx4 v200, s[24:27], s56 offen lds\n"
    "buffer_load_dwordx4 v201, s[24:27], s57 offen offset:1024 lds\n"
    "buffer_load_dwordx4 v200, s[24:27], s58 offen offset:2048 lds\n"
    "buffer_load_dwordx4 v201, s[24:27], s59 offen offset:3072 lds\n"
    "s_add_u32 s82, s82, 262144\n"
    "s_cmp_ge_u32 s82, 3670016\n"
    "s_cselect_b32 s82, 0, s82\n"
    "s_add_u32 s24, s83, s82\n"
    "s_addc_u32 s25, s84, 0\n"
    "s_add_u32 s38, s85, 4096\n"
    "s_mov_b32 m0, s38\n"
    "s_nop 0\n"
    "buffer_load_dwordx4 v200, s[24:27], s56 offen lds\n"
    "buffer_load_dwordx4 v201, s[24:27], s57 offen offset:1024 lds\n"
    "buffer_load_dwordx4 v200, s[24:27], s58 offen offset:2048 lds\n"
    "buffer_load_dwordx4 v201, s[24:27], s59 offen offset:3072 lds\n"
    "s_add_u32 s82, s82, 262144\n"
    "s_cmp_ge_u32 s82, 3670016\n"
    "s_cselect_b32 s82, 0, s82\n"
    "v_and_b32 v244, 15, v254\n"
    "v_lshrrev_b32 v245, 4, v254\n"
    "v_and_b32 v239, 1, v245\n"
    "v_lshlrev_b32 v239, 1, v239\n"
    "v_bfe_u32 v240, v254, 3, 1\n"
    "v_add_u32 v239, v239, v240\n"
    "v_lshlrev_b32 v246, 5, v239\n"
    "v_and_b32 v239, 3, v254\n"
    "v_lshlrev_b32 v239, 3, v239\n"
    "v_add_u32 v246, v246, v239\n"
    "v_bfe_u32 v247, v254, 2, 2\n"
    "v_lshlrev_b32 v239, 10, v245\n"
    "v_lshlrev_b32 v240, 7, v247\n"
    "v_add_u32 v239, v239, v240\n"
    "v_xor_b32 v240, 0, v246\n"
    "v_add_u32 v240, v240, v239\n"
    "v_add_u32 v192, s85, v240\n"
    "v_add_u32 v196, s86, v240\n"
    "v_xor_b32 v240, 32, v246\n"
    "v_add_u32 v240, v240, v239\n"
    "v_add_u32 v193, s85, v240\n"
    "v_add_u32 v197, s86, v240\n"
    "v_xor_b32 v240, 64, v246\n"
    "v_add_u32 v240, v240, v239\n"
    "v_add_u32 v194, s85, v240\n"
    "v_add_u32 v198, s86, v240\n"
    "v_xor_b32 v240, 96, v246\n"
    "v_add_u32 v240, v240, v239\n"
    "v_add_u32 v195, s85, v240\n"
    "v_add_u32 v199, s86, v240\n"
    "v_lshrrev_b32 v239, 3, v244\n"
    "v_lshlrev_b32 v239, 10, v239\n"
    "v_and_b32 v240, 7, v244\n"
    "v_lshlrev_b32 v240, 7, v240\n"
    "v_add_u32 v239, v239, v240\n"
    "v_and_b32 v240, 6, v244\n"
    "v_xor_b32 v240, v240, v245\n"
    "v_lshlrev_b32 v240, 4, v240\n"
    "v_add_u32 v203, v239, v240\n"
    "v_xor_b32 v204, 64, v203\n"
    "v_lshrrev_b32 v239, 3, v244\n"
    "v_lshlrev_b32 v239, 12, v239\n"
    "v_and_b32 v241, 7, v244\n"
    "v_lshlrev_b32 v241, 7, v241\n"
    "v_add_u32 v239, v239, v241\n"
    "v_add_u32 v239, v239, v240\n"
    "s_lshl_b32 s38, s36, 14\n"
    "v_add_u32 v223, s38, v239\n"
    "v_xor_b32 v224, 64, v223\n"
    "v_lshrrev_b32 v239, 1, v245\n"
    "v_lshlrev_b32 v239, 10, v239\n"
    "v_and_b32 v240, 1, v245\n"
    "v_lshlrev_b32 v240, 2, v240\n"
    "v_add_u32 v240, v240, v247\n"
    "v_lshlrev_b32 v240, 7, v240\n"
    "v_add_u32 v239, v239, v240\n"
    "v_add_u32 v239, s85, v239\n"
    "v_xor_b32 v240, 0, v246\n"
    "v_add_u32 v225, v239, v240\n"
    "v_xor_b32 v240, 32, v246\n"
    "v_add_u32 v226, v239, v240\n"
    "v_xor_b32 v240, 64, v246\n"
    "v_add_u32 v227, v239, v240\n"
    "v_xor_b32 v240, 96, v246\n"
    "v_add_u32 v228, v239, v240\n"
    "v_lshlrev_b32 v239, 4, v254\n"
    "v_add_u32 v239, 131072, v239\n"
    "s_lshl_b32 s38, s36, 10\n"
    "v_add_u32 v229, s38, v239\n"
    "s_add_u32 s38, s36, 0\n"
    "s_and_b32 s38, s38, 3\n"
    "s_lshl_b32 s38, s38, 10\n"
    "v_add_u32 v230, s38, v239\n"
    "s_add_u32 s38, s36, 1\n"
    "s_and_b32 s38, s38, 3\n"
    "s_lshl_b32 s38, s38, 10\n"
    "v_add_u32 v231, s38, v239\n"
    "s_add_u32 s38, s36, 2\n"
    "s_and_b32 s38, s38, 3\n"
    "s_lshl_b32 s38, s38, 10\n"
    "v_add_u32 v232, s38, v239\n"
    "s_add_u32 s38, s36, 3\n"
    "s_and_b32 s38, s38, 3\n"
    "s_lshl_b32 s38, s38, 10\n"
    "v_add_u32 v233, s38, v239\n"
    "v_mov_b32 v239, 0x3f803f80\n"
    "v_accvgpr_write_b32 a252, v239\n"
    "v_accvgpr_write_b32 a253, v239\n"
    "v_accvgpr_write_b32 a254, v239\n"
    "v_accvgpr_write_b32 a255, v239\n"
    "v_accvgpr_write_b32 a224, 0\n"
    "v_accvgpr_write_b32 a225, 0\n"
    "v_accvgpr_write_b32 a226, 0\n"
    "v_accvgpr_write_b32 a227, 0\n"
    "v_accvgpr_write_b32 a228, 0\n"
    "v_accvgpr_write_b32 a229, 0\n"
    "v_accvgpr_write_b32 a230, 0\n"
    "v_accvgpr_write_b32 a231, 0\n"
    "v_accvgpr_write_b32 a232, 0\n"
    "v_accvgpr_write_b32 a233, 0\n"
    "v_accvgpr_write_b32 a234, 0\n"
    "v_accvgpr_write_b32 a235, 0\n"
    "v_accvgpr_write_b32 a236, 0\n"
    "v_accvgpr_write_b32 a237, 0\n"
    "v_accvgpr_write_b32 a238, 0\n"
    "v_accvgpr_write_b32 a239, 0\n"
    "v_accvgpr_write_b32 a240, 0\n"
    "v_accvgpr_write_b32 a241, 0\n"
    "v_accvgpr_write_b32 a242, 0\n"
    "v_accvgpr_write_b32 a243, 0\n"
    "v_accvgpr_write_b32 a244, 0\n"
    "v_accvgpr_write_b32 a245, 0\n"
    "v_accvgpr_write_b32 a246, 0\n"
    "v_accvgpr_write_b32 a247, 0\n"
    "v_accvgpr_write_b32 a248, 0\n"
    "v_accvgpr_write_b32 a249, 0\n"
    "v_accvgpr_write_b32 a250, 0\n"
    "v_accvgpr_write_b32 a251, 0\n"
    "v_lshrrev_b32 v239, 1, v245\n"
    "s_lshl_b32 s38, s36, 3\n"
    "v_add_u32 v239, s38, v239\n"
    "v_xor_b32 v239, v239, v244\n"
    "v_lshlrev_b32 v239, 4, v239\n"
    "v_and_b32 v240, 1, v245\n"
    "v_lshlrev_b32 v240, 3, v240\n"
    "v_lshlrev_b32 v241, 9, v244\n"
    "v_add_u32 v240, v240, v241\n"
    "v_add_u32 v240, 131072, v240\n"
    "v_add_u32 v241, v239, v240\n"
    "v_xor_b32 v239, 128, v239\n"
    "v_add_u32 v242, v239, v240\n"
    "v_add_u32 v242, 28672, v242\n"
    "v_xor_b32 v160, 0, v241\n"
    "v_xor_b32 v164, 0, v242\n"
    "v_xor_b32 v161, 32, v241\n"
    "v_xor_b32 v165, 32, v242\n"
    "v_xor_b32 v162, 64, v241\n"
    "v_xor_b32 v166, 64, v242\n"
    "v_xor_b32 v163, 96, v241\n"
    "v_xor_b32 v167, 96, v242\n"
    "v_xor_b32 v239, v245, v244\n"
    "v_lshlrev_b32 v239, 4, v239\n"
    "v_xor_b32 v240, 128, v239\n"
    "v_lshlrev_b32 v241, 9, v244\n"
    "v_add_u32 v241, 131072, v241\n"
    "v_add_u32 v168, v239, v241\n"
    "v_add_u32 v169, v240, v241\n"
    "v_add_u32 v169, 4096, v169\n"
    "v_cmp_gt_u32 vcc, 8, v244\n"
    "v_add_u32 v242, 24576, v168\n"
    "v_add_u32 v243, v240, v241\n"
    "v_add_u32 v243, 4294963200, v243\n"
    "v_cndmask_b32 v170, v243, v242, vcc\n"
    "v_add_u32 v243, 16384, v169\n"
    "v_cndmask_b32 v171, v242, v243, vcc\n"
    "s_mov_b64 s[92:93], vcc\n"
    "s_mov_b32 s88, 0x3db8aa3b\n"
    "s_mov_b32 s89, 0x3db8aa3b\n"
    "s_waitcnt vmcnt(8)\n"
    "s_barrier\n"
    "ds_read_b128 a[0:3], v203\n"
    "ds_read_b128 a[4:7], v203 offset:2048\n"
    "ds_read_b128 a[8:11], v203 offset:4096\n"
    "ds_read_b128 a[12:15], v203 offset:6144\n"
    "s_waitcnt vmcnt(4)\n"
    "ds_read_b64_tr_b16 v[128:129], v192\n"
    "ds_read_b64_tr_b16 v[130:131], v192 offset:512\n"
    "ds_read_b64_tr_b16 v[132:133], v193\n"
    "ds_read_b64_tr_b16 v[134:135], v193 offset:512\n"
    "ds_read_b64_tr_b16 v[136:137], v194\n"
    "ds_read_b64_tr_b16 v[138:139], v194 offset:512\n"
    "ds_read_b64_tr_b16 v[140:141], v195\n"
    "ds_read_b64_tr_b16 v[142:143], v195 offset:512\n"
    "ds_read_b128 a[16:19], v204\n"
    "ds_read_b128 a[20:23], v204 offset:2048\n"
    "ds_read_b128 a[24:27], v204 offset:4096\n"
    "ds_read_b128 a[28:31], v204 offset:6144\n"
    "s_add_u32 s38, s36, 4\n"
    "s_min_u32 s38, s38, 6\n"
    "s_lshl_b32 s80, s38, 13\n"
    "s_add_u32 s38, s38, s16\n"
    "s_cmp_ge_u32 s38, 7\n"
    "s_cselect_b32 s39, 7, 0\n"
    "s_sub_u32 s38, s38, s39\n"
    "s_lshl_b32 s38, s38, 7\n"
    "s_add_u32 s20, s4, s38\n"
    "s_addc_u32 s21, s5, 0\n"
    "s_and_b32 s21, s21, 0xffff\n"
    "s_waitcnt lgkmcnt(10)\n"
    "v_mfma_f32_16x16x32_bf16 v[0:3], v[128:131], a[0:3], 0\n"
    "s_add_u32 s24, s83, s82\n"
    "s_addc_u32 s25, s84, 0\n"
    "s_add_u32 s38, s85, 8192\n"
    "s_mov_b32 m0, s38\n"
    "v_mfma_f32_16x16x32_bf16 v[4:7], v[128:131], a[4:7], 0\n"
    "buffer_load_dwordx4 v200, s[24:27], s56 offen lds\n"
    "v_mfma_f32_16x16x32_bf16 v[8:11], v[128:131], a[8:11], 0\n"
    "buffer_load_dwordx4 v201, s[24:27], s57 offen offset:1024 lds\n"
    "v_mfma_f32_16x16x32_bf16 v[12:15], v[128:131], a[12:15], 0\n"
    "buffer_load_dwordx4 v200, s[24:27], s58 offen offset:2048 lds\n"
    "s_waitcnt lgkmcnt(8)\n"
    "v_mfma_f32_16x16x32_bf16 v[16:19], v[132:135], a[0:3], 0\n"
    "buffer_load_dwordx4 v201, s[24:27], s59 offen offset:3072 lds\n"
    "s_add_u32 s82, s82, 262144\n"
    "s_cmp_ge_u32 s82, 3670016\n"
    "s_cselect_b32 s82, 0, s82\n"
    "s_add_u32 s24, s83, s82\n"
    "s_addc_u32 s25, s84, 0\n"
    "s_add_u32 s38, s85, 12288\n"
    "s_mov_b32 m0, s38\n"
    "ds_read_b128 a[32:35], v203 offset:8192\n"
    "v_mfma_f32_16x16x32_bf16 v[20:23], v[132:135], a[4:7], 0\n"
    "buffer_load_dwordx4 v200, s[24:27], s56 offen lds\n"
    "ds_read_b128 a[36:39], v203 offset:10240\n"
    "v_mfma_f32_16x16x32_bf16 v[24:27], v[132:135], a[8:11], 0\n"
    "buffer_load_dwordx4 v201, s[24:27], s57 offen offset:1024 lds\n"
    "ds_read_b128 a[40:43], v203 offset:12288\n"
    "v_mfma_f32_16x16x32_bf16 v[28:31], v[132:135], a[12:15], 0\n"
    "buffer_load_dwordx4 v200, s[24:27], s58 offen offset:2048 lds\n"
    "ds_read_b128 a[44:47], v203 offset:14336\n"
    "s_waitcnt lgkmcnt(10)\n"
    "v_mfma_f32_16x16x32_bf16 v[32:35], v[136:139], a[0:3], 0\n"
    "buffer_load_dwordx4 v201, s[24:27], s59 offen offset:3072 lds\n"
    "s_add_u32 s82, s82, 262144\n"
    "s_cmp_ge_u32 s82, 3670016\n"
    "s_cselect_b32 s82, 0, s82\n"
    "s_add_u32 s24, s83, s82\n"
    "s_addc_u32 s25, s84, 0\n"
    "s_mov_b32 m0, s86\n"
    "ds_read_b128 a[48:51], v204 offset:8192\n"
    "v_mfma_f32_16x16x32_bf16 v[36:39], v[136:139], a[4:7], 0\n"
    "buffer_load_dwordx4 v200, s[24:27], s56 offen lds\n"
    "v_mfma_f32_16x16x32_bf16 v[40:43], v[136:139], a[8:11], 0\n"
    "buffer_load_dwordx4 v201, s[24:27], s57 offen offset:1024 lds\n"
    "v_mfma_f32_16x16x32_bf16 v[44:47], v[136:139], a[12:15], 0\n"
    "buffer_load_dwordx4 v200, s[24:27], s58 offen offset:2048 lds\n"
    "s_waitcnt lgkmcnt(9)\n"
    "v_mfma_f32_16x16x32_bf16 v[48:51], v[140:143], a[0:3], 0\n"
    "buffer_load_dwordx4 v201, s[24:27], s59 offen offset:3072 lds\n"
    "s_add_u32 s82, s82, 262144\n"
    "s_cmp_ge_u32 s82, 3670016\n"
    "s_cselect_b32 s82, 0, s82\n"
    "v_mfma_f32_16x16x32_bf16 v[52:55], v[140:143], a[4:7], 0\n"
    "s_waitcnt vmcnt(12)\n"
    "ds_read_b64_tr_b16 v[144:145], v192 offset:4096\n"
    "v_mfma_f32_16x16x32_bf16 v[56:59], v[140:143], a[8:11], 0\n"
    "ds_read_b64_tr_b16 v[146:147], v192 offset:4608\n"
    "v_mfma_f32_16x16x32_bf16 v[60:63], v[140:143], a[12:15], 0\n"
    "ds_read_b64_tr_b16 v[148:149], v193 offset:4096\n"
    "ds_read_b64_tr_b16 v[150:151], v193 offset:4608\n"
    "ds_read_b64_tr_b16 v[152:153], v194 offset:4096\n"
    "ds_read_b64_tr_b16 v[154:155], v194 offset:4608\n"
    "ds_read_b64_tr_b16 v[156:157], v195 offset:4096\n"
    "ds_read_b64_tr_b16 v[158:159], v195 offset:4608\n"
    "s_waitcnt lgkmcnt(6)\n"
    "v_mfma_f32_16x16x32_bf16 v[0:3], v[144:147], a[16:19], v[0:3]\n"
    "s_add_u32 s24, s83, s82\n"
    "s_addc_u32 s25, s84, 0\n"
    "s_add_u32 s38, s86, 4096\n"
    "s_mov_b32 m0, s38\n"
    "v_mfma_f32_16x16x32_bf16 v[4:7], v[144:147], a[20:23], v[4:7]\n"
    "buffer_load_dwordx4 v200, s[24:27], s56 offen lds\n"
    "v_mfma_f32_16x16x32_bf16 v[8:11], v[144:147], a[24:27], v[8:11]\n"
    "buffer_load_dwordx4 v201, s[24:27], s57 offen offset:1024 lds\n"
    "v_mfma_f32_16x16x32_bf16 v[12:15], v[144:147], a[28:31], v[12:15]\n"
    "buffer_load_dwordx4 v200, s[24:27], s58 offen offset:2048 lds\n"
    "s_waitcnt lgkmcnt(4)\n"
    "v_mfma_f32_16x16x32_bf16 v[16:19], v[148:151], a[16:19], v[16:19]\n"
    "buffer_load_dwordx4 v201, s[24:27], s59 offen offset:3072 lds\n"
    "s_add_u32 s82, s82, 262144\n"
    "s_cmp_ge_u32 s82, 3670016\n"
    "s_cselect_b32 s82, 0, s82\n"
    "ds_read_b128 a[52:55], v204 offset:10240\n"
    "v_mfma_f32_16x16x32_bf16 v[20:23], v[148:151], a[20:23], v[20:23]\n"
    "s_mov_b32 m0, s80\n"
    "s_nop 0\n"
    "buffer_load_dwordx4 v202, s[20:23], s40 offen lds\n"
    "ds_read_b128 a[56:59], v204 offset:12288\n"
    "v_mfma_f32_16x16x32_bf16 v[24:27], v[148:151], a[24:27], v[24:27]\n"
    "buffer_load_dwordx4 v202, s[20:23], s41 offen offset:1024 lds\n"
    "ds_read_b128 a[60:63], v204 offset:14336\n"
    "v_mfma_f32_16x16x32_bf16 v[28:31], v[148:151], a[28:31], v[28:31]\n"
    "buffer_load_dwordx4 v202, s[20:23], s42 offen offset:2048 lds\n"
    "ds_read_b128 a[64:67], v203 offset:16384\n"
    "s_waitcnt lgkmcnt(6)\n"
    "v_mfma_f32_16x16x32_bf16 v[32:35], v[152:155], a[16:19], v[32:35]\n"
    "buffer_load_dwordx4 v202, s[20:23], s43 offen offset:3072 lds\n"
    "ds_read_b128 a[68:71], v203 offset:18432\n"
    "v_mfma_f32_16x16x32_bf16 v[36:39], v[152:155], a[20:23], v[36:39]\n"
    "s_add_u32 s81, s80, 4096\n"
    "s_mov_b32 m0, s81\n"
    "s_nop 0\n"
    "buffer_load_dwordx4 v202, s[20:23], s44 offen lds\n"
    "v_mfma_f32_16x16x32_bf16 v[40:43], v[152:155], a[24:27], v[40:43]\n"
    "buffer_load_dwordx4 v202, s[20:23], s45 offen offset:1024 lds\n"
    "v_mfma_f32_16x16x32_bf16 v[44:47], v[152:155], a[28:31], v[44:47]\n"
    "buffer_load_dwordx4 v202, s[20:23], s46 offen offset:2048 lds\n"
    "s_waitcnt lgkmcnt(5)\n"
    "v_mfma_f32_16x16x32_bf16 v[48:51], v[156:159], a[16:19], v[48:51]\n"
    "buffer_load_dwordx4 v202, s[20:23], s47 offen offset:3072 lds\n"
    "v_mfma_f32_16x16x32_bf16 v[52:55], v[156:159], a[20:23], v[52:55]\n"
    "s_waitcnt vmcnt(20)\n"
    "ds_read_b64_tr_b16 v[128:129], v192 offset:8192\n"
    "v_mfma_f32_16x16x32_bf16 v[56:59], v[156:159], a[24:27], v[56:59]\n"
    "ds_read_b64_tr_b16 v[130:131], v192 offset:8704\n"
    "v_mfma_f32_16x16x32_bf16 v[60:63], v[156:159], a[28:31], v[60:63]\n"
    "ds_read_b64_tr_b16 v[132:133], v193 offset:8192\n"
    "ds_read_b64_tr_b16 v[134:135], v193 offset:8704\n"
    "ds_read_b64_tr_b16 v[136:137], v194 offset:8192\n"
    "ds_read_b64_tr_b16 v[138:139], v194 offset:8704\n"
    "ds_read_b64_tr_b16 v[140:141], v195 offset:8192\n"
    "ds_read_b64_tr_b16 v[142:143], v195 offset:8704\n"
    "s_waitcnt lgkmcnt(6)\n"
    "v_mfma_f32_16x16x32_bf16 v[0:3], v[128:131], a[32:35], v[0:3]\n"
    "s_add_u32 s24, s83, s82\n"
    "s_addc_u32 s25, s84, 0\n"
    "s_mov_b32 m0, s85\n"
    "v_mfma_f32_16x16x32_bf16 v[4:7], v[128:131], a[36:39], v[4:7]\n"
    "buffer_load_dwordx4 v200, s[24:27], s56 offen lds\n"
    "v_mfma_f32_16x16x32_bf16 v[8:11], v[128:131], a[40:43], v[8:11]\n"
    "buffer_load_dwordx4 v201, s[24:27], s57 offen offset:1024 lds\n"
    "s_lshl_b32 s90, s36, 14\n"
    "s_add_u32 s20, s8, s90\n"
    "s_addc_u32 s21, s9, 0\n"
    "v_mfma_f32_16x16x32_bf16 v[12:15], v[128:131], a[44:47], v[12:15]\n"
    "buffer_load_dwordx4 v200, s[24:27], s58 offen offset:2048 lds\n"
    "s_sub_u32 s20, s20, 2688\n"
    "s_subb_u32 s21, s21, 0\n"
    "s_waitcnt lgkmcnt(4)\n"
    "v_mfma_f32_16x16x32_bf16 v[16:19], v[132:135], a[32:35], v[16:19]\n"
    "buffer_load_dwordx4 v201, s[24:27], s59 offen offset:3072 lds\n"
    "s_add_u32 s82, s82, 262144\n"
    "s_cmp_ge_u32 s82, 3670016\n"
    "s_cselect_b32 s82, 0, s82\n"
    "s_add_u32 s24, s83, s82\n"
    "s_addc_u32 s25, s84, 0\n"
    "s_add_u32 s38, s85, 4096\n"
    "s_mov_b32 m0, s38\n"
    "ds_read_b128 a[72:75], v203 offset:20480\n"
    "v_mfma_f32_16x16x32_bf16 v[20:23], v[132:135], a[36:39], v[20:23]\n"
    "buffer_load_dwordx4 v200, s[24:27], s56 offen lds\n"
    "ds_read_b128 a[76:79], v203 offset:22528\n"
    "v_mfma_f32_16x16x32_bf16 v[24:27], v[132:135], a[40:43], v[24:27]\n"
    "buffer_load_dwordx4 v201, s[24:27], s57 offen offset:1024 lds\n"
    "ds_read_b128 a[80:83], v204 offset:16384\n"
    "v_mfma_f32_16x16x32_bf16 v[28:31], v[132:135], a[44:47], v[28:31]\n"
    "buffer_load_dwordx4 v200, s[24:27], s58 offen offset:2048 lds\n"
    "ds_read_b128 a[84:87], v204 offset:18432\n"
    "s_waitcnt lgkmcnt(6)\n"
    "v_mfma_f32_16x16x32_bf16 v[32:35], v[136:139], a[32:35], v[32:35]\n"
    "buffer_load_dwordx4 v201, s[24:27], s59 offen offset:3072 lds\n"
    "s_add_u32 s82, s82, 262144\n"
    "s_cmp_ge_u32 s82, 3670016\n"
    "s_cselect_b32 s82, 0, s82\n"
    "ds_read_b128 a[88:91], v204 offset:20480\n"
    "v_mfma_f32_16x16x32_bf16 v[36:39], v[136:139], a[36:39], v[36:39]\n"
    "s_waitcnt vmcnt(24)\n"
    "ds_read_b64_tr_b16 v[144:145], v192 offset:12288\n"
    "s_and_b32 s21, s21, 0xffff\n"
    "s_mov_b32 s22, 0x4a80\n"
    "v_mfma_f32_16x16x32_bf16 v[40:43], v[136:139], a[40:43], v[40:43]\n"
    "ds_read_b64_tr_b16 v[146:147], v192 offset:12800\n"
    "s_mov_b32 s40, 0xa80\n"
    "v_mfma_f32_16x16x32_bf16 v[44:47], v[136:139], a[44:47], v[44:47]\n"
    "ds_read_b64_tr_b16 v[148:149], v193 offset:12288\n"
    "s_waitcnt lgkmcnt(8)\n"
    "v_mfma_f32_16x16x32_bf16 v[48:51], v[140:143], a[32:35], v[48:51]\n"
    "ds_read_b64_tr_b16 v[150:151], v193 offset:12800\n"
    "v_mfma_f32_16x16x32_bf16 v[52:55], v[140:143], a[36:39], v[52:55]\n"
    "ds_read_b64_tr_b16 v[152:153], v194 offset:12288\n"
    "v_mfma_f32_16x16x32_bf16 v[56:59], v[140:143], a[40:43], v[56:59]\n"
    "ds_read_b64_tr_b16 v[154:155], v194 offset:12800\n"
    "v_mfma_f32_16x16x32_bf16 v[60:63], v[140:143], a[44:47], v[60:63]\n"
    "ds_read_b64_tr_b16 v[156:157], v195 offset:12288\n"
    "ds_read_b64_tr_b16 v[158:159], v195 offset:12800\n"
    "s_waitcnt lgkmcnt(6)\n"
    "v_mfma_f32_16x16x32_bf16 v[0:3], v[144:147], a[48:51], v[0:3]\n"
    "s_add_u32 s24, s83, s82\n"
    "s_addc_u32 s25, s84, 0\n"
    "s_add_u32 s38, s85, 8192\n"
    "s_mov_b32 m0, s38\n"
    "v_mfma_f32_16x16x32_bf16 v[4:7], v[144:147], a[52:55], v[4:7]\n"
    "buffer_load_dwordx4 v200, s[24:27], s56 offen lds\n"
    "v_mfma_f32_16x16x32_bf16 v[8:11], v[144:147], a[56:59], v[8:11]\n"
    "s_waitcnt vmcnt(21)\n"
    "ds_read_b64_tr_b16 v[128:129], v196\n"
    "s_mov_b32 s41, 0x700\n"
    "s_mov_b32 s42, 0x380\n"
    "v_mfma_f32_16x16x32_bf16 v[12:15], v[144:147], a[60:63], v[12:15]\n"
    "buffer_load_dwordx4 v201, s[24:27], s57 offen offset:1024 lds\n"
    "s_mov_b32 s43, 0\n"
    "s_mov_b32 s44, 0x1a80\n"
    "s_waitcnt lgkmcnt(5)\n"
    "v_mfma_f32_16x16x32_bf16 v[16:19], v[148:151], a[48:51], v[16:19]\n"
    "ds_read_b64_tr_b16 v[130:131], v196 offset:512\n"
    "s_mov_b32 s45, 0x1700\n"
    "v_mfma_f32_16x16x32_bf16 v[20:23], v[148:151], a[52:55], v[20:23]\n"
    "buffer_load_dwordx4 v200, s[24:27], s58 offen offset:2048 lds\n"
    "v_mfma_f32_16x16x32_bf16 v[24:27], v[148:151], a[56:59], v[24:27]\n"
    "ds_read_b64_tr_b16 v[132:133], v197\n"
    "v_mfma_f32_16x16x32_bf16 v[28:31], v[148:151], a[60:63], v[28:31]\n"
    "buffer_load_dwordx4 v201, s[24:27], s59 offen offset:3072 lds\n"
    "s_add_u32 s82, s82, 262144\n"
    "s_cmp_ge_u32 s82, 3670016\n"
    "s_cselect_b32 s82, 0, s82\n"
    "s_waitcnt lgkmcnt(5)\n"
    "v_mfma_f32_16x16x32_bf16 v[32:35], v[152:155], a[48:51], v[32:35]\n"
    "ds_read_b64_tr_b16 v[134:135], v197 offset:512\n"
    "v_mfma_f32_16x16x32_bf16 v[36:39], v[152:155], a[52:55], v[36:39]\n"
    "ds_read_b64_tr_b16 v[136:137], v198\n"
    "v_mfma_f32_16x16x32_bf16 v[40:43], v[152:155], a[56:59], v[40:43]\n"
    "ds_read_b64_tr_b16 v[138:139], v198 offset:512\n"
    "v_mfma_f32_16x16x32_bf16 v[44:47], v[152:155], a[60:63], v[44:47]\n"
    "ds_read_b64_tr_b16 v[140:141], v199\n"
    "s_waitcnt lgkmcnt(7)\n"
    "v_mfma_f32_16x16x32_bf16 v[48:51], v[156:159], a[48:51], v[48:51]\n"
    "ds_read_b64_tr_b16 v[142:143], v199 offset:512\n"
    "v_mfma_f32_16x16x32_bf16 v[52:55], v[156:159], a[52:55], v[52:55]\n"
    "ds_read_b128 a[92:95], v204 offset:22528\n"
    "v_mfma_f32_16x16x32_bf16 v[56:59], v[156:159], a[56:59], v[56:59]\n"
    "ds_read_b128 a[96:99], v203 offset:24576\n"
    "v_mfma_f32_16x16x32_bf16 v[60:63], v[156:159], a[60:63], v[60:63]\n"
    "ds_read_b128 a[100:103], v203 offset:26624\n"
    "ds_read_b128 a[104:107], v203 offset:28672\n"
    "ds_read_b128 a[108:111], v203 offset:30720\n"
    "s_waitcnt lgkmcnt(11)\n"
    "v_mfma_f32_16x16x32_bf16 v[0:3], v[128:131], a[64:67], v[0:3]\n"
    "s_add_u32 s24, s83, s82\n"
    "s_addc_u32 s25, s84, 0\n"
    "s_add_u32 s38, s85, 12288\n"
    "s_mov_b32 m0, s38\n"
    "v_mfma_f32_16x16x32_bf16 v[4:7], v[128:131], a[68:71], v[4:7]\n"
    "buffer_load_dwordx4 v200, s[24:27], s56 offen lds\n"
    "v_mfma_f32_16x16x32_bf16 v[8:11], v[128:131], a[72:75], v[8:11]\n"
    "s_waitcnt vmcnt(21)\n"
    "ds_read_b64_tr_b16 v[144:145], v196 offset:4096\n"
    "s_mov_b32 s46, 0x1380\n"
    "s_mov_b32 s47, 0x1000\n"
    "v_mfma_f32_16x16x32_bf16 v[12:15], v[128:131], a[76:79], v[12:15]\n"
    "buffer_load_dwordx4 v201, s[24:27], s57 offen offset:1024 lds\n"
    "s_mov_b32 s48, 0x2a80\n"
    "s_mov_b32 s49, 0x2700\n"
    "s_waitcnt lgkmcnt(10)\n"
    "v_mfma_f32_16x16x32_bf16 v[16:19], v[132:135], a[64:67], v[16:19]\n"
    "ds_read_b64_tr_b16 v[146:147], v196 offset:4608\n"
    "s_mov_b32 s50, 0x2380\n"
    "v_mfma_f32_16x16x32_bf16 v[20:23], v[132:135], a[68:71], v[20:23]\n"
    "buffer_load_dwordx4 v200, s[24:27], s58 offen offset:2048 lds\n"
    "v_mfma_f32_16x16x32_bf16 v[24:27], v[132:135], a[72:75], v[24:27]\n"
    "ds_read_b64_tr_b16 v[148:149], v197 offset:4096\n"
    "v_mfma_f32_16x16x32_bf16 v[28:31], v[132:135], a[76:79], v[28:31]\n"
    "buffer_load_dwordx4 v201, s[24:27], s59 offen offset:3072 lds\n"
    "s_add_u32 s82, s82, 262144\n"
    "s_cmp_ge_u32 s82, 3670016\n"
    "s_cselect_b32 s82, 0, s82\n"
    "s_waitcnt lgkmcnt(10)\n"
    "v_mfma_f32_16x16x32_bf16 v[32:35], v[136:139], a[64:67], v[32:35]\n"
    "ds_read_b64_tr_b16 v[150:151], v197 offset:4608\n"
    "v_mfma_f32_16x16x32_bf16 v[36:39], v[136:139], a[68:71], v[36:39]\n"
    "ds_read_b64_tr_b16 v[152:153], v198 offset:4096\n"
    "v_mfma_f32_16x16x32_bf16 v[40:43], v[136:139], a[72:75], v[40:43]\n"
    "ds_read_b64_tr_b16 v[154:155], v198 offset:4608\n"
    "v_mfma_f32_16x16x32_bf16 v[44:47], v[136:139], a[76:79], v[44:47]\n"
    "ds_read_b64_tr_b16 v[156:157], v199 offset:4096\n"
    "s_waitcnt lgkmcnt(12)\n"
    "v_mfma_f32_16x16x32_bf16 v[48:51], v[140:143], a[64:67], v[48:51]\n"
    "ds_read_b64_tr_b16 v[158:159], v199 offset:4608\n"
    "v_mfma_f32_16x16x32_bf16 v[52:55], v[140:143], a[68:71], v[52:55]\n"
    "ds_read_b128 a[112:115], v204 offset:24576\n"
    "v_mfma_f32_16x16x32_bf16 v[56:59], v[140:143], a[72:75], v[56:59]\n"
    "ds_read_b128 a[116:119], v204 offset:26624\n"
    "v_mfma_f32_16x16x32_bf16 v[60:63], v[140:143], a[76:79], v[60:63]\n"
    "ds_read_b128 a[120:123], v204 offset:28672\n"
    "ds_read_b128 a[124:127], v204 offset:30720\n"
    "s_waitcnt lgkmcnt(10)\n"
    "v_mfma_f32_16x16x32_bf16 v[0:3], v[144:147], a[80:83], v[0:3]\n"
    "s_add_u32 s24, s83, s82\n"
    "s_addc_u32 s25, s84, 0\n"
    "s_mov_b32 m0, s86\n"
    "s_waitcnt vmcnt(16)\n"
    "s_barrier\n"
    "v_mfma_f32_16x16x32_bf16 v[4:7], v[144:147], a[84:87], v[4:7]\n"
    "buffer_load_dwordx4 v200, s[24:27], s56 offen lds\n"
    "v_mfma_f32_16x16x32_bf16 v[8:11], v[144:147], a[88:91], v[8:11]\n"
    "buffer_load_dwordx4 v201, s[24:27], s57 offen offset:1024 lds\n"
    "s_mov_b32 s51, 0x2000\n"
    "s_mov_b32 s52, 0x3a80\n"
    "v_mfma_f32_16x16x32_bf16 v[12:15], v[144:147], a[92:95], v[12:15]\n"
    "buffer_load_dwordx4 v200, s[24:27], s58 offen offset:2048 lds\n"
    "s_mov_b32 s53, 0x3700\n"
    "s_mov_b32 s54, 0x3380\n"
    "s_waitcnt lgkmcnt(8)\n"
    "v_mfma_f32_16x16x32_bf16 v[16:19], v[148:151], a[80:83], v[16:19]\n"
    "buffer_load_dwordx4 v201, s[24:27], s59 offen offset:3072 lds\n"
    "s_add_u32 s82, s82, 262144\n"
    "s_cmp_ge_u32 s82, 3670016\n"
    "s_cselect_b32 s82, 0, s82\n"
    "s_add_u32 s24, s83, s82\n"
    "s_addc_u32 s25, s84, 0\n"
    "s_add_u32 s38, s86, 4096\n"
    "s_mov_b32 m0, s38\n"
    "ds_read_b128 a[128:131], v203 offset:32768\n"
    "v_mfma_f32_16x16x32_bf16 v[20:23], v[148:151], a[84:87], v[20:23]\n"
    "s_waitcnt lgkmcnt(5)\n"
    "buffer_load_dwordx4 v200, s[24:27], s56 offen lds\n"
    "ds_read_b128 a[132:135], v203 offset:34816\n"
    "v_mfma_f32_16x16x32_bf16 v[24:27], v[148:151], a[88:91], v[24:27]\n"
    "buffer_load_dwordx4 v201, s[24:27], s57 offen offset:1024 lds\n"
    "ds_read_b128 a[136:139], v203 offset:36864\n"
    "v_mfma_f32_16x16x32_bf16 v[28:31], v[148:151], a[92:95], v[28:31]\n"
    "buffer_load_dwordx4 v200, s[24:27], s58 offen offset:2048 lds\n"
    "ds_read_b128 a[140:143], v203 offset:38912\n"
    "v_mfma_f32_16x16x32_bf16 v[32:35], v[152:155], a[80:83], v[32:35]\n"
    "buffer_load_dwordx4 v201, s[24:27], s59 offen offset:3072 lds\n"
    "s_add_u32 s82, s82, 262144\n"
    "s_cmp_ge_u32 s82, 3670016\n"
    "s_cselect_b32 s82, 0, s82\n"
    "ds_read_b128 a[144:147], v204 offset:32768\n"
    "v_mfma_f32_16x16x32_bf16 v[36:39], v[152:155], a[84:87], v[36:39]\n"
    "s_waitcnt vmcnt(20)\n"
    "ds_read_b64_tr_b16 v[128:129], v192\n"
    "s_mov_b32 s55, 0x3000\n"
    "v_mfma_f32_16x16x32_bf16 v[40:43], v[152:155], a[88:91], v[40:43]\n"
    "ds_read_b64_tr_b16 v[130:131], v192 offset:512\n"
    "v_mfma_f32_16x16x32_bf16 v[44:47], v[152:155], a[92:95], v[44:47]\n"
    "ds_read_b64_tr_b16 v[132:133], v193\n"
    "v_mfma_f32_16x16x32_bf16 v[48:51], v[156:159], a[80:83], v[48:51]\n"
    "ds_read_b64_tr_b16 v[134:135], v193 offset:512\n"
    "v_mfma_f32_16x16x32_bf16 v[52:55], v[156:159], a[84:87], v[52:55]\n"
    "ds_read_b64_tr_b16 v[136:137], v194\n"
    "v_mfma_f32_16x16x32_bf16 v[56:59], v[156:159], a[88:91], v[56:59]\n"
    "ds_read_b64_tr_b16 v[138:139], v194 offset:512\n"
    "v_mfma_f32_16x16x32_bf16 v[60:63], v[156:159], a[92:95], v[60:63]\n"
    "ds_read_b64_tr_b16 v[140:141], v195\n"
    "ds_read_b64_tr_b16 v[142:143], v195 offset:512\n"
    "s_waitcnt lgkmcnt(6)\n"
    "v_mfma_f32_16x16x32_bf16 v[0:3], v[128:131], a[96:99], v[0:3]\n"
    "s_add_u32 s24, s83, s82\n"
    "s_addc_u32 s25, s84, 0\n"
    "s_mov_b32 m0, s85\n"
    "v_mfma_f32_16x16x32_bf16 v[4:7], v[128:131], a[100:103], v[4:7]\n"
    "s_waitcnt lgkmcnt(0)\n"
    "buffer_load_dwordx4 v200, s[24:27], s56 offen lds\n"
    "v_mfma_f32_16x16x32_bf16 v[8:11], v[128:131], a[104:107], v[8:11]\n"
    "s_waitcnt vmcnt(17)\n"
    "ds_read_b64_tr_b16 v[144:145], v192 offset:4096\n"
    "s_add_u32 s72, s90, 0\n"
    "v_mfma_f32_16x16x32_bf16 v[12:15], v[128:131], a[108:111], v[12:15]\n"
    "buffer_load_dwordx4 v201, s[24:27], s57 offen offset:1024 lds\n"
    "s_sub_u32 s39, 4, s36\n"
    "s_and_b32 s39, s39, 3\n"
    "s_lshl_b32 s39, s39, 12\n"
    "s_add_u32 s39, s39, s90\n"
    "s_add_u32 s76, s39, 65536\n"
    "v_mfma_f32_16x16x32_bf16 v[16:19], v[132:135], a[96:99], v[16:19]\n"
    "ds_read_b64_tr_b16 v[146:147], v192 offset:4608\n"
    "s_add_u32 s73, s90, 4096\n"
    "v_mfma_f32_16x16x32_bf16 v[20:23], v[132:135], a[100:103], v[20:23]\n"
    "buffer_load_dwordx4 v200, s[24:27], s58 offen offset:2048 lds\n"
    "s_sub_u32 s39, 5, s36\n"
    "s_and_b32 s39, s39, 3\n"
    "s_lshl_b32 s39, s39, 12\n"
    "s_add_u32 s39, s39, s90\n"
    "s_add_u32 s77, s39, 65536\n"
    "v_mfma_f32_16x16x32_bf16 v[24:27], v[132:135], a[104:107], v[24:27]\n"
    "ds_read_b64_tr_b16 v[148:149], v193 offset:4096\n"
    "s_add_u32 s74, s90, 8192\n"
    "v_mfma_f32_16x16x32_bf16 v[28:31], v[132:135], a[108:111], v[28:31]\n"
    "buffer_load_dwordx4 v201, s[24:27], s59 offen offset:3072 lds\n"
    "s_add_u32 s82, s82, 262144\n"
    "s_cmp_ge_u32 s82, 3670016\n"
    "s_cselect_b32 s82, 0, s82\n"
    "v_mfma_f32_16x16x32_bf16 v[32:35], v[136:139], a[96:99], v[32:35]\n"
    "ds_read_b64_tr_b16 v[150:151], v193 offset:4608\n"
    "v_mfma_f32_16x16x32_bf16 v[36:39], v[136:139], a[100:103], v[36:39]\n"
    "ds_read_b64_tr_b16 v[152:153], v194 offset:4096\n"
    "v_mfma_f32_16x16x32_bf16 v[40:43], v[136:139], a[104:107], v[40:43]\n"
    "ds_read_b64_tr_b16 v[154:155], v194 offset:4608\n"
    "v_mfma_f32_16x16x32_bf16 v[44:47], v[136:139], a[108:111], v[44:47]\n"
    "ds_read_b64_tr_b16 v[156:157], v195 offset:4096\n"
    "v_mfma_f32_16x16x32_bf16 v[48:51], v[140:143], a[96:99], v[48:51]\n"
    "ds_read_b64_tr_b16 v[158:159], v195 offset:4608\n"
    "v_mfma_f32_16x16x32_bf16 v[52:55], v[140:143], a[100:103], v[52:55]\n"
    "ds_read_b128 a[148:151], v204 offset:34816\n"
    "v_mfma_f32_16x16x32_bf16 v[56:59], v[140:143], a[104:107], v[56:59]\n"
    "ds_read_b128 a[152:155], v204 offset:36864\n"
    "v_mfma_f32_16x16x32_bf16 v[60:63], v[140:143], a[108:111], v[60:63]\n"
    "ds_read_b128 a[156:159], v204 offset:38912\n"
    "ds_read_b128 a[160:163], v203 offset:40960\n"
    "ds_read_b128 a[164:167], v203 offset:43008\n"
    "s_waitcnt lgkmcnt(11)\n"
    "v_mfma_f32_16x16x32_bf16 v[0:3], v[144:147], a[112:115], v[0:3]\n"
    "s_add_u32 s24, s83, s82\n"
    "s_addc_u32 s25, s84, 0\n"
    "s_add_u32 s38, s85, 4096\n"
    "s_mov_b32 m0, s38\n"
    "v_mfma_f32_16x16x32_bf16 v[4:7], v[144:147], a[116:119], v[4:7]\n"
    "s_waitcnt lgkmcnt(5)\n"
    "buffer_load_dwordx4 v200, s[24:27], s56 offen lds\n"
    "v_mfma_f32_16x16x32_bf16 v[8:11], v[144:147], a[120:123], v[8:11]\n"
    "s_waitcnt vmcnt(17)\n"
    "ds_read_b64_tr_b16 v[128:129], v192 offset:8192\n"
    "v_mfma_f32_16x16x32_bf16 v[12:15], v[144:147], a[124:127], v[12:15]\n"
    "buffer_load_dwordx4 v201, s[24:27], s57 offen offset:1024 lds\n"
    "s_sub_u32 s39, 6, s36\n"
    "s_and_b32 s39, s39, 3\n"
    "s_lshl_b32 s39, s39, 12\n"
    "s_add_u32 s39, s39, s90\n"
    "s_add_u32 s78, s39, 65536\n"
    "v_mfma_f32_16x16x32_bf16 v[16:19], v[148:151], a[112:115], v[16:19]\n"
    "ds_read_b64_tr_b16 v[130:131], v192 offset:8704\n"
    "s_add_u32 s75, s90, 12288\n"
    "v_mfma_f32_16x16x32_bf16 v[20:23], v[148:151], a[116:119], v[20:23]\n"
    "buffer_load_dwordx4 v200, s[24:27], s58 offen offset:2048 lds\n"
    "s_sub_u32 s39, 7, s36\n"
    "s_and_b32 s39, s39, 3\n"
    "s_lshl_b32 s39, s39, 12\n"
    "s_add_u32 s39, s39, s90\n"
    "s_add_u32 s79, s39, 65536\n"
    "v_mfma_f32_16x16x32_bf16 v[24:27], v[148:151], a[120:123], v[24:27]\n"
    "ds_read_b64_tr_b16 v[132:133], v193 offset:8192\n"
    "v_mfma_f32_16x16x32_bf16 v[28:31], v[148:151], a[124:127], v[28:31]\n"
    "buffer_load_dwordx4 v201, s[24:27], s59 offen offset:3072 lds\n"
    "s_add_u32 s82, s82, 262144\n"
    "s_cmp_ge_u32 s82, 3670016\n"
    "s_cselect_b32 s82, 0, s82\n"
    "v_mfma_f32_16x16x32_bf16 v[32:35], v[152:155], a[112:115], v[32:35]\n"
    "ds_read_b64_tr_b16 v[134:135], v193 offset:8704\n"
    "v_mfma_f32_16x16x32_bf16 v[36:39], v[152:155], a[116:119], v[36:39]\n"
    "ds_read_b64_tr_b16 v[136:137], v194 offset:8192\n"
    "v_mfma_f32_16x16x32_bf16 v[40:43], v[152:155], a[120:123], v[40:43]\n"
    "ds_read_b64_tr_b16 v[138:139], v194 offset:8704\n"
    "v_mfma_f32_16x16x32_bf16 v[44:47], v[152:155], a[124:127], v[44:47]\n"
    "ds_read_b64_tr_b16 v[140:141], v195 offset:8192\n"
    "v_mfma_f32_16x16x32_bf16 v[48:51], v[156:159], a[112:115], v[48:51]\n"
    "ds_read_b64_tr_b16 v[142:143], v195 offset:8704\n"
    "v_mfma_f32_16x16x32_bf16 v[52:55], v[156:159], a[116:119], v[52:55]\n"
    "ds_read_b128 a[168:171], v203 offset:45056\n"
    "v_mfma_f32_16x16x32_bf16 v[56:59], v[156:159], a[120:123], v[56:59]\n"
    "ds_read_b128 a[172:175], v203 offset:47104\n"
    "v_mfma_f32_16x16x32_bf16 v[60:63], v[156:159], a[124:127], v[60:63]\n"
    "ds_read_b128 a[176:179], v204 offset:40960\n"
    "ds_read_b128 a[180:183], v204 offset:43008\n"
    "ds_read_b128 a[184:187], v204 offset:45056\n"
    "s_waitcnt lgkmcnt(11)\n"
    "v_mfma_f32_16x16x32_bf16 v[0:3], v[128:131], a[128:131], v[0:3]\n"
    "s_add_u32 s24, s83, s82\n"
    "s_addc_u32 s25, s84, 0\n"
    "s_add_u32 s38, s85, 8192\n"
    "s_mov_b32 m0, s38\n"
    "v_mfma_f32_16x16x32_bf16 v[4:7], v[128:131], a[132:135], v[4:7]\n"
    "s_waitcnt lgkmcnt(5)\n"
    "buffer_load_dwordx4 v200, s[24:27], s60 offen lds\n"
    "v_mfma_f32_16x16x32_bf16 v[8:11], v[128:131], a[136:139], v[8:11]\n"
    "s_waitcnt vmcnt(17)\n"
    "ds_read_b64_tr_b16 v[144:145], v192 offset:12288\n"
    "v_mfma_f32_16x16x32_bf16 v[12:15], v[128:131], a[140:143], v[12:15]\n"
    "buffer_load_dwordx4 v201, s[24:27], s61 offen offset:1024 lds\n"
    "v_mfma_f32_16x16x32_bf16 v[16:19], v[132:135], a[128:131], v[16:19]\n"
    "ds_read_b64_tr_b16 v[146:147], v192 offset:12800\n"
    "v_mfma_f32_16x16x32_bf16 v[20:23], v[132:135], a[132:135], v[20:23]\n"
    "buffer_load_dwordx4 v200, s[24:27], s62 offen offset:2048 lds\n"
    "v_mfma_f32_16x16x32_bf16 v[24:27], v[132:135], a[136:139], v[24:27]\n"
    "ds_read_b64_tr_b16 v[148:149], v193 offset:12288\n"
    "v_mfma_f32_16x16x32_bf16 v[28:31], v[132:135], a[140:143], v[28:31]\n"
    "buffer_load_dwordx4 v201, s[24:27], s63 offen offset:3072 lds\n"
    "s_add_u32 s82, s82, 262144\n"
    "s_cmp_ge_u32 s82, 3670016\n"
    "s_cselect_b32 s82, 0, s82\n"
    "v_mfma_f32_16x16x32_bf16 v[32:35], v[136:139], a[128:131], v[32:35]\n"
    "ds_read_b64_tr_b16 v[150:151], v193 offset:12800\n"
    "v_mfma_f32_16x16x32_bf16 v[36:39], v[136:139], a[132:135], v[36:39]\n"
    "ds_read_b64_tr_b16 v[152:153], v194 offset:12288\n"
    "v_mfma_f32_16x16x32_bf16 v[40:43], v[136:139], a[136:139], v[40:43]\n"
    "ds_read_b64_tr_b16 v[154:155], v194 offset:12800\n"
    "v_mfma_f32_16x16x32_bf16 v[44:47], v[136:139], a[140:143], v[44:47]\n"
    "ds_read_b64_tr_b16 v[156:157], v195 offset:12288\n"
    "v_mfma_f32_16x16x32_bf16 v[48:51], v[140:143], a[128:131], v[48:51]\n"
    "ds_read_b64_tr_b16 v[158:159], v195 offset:12800\n"
    "v_mfma_f32_16x16x32_bf16 v[52:55], v[140:143], a[132:135], v[52:55]\n"
    "ds_read_b128 a[188:191], v204 offset:47104\n"
    "v_mfma_f32_16x16x32_bf16 v[56:59], v[140:143], a[136:139], v[56:59]\n"
    "ds_read_b128 a[192:195], v203 offset:49152\n"
    "v_mfma_f32_16x16x32_bf16 v[60:63], v[140:143], a[140:143], v[60:63]\n"
    "ds_read_b128 a[196:199], v203 offset:51200\n"
    "ds_read_b128 a[200:203], v203 offset:53248\n"
    "ds_read_b128 a[204:207], v203 offset:55296\n"
    "s_waitcnt lgkmcnt(11)\n"
    "v_mfma_f32_16x16x32_bf16 v[0:3], v[144:147], a[144:147], v[0:3]\n"
    "s_add_u32 s24, s83, s82\n"
    "s_addc_u32 s25, s84, 0\n"
    "s_add_u32 s38, s85, 12288\n"
    "s_mov_b32 m0, s38\n"
    "v_mfma_f32_16x16x32_bf16 v[4:7], v[144:147], a[148:151], v[4:7]\n"
    "s_waitcnt lgkmcnt(5)\n"
    "buffer_load_dwordx4 v200, s[24:27], s60 offen lds\n"
    "v_mfma_f32_16x16x32_bf16 v[8:11], v[144:147], a[152:155], v[8:11]\n"
    "s_waitcnt vmcnt(17)\n"
    "ds_read_b64_tr_b16 v[128:129], v196\n"
    "v_mfma_f32_16x16x32_bf16 v[12:15], v[144:147], a[156:159], v[12:15]\n"
    "buffer_load_dwordx4 v201, s[24:27], s61 offen offset:1024 lds\n"
    "v_mfma_f32_16x16x32_bf16 v[16:19], v[148:151], a[144:147], v[16:19]\n"
    "ds_read_b64_tr_b16 v[130:131], v196 offset:512\n"
    "v_mfma_f32_16x16x32_bf16 v[20:23], v[148:151], a[148:151], v[20:23]\n"
    "buffer_load_dwordx4 v200, s[24:27], s62 offen offset:2048 lds\n"
    "v_mfma_f32_16x16x32_bf16 v[24:27], v[148:151], a[152:155], v[24:27]\n"
    "ds_read_b64_tr_b16 v[132:133], v197\n"
    "v_mfma_f32_16x16x32_bf16 v[28:31], v[148:151], a[156:159], v[28:31]\n"
    "buffer_load_dwordx4 v201, s[24:27], s63 offen offset:3072 lds\n"
    "s_add_u32 s82, s82, 262144\n"
    "s_cmp_ge_u32 s82, 3670016\n"
    "s_cselect_b32 s82, 0, s82\n"
    "v_mfma_f32_16x16x32_bf16 v[32:35], v[152:155], a[144:147], v[32:35]\n"
    "ds_read_b64_tr_b16 v[134:135], v197 offset:512\n"
    "v_mfma_f32_16x16x32_bf16 v[36:39], v[152:155], a[148:151], v[36:39]\n"
    "ds_read_b64_tr_b16 v[136:137], v198\n"
    "v_mfma_f32_16x16x32_bf16 v[40:43], v[152:155], a[152:155], v[40:43]\n"
    "ds_read_b64_tr_b16 v[138:139], v198 offset:512\n"
    "v_mfma_f32_16x16x32_bf16 v[44:47], v[152:155], a[156:159], v[44:47]\n"
    "ds_read_b64_tr_b16 v[140:141], v199\n"
    "v_mfma_f32_16x16x32_bf16 v[48:51], v[156:159], a[144:147], v[48:51]\n"
    "ds_read_b64_tr_b16 v[142:143], v199 offset:512\n"
    "v_mfma_f32_16x16x32_bf16 v[52:55], v[156:159], a[148:151], v[52:55]\n"
    "ds_read_b128 a[208:211], v204 offset:49152\n"
    "v_mfma_f32_16x16x32_bf16 v[56:59], v[156:159], a[152:155], v[56:59]\n"
    "ds_read_b128 a[212:215], v204 offset:51200\n"
    "v_mfma_f32_16x16x32_bf16 v[60:63], v[156:159], a[156:159], v[60:63]\n"
    "ds_read_b128 a[216:219], v204 offset:53248\n"
    "ds_read_b128 a[220:223], v204 offset:55296\n"
    "s_waitcnt lgkmcnt(10)\n"
    "v_mfma_f32_16x16x32_bf16 v[0:3], v[128:131], a[160:163], v[0:3]\n"
    "s_add_u32 s24, s83, s82\n"
    "s_addc_u32 s25, s84, 0\n"
    "s_mov_b32 m0, s86\n"
    "v_mfma_f32_16x16x32_bf16 v[4:7], v[128:131], a[164:167], v[4:7]\n"
    "s_waitcnt lgkmcnt(4)\n"
    "buffer_load_dwordx4 v200, s[24:27], s60 offen lds\n"
    "v_mfma_f32_16x16x32_bf16 v[8:11], v[128:131], a[168:171], v[8:11]\n"
    "s_waitcnt vmcnt(17)\n"
    "ds_read_b64_tr_b16 v[144:145], v196 offset:4096\n"
    "v_mfma_f32_16x16x32_bf16 v[12:15], v[128:131], a[172:175], v[12:15]\n"
    "buffer_load_dwordx4 v201, s[24:27], s61 offen offset:1024 lds\n"
    "v_mfma_f32_16x16x32_bf16 v[16:19], v[132:135], a[160:163], v[16:19]\n"
    "ds_read_b64_tr_b16 v[146:147], v196 offset:4608\n"
    "v_mfma_f32_16x16x32_bf16 v[20:23], v[132:135], a[164:167], v[20:23]\n"
    "buffer_load_dwordx4 v200, s[24:27], s62 offen offset:2048 lds\n"
    "v_mfma_f32_16x16x32_bf16 v[24:27], v[132:135], a[168:171], v[24:27]\n"
    "ds_read_b64_tr_b16 v[148:149], v197 offset:4096\n"
    "v_mfma_f32_16x16x32_bf16 v[28:31], v[132:135], a[172:175], v[28:31]\n"
    "buffer_load_dwordx4 v201, s[24:27], s63 offen offset:3072 lds\n"
    "s_add_u32 s82, s82, 262144\n"
    "s_cmp_ge_u32 s82, 3670016\n"
    "s_cselect_b32 s82, 0, s82\n"
    "v_mfma_f32_16x16x32_bf16 v[32:35], v[136:139], a[160:163], v[32:35]\n"
    "ds_read_b64_tr_b16 v[150:151], v197 offset:4608\n"
    "v_mfma_f32_16x16x32_bf16 v[36:39], v[136:139], a[164:167], v[36:39]\n"
    "ds_read_b64_tr_b16 v[152:153], v198 offset:4096\n"
    "v_mfma_f32_16x16x32_bf16 v[40:43], v[136:139], a[168:171], v[40:43]\n"
    "ds_read_b64_tr_b16 v[154:155], v198 offset:4608\n"
    "v_mfma_f32_16x16x32_bf16 v[44:47], v[136:139], a[172:175], v[44:47]\n"
    "ds_read_b64_tr_b16 v[156:157], v199 offset:4096\n"
    "v_mfma_f32_16x16x32_bf16 v[48:51], v[140:143], a[160:163], v[48:51]\n"
    "ds_read_b64_tr_b16 v[158:159], v199 offset:4608\n"
    "v_mfma_f32_16x16x32_bf16 v[52:55], v[140:143], a[164:167], v[52:55]\n"
    "v_mfma_f32_16x16x32_bf16 v[56:59], v[140:143], a[168:171], v[56:59]\n"
    "v_mfma_f32_16x16x32_bf16 v[60:63], v[140:143], a[172:175], v[60:63]\n"
    "s_waitcnt lgkmcnt(6)\n"
    "v_mfma_f32_16x16x32_bf16 v[0:3], v[144:147], a[176:179], v[0:3]\n"
    "s_add_u32 s24, s83, s82\n"
    "s_addc_u32 s25, s84, 0\n"
    "s_add_u32 s38, s86, 4096\n"
    "s_mov_b32 m0, s38\n"
    "v_mfma_f32_16x16x32_bf16 v[4:7], v[144:147], a[180:183], v[4:7]\n"
    "s_waitcnt lgkmcnt(0)\n"
    "buffer_load_dwordx4 v200, s[24:27], s60 offen lds\n"
    "v_mfma_f32_16x16x32_bf16 v[8:11], v[144:147], a[184:187], v[8:11]\n"
    "s_waitcnt vmcnt(17)\n"
    "ds_read_b64_tr_b16 v[128:129], v192\n"
    "v_mfma_f32_16x16x32_bf16 v[12:15], v[144:147], a[188:191], v[12:15]\n"
    "buffer_load_dwordx4 v201, s[24:27], s61 offen offset:1024 lds\n"
    "v_mfma_f32_16x16x32_bf16 v[16:19], v[148:151], a[176:179], v[16:19]\n"
    "ds_read_b64_tr_b16 v[130:131], v192 offset:512\n"
    "v_mfma_f32_16x16x32_bf16 v[20:23], v[148:151], a[180:183], v[20:23]\n"
    "buffer_load_dwordx4 v200, s[24:27], s62 offen offset:2048 lds\n"
    "v_mfma_f32_16x16x32_bf16 v[24:27], v[148:151], a[184:187], v[24:27]\n"
    "ds_read_b64_tr_b16 v[132:133], v193\n"
    "v_mfma_f32_16x16x32_bf16 v[28:31], v[148:151], a[188:191], v[28:31]\n"
    "buffer_load_dwordx4 v201, s[24:27], s63 offen offset:3072 lds\n"
    "s_add_u32 s82, s82, 262144\n"
    "s_cmp_ge_u32 s82, 3670016\n"
    "s_cselect_b32 s82, 0, s82\n"
    "v_mfma_f32_16x16x32_bf16 v[32:35], v[152:155], a[176:179], v[32:35]\n"
    "ds_read_b64_tr_b16 v[134:135], v193 offset:512\n"
    "v_mfma_f32_16x16x32_bf16 v[36:39], v[152:155], a[180:183], v[36:39]\n"
    "ds_read_b64_tr_b16 v[136:137], v194\n"
    "v_mfma_f32_16x16x32_bf16 v[40:43], v[152:155], a[184:187], v[40:43]\n"
    "ds_read_b64_tr_b16 v[138:139], v194 offset:512\n"
    "v_mfma_f32_16x16x32_bf16 v[44:47], v[152:155], a[188:191], v[44:47]\n"
    "ds_read_b64_tr_b16 v[140:141], v195\n"
    "v_mfma_f32_16x16x32_bf16 v[48:51], v[156:159], a[176:179], v[48:51]\n"
    "ds_read_b64_tr_b16 v[142:143], v195 offset:512\n"
    "v_mfma_f32_16x16x32_bf16 v[52:55], v[156:159], a[180:183], v[52:55]\n"
    "v_mfma_f32_16x16x32_bf16 v[56:59], v[156:159], a[184:187], v[56:59]\n"
    "v_mfma_f32_16x16x32_bf16 v[60:63], v[156:159], a[188:191], v[60:63]\n"
    "s_waitcnt lgkmcnt(6)\n"
    "v_mfma_f32_16x16x32_bf16 v[0:3], v[128:131], a[192:195], v[0:3]\n"
    "s_add_u32 s24, s83, s82\n"
    "s_addc_u32 s25, s84, 0\n"
    "s_mov_b32 m0, s85\n"
    "v_mfma_f32_16x16x32_bf16 v[4:7], v[128:131], a[196:199], v[4:7]\n"
    "s_waitcnt lgkmcnt(0)\n"
    "buffer_load_dwordx4 v200, s[24:27], s60 offen lds\n"
    "v_mfma_f32_16x16x32_bf16 v[8:11], v[128:131], a[200:203], v[8:11]\n"
    "s_waitcnt vmcnt(17)\n"
    "ds_read_b64_tr_b16 v[144:145], v192 offset:4096\n"
    "v_mfma_f32_16x16x32_bf16 v[12:15], v[128:131], a[204:207], v[12:15]\n"
    "buffer_load_dwordx4 v201, s[24:27], s61 offen offset:1024 lds\n"
    "v_mfma_f32_16x16x32_bf16 v[16:19], v[132:135], a[192:195], v[16:19]\n"
    "ds_read_b64_tr_b16 v[146:147], v192 offset:4608\n"
    "v_mfma_f32_16x16x32_bf16 v[20:23], v[132:135], a[196:199], v[20:23]\n"
    "buffer_load_dwordx4 v200, s[24:27], s62 offen offset:2048 lds\n"
    "v_mfma_f32_16x16x32_bf16 v[24:27], v[132:135], a[200:203], v[24:27]\n"
    "ds_read_b64_tr_b16 v[148:149], v193 offset:4096\n"
    "v_mfma_f32_16x16x32_bf16 v[28:31], v[132:135], a[204:207], v[28:31]\n"
    "buffer_load_dwordx4 v201, s[24:27], s63 offen offset:3072 lds\n"
    "s_add_u32 s82, s82, 262144\n"
    "s_cmp_ge_u32 s82, 3670016\n"
    "s_cselect_b32 s82, 0, s82\n"
    "v_mfma_f32_16x16x32_bf16 v[32:35], v[136:139], a[192:195], v[32:35]\n"
    "ds_read_b64_tr_b16 v[150:151], v193 offset:4608\n"
    "v_mfma_f32_16x16x32_bf16 v[36:39], v[136:139], a[196:199], v[36:39]\n"
    "ds_read_b64_tr_b16 v[152:153], v194 offset:4096\n"
    "v_mfma_f32_16x16x32_bf16 v[40:43], v[136:139], a[200:203], v[40:43]\n"
    "ds_read_b64_tr_b16 v[154:155], v194 offset:4608\n"
    "v_mfma_f32_16x16x32_bf16 v[44:47], v[136:139], a[204:207], v[44:47]\n"
    "ds_read_b64_tr_b16 v[156:157], v195 offset:4096\n"
    "v_mfma_f32_16x16x32_bf16 v[48:51], v[140:143], a[192:195], v[48:51]\n"
    "ds_read_b64_tr_b16 v[158:159], v195 offset:4608\n"
    "v_mfma_f32_16x16x32_bf16 v[52:55], v[140:143], a[196:199], v[52:55]\n"
    "v_mfma_f32_16x16x32_bf16 v[56:59], v[140:143], a[200:203], v[56:59]\n"
    "v_mfma_f32_16x16x32_bf16 v[60:63], v[140:143], a[204:207], v[60:63]\n"
    "s_waitcnt lgkmcnt(6)\n"
    "v_mfma_f32_16x16x32_bf16 v[0:3], v[144:147], a[208:211], v[0:3]\n"
    "s_add_u32 s24, s83, s82\n"
    "s_addc_u32 s25, s84, 0\n"
    "s_add_u32 s38, s85, 4096\n"
    "s_mov_b32 m0, s38\n"
    "v_mfma_f32_16x16x32_bf16 v[4:7], v[144:147], a[212:215], v[4:7]\n"
    "s_waitcnt lgkmcnt(0)\n"
    "buffer_load_dwordx4 v200, s[24:27], s60 offen lds\n"
    "v_mfma_f32_16x16x32_bf16 v[8:11], v[144:147], a[216:219], v[8:11]\n"
    "s_waitcnt vmcnt(17)\n"
    "ds_read_b64_tr_b16 v[128:129], v192 offset:8192\n"
    "s_barrier\n"
    "v_mfma_f32_16x16x32_bf16 v[12:15], v[144:147], a[220:223], v[12:15]\n"
    "buffer_load_dwordx4 v201, s[24:27], s61 offen offset:1024 lds\n"
    "v_mfma_f32_16x16x32_bf16 v[16:19], v[148:151], a[208:211], v[16:19]\n"
    "ds_read_b64_tr_b16 v[130:131], v192 offset:8704\n"
    "v_mfma_f32_16x16x32_bf16 v[20:23], v[148:151], a[212:215], v[20:23]\n"
    "buffer_load_dwordx4 v200, s[24:27], s62 offen offset:2048 lds\n"
    "v_mfma_f32_16x16x32_bf16 v[24:27], v[148:151], a[216:219], v[24:27]\n"
    "ds_read_b64_tr_b16 v[132:133], v193 offset:8192\n"
    "v_mfma_f32_16x16x32_bf16 v[28:31], v[148:151], a[220:223], v[28:31]\n"
    "buffer_load_dwordx4 v201, s[24:27], s63 offen offset:3072 lds\n"
    "s_add_u32 s82, s82, 262144\n"
    "s_cmp_ge_u32 s82, 3670016\n"
    "s_cselect_b32 s82, 0, s82\n"
    "v_mfma_f32_16x16x32_bf16 v[32:35], v[152:155], a[208:211], v[32:35]\n"
    "ds_read_b64_tr_b16 v[134:135], v193 offset:8704\n"
    "v_mfma_f32_16x16x32_bf16 v[36:39], v[152:155], a[212:215], v[36:39]\n"
    "ds_read_b64_tr_b16 v[136:137], v194 offset:8192\n"
    "v_mfma_f32_16x16x32_bf16 v[40:43], v[152:155], a[216:219], v[40:43]\n"
    "ds_read_b64_tr_b16 v[138:139], v194 offset:8704\n"
    "v_mfma_f32_16x16x32_bf16 v[44:47], v[152:155], a[220:223], v[44:47]\n"
    "ds_read_b64_tr_b16 v[140:141], v195 offset:8192\n"
    "v_mfma_f32_16x16x32_bf16 v[48:51], v[156:159], a[208:211], v[48:51]\n"
    "ds_read_b64_tr_b16 v[142:143], v195 offset:8704\n"
    "v_mfma_f32_16x16x32_bf16 v[52:55], v[156:159], a[212:215], v[52:55]\n"
    "v_mfma_f32_16x16x32_bf16 v[56:59], v[156:159], a[216:219], v[56:59]\n"
    "v_mfma_f32_16x16x32_bf16 v[60:63], v[156:159], a[220:223], v[60:63]\n"
    "s_waitcnt lgkmcnt(6)\n"
    "v_mfma_f32_16x16x32_bf16 v[64:67], v[128:131], a[0:3], 0\n"
    "s_add_u32 s24, s83, s82\n"
    "s_addc_u32 s25, s84, 0\n"
    "s_add_u32 s38, s85, 8192\n"
    "s_mov_b32 m0, s38\n"
    "v_mfma_f32_16x16x32_bf16 v[68:71], v[128:131], a[4:7], 0\n"
    "s_waitcnt lgkmcnt(0)\n"
    "buffer_load_dwordx4 v200, s[24:27], s60 offen lds\n"
    "v_mfma_f32_16x16x32_bf16 v[72:75], v[128:131], a[8:11], 0\n"
    "s_waitcnt vmcnt(17)\n"
    "ds_read_b64_tr_b16 v[144:145], v192 offset:12288\n"
    "v_mul_f32 v0, s88, v0\n"
    "v_mfma_f32_16x16x32_bf16 v[76:79], v[128:131], a[12:15], 0\n"
    "buffer_load_dwordx4 v201, s[24:27], s61 offen offset:1024 lds\n"
    "v_mfma_f32_16x16x32_bf16 v[80:83], v[132:135], a[0:3], 0\n"
    "ds_read_b64_tr_b16 v[146:147], v192 offset:12800\n"
    "v_mul_f32 v1, s88, v1\n"
    "v_mfma_f32_16x16x32_bf16 v[84:87], v[132:135], a[4:7], 0\n"
    "buffer_load_dwordx4 v200, s[24:27], s62 offen offset:2048 lds\n"
    "v_mfma_f32_16x16x32_bf16 v[88:91], v[132:135], a[8:11], 0\n"
    "ds_read_b64_tr_b16 v[148:149], v193 offset:12288\n"
    "v_mul_f32 v2, s88, v2\n"
    "v_mfma_f32_16x16x32_bf16 v[92:95], v[132:135], a[12:15], 0\n"
    "buffer_load_dwordx4 v201, s[24:27], s63 offen offset:3072 lds\n"
    "s_add_u32 s82, s82, 262144\n"
    "s_cmp_ge_u32 s82, 3670016\n"
    "s_cselect_b32 s82, 0, s82\n"
    "v_mfma_f32_16x16x32_bf16 v[96:99], v[136:139], a[0:3], 0\n"
    "ds_read_b64_tr_b16 v[150:151], v193 offset:12800\n"
    "v_mul_f32 v3, s88, v3\n"
    "v_mfma_f32_16x16x32_bf16 v[100:103], v[136:139], a[4:7], 0\n"
    "ds_read_b64_tr_b16 v[152:153], v194 offset:12288\n"
    "v_cvt_pk_bf16_f32 v0, v0, v1\n"
    "v_mfma_f32_16x16x32_bf16 v[104:107], v[136:139], a[8:11], 0\n"
    "ds_read_b64_tr_b16 v[154:155], v194 offset:12800\n"
    "v_cvt_pk_bf16_f32 v1, v2, v3\n"
    "v_mfma_f32_16x16x32_bf16 v[108:111], v[136:139], a[12:15], 0\n"
    "ds_read_b64_tr_b16 v[156:157], v195 offset:12288\n"
    "v_mul_f32 v16, s88, v16\n"
    "v_mfma_f32_16x16x32_bf16 v[112:115], v[140:143], a[0:3], 0\n"
    "ds_read_b64_tr_b16 v[158:159], v195 offset:12800\n"
    "v_mul_f32 v17, s88, v17\n"
    "v_mfma_f32_16x16x32_bf16 v[116:119], v[140:143], a[4:7], 0\n"
    "v_mul_f32 v18, s88, v18\n"
    "v_mul_f32 v19, s88, v19\n"
    "v_mfma_f32_16x16x32_bf16 v[120:123], v[140:143], a[8:11], 0\n"
    "v_cvt_pk_bf16_f32 v16, v16, v17\n"
    "v_cvt_pk_bf16_f32 v17, v18, v19\n"
    "v_mfma_f32_16x16x32_bf16 v[124:127], v[140:143], a[12:15], 0\n"
    "s_waitcnt lgkmcnt(6)\n"
    "v_mfma_f32_16x16x32_bf16 v[64:67], v[144:147], a[16:19], v[64:67]\n"
    "s_add_u32 s24, s83, s82\n"
    "s_addc_u32 s25, s84, 0\n"
    "s_add_u32 s38, s85, 12288\n"
    "s_mov_b32 m0, s38\n"
    "v_mfma_f32_16x16x32_bf16 v[68:71], v[144:147], a[20:23], v[68:71]\n"
    "s_waitcnt lgkmcnt(0)\n"
    "buffer_load_dwordx4 v200, s[24:27], s60 offen lds\n"
    "v_mfma_f32_16x16x32_bf16 v[72:75], v[144:147], a[24:27], v[72:75]\n"
    "s_waitcnt vmcnt(17)\n"
    "ds_read_b64_tr_b16 v[128:129], v196\n"
    "v_mul_f32 v32, s88, v32\n"
    "v_mfma_f32_16x16x32_bf16 v[76:79], v[144:147], a[28:31], v[76:79]\n"
    "buffer_load_dwordx4 v201, s[24:27], s61 offen offset:1024 lds\n"
    "v_mfma_f32_16x16x32_bf16 v[80:83], v[148:151], a[16:19], v[80:83]\n"
    "ds_read_b64_tr_b16 v[130:131], v196 offset:512\n"
    "v_mul_f32 v33, s88, v33\n"
    "v_mfma_f32_16x16x32_bf16 v[84:87], v[148:151], a[20:23], v[84:87]\n"
    "buffer_load_dwordx4 v200, s[24:27], s62 offen offset:2048 lds\n"
    "v_mfma_f32_16x16x32_bf16 v[88:91], v[148:151], a[24:27], v[88:91]\n"
    "ds_read_b64_tr_b16 v[132:133], v197\n"
    "v_mul_f32 v34, s88, v34\n"
    "v_mfma_f32_16x16x32_bf16 v[92:95], v[148:151], a[28:31], v[92:95]\n"
    "buffer_load_dwordx4 v201, s[24:27], s63 offen offset:3072 lds\n"
    "s_add_u32 s82, s82, 262144\n"
    "s_cmp_ge_u32 s82, 3670016\n"
    "s_cselect_b32 s82, 0, s82\n"
    "v_mfma_f32_16x16x32_bf16 v[96:99], v[152:155], a[16:19], v[96:99]\n"
    "ds_read_b64_tr_b16 v[134:135], v197 offset:512\n"
    "v_mul_f32 v35, s88, v35\n"
    "v_mfma_f32_16x16x32_bf16 v[100:103], v[152:155], a[20:23], v[100:103]\n"
    "ds_read_b64_tr_b16 v[136:137], v198\n"
    "v_cvt_pk_bf16_f32 v32, v32, v33\n"
    "v_mfma_f32_16x16x32_bf16 v[104:107], v[152:155], a[24:27], v[104:107]\n"
    "ds_read_b64_tr_b16 v[138:139], v198 offset:512\n"
    "v_cvt_pk_bf16_f32 v33, v34, v35\n"
    "v_mfma_f32_16x16x32_bf16 v[108:111], v[152:155], a[28:31], v[108:111]\n"
    "ds_read_b64_tr_b16 v[140:141], v199\n"
    "v_mul_f32 v48, s88, v48\n"
    "v_mfma_f32_16x16x32_bf16 v[112:115], v[156:159], a[16:19], v[112:115]\n"
    "ds_read_b64_tr_b16 v[142:143], v199 offset:512\n"
    "v_mul_f32 v49, s88, v49\n"
    "v_mfma_f32_16x16x32_bf16 v[116:119], v[156:159], a[20:23], v[116:119]\n"
    "v_mul_f32 v50, s88, v50\n"
    "v_mul_f32 v51, s88, v51\n"
    "v_mfma_f32_16x16x32_bf16 v[120:123], v[156:159], a[24:27], v[120:123]\n"
    "v_cvt_pk_bf16_f32 v48, v48, v49\n"
    "v_cvt_pk_bf16_f32 v49, v50, v51\n"
    "v_mfma_f32_16x16x32_bf16 v[124:127], v[156:159], a[28:31], v[124:127]\n"
    "s_waitcnt lgkmcnt(6)\n"
    "v_mfma_f32_16x16x32_bf16 v[64:67], v[128:131], a[32:35], v[64:67]\n"
    "s_add_u32 s24, s83, s82\n"
    "s_addc_u32 s25, s84, 0\n"
    "s_mov_b32 m0, s86\n"
    "v_mfma_f32_16x16x32_bf16 v[68:71], v[128:131], a[36:39], v[68:71]\n"
    "s_waitcnt lgkmcnt(0)\n"
    "buffer_load_dwordx4 v200, s[24:27], s60 offen lds\n"
    "v_mfma_f32_16x16x32_bf16 v[72:75], v[128:131], a[40:43], v[72:75]\n"
    "s_waitcnt vmcnt(17)\n"
    "ds_read_b64_tr_b16 v[144:145], v196 offset:4096\n"
    "v_mul_f32 v4, s88, v4\n"
    "v_mfma_f32_16x16x32_bf16 v[76:79], v[128:131], a[44:47], v[76:79]\n"
    "buffer_load_dwordx4 v201, s[24:27], s61 offen offset:1024 lds\n"
    "v_mfma_f32_16x16x32_bf16 v[80:83], v[132:135], a[32:35], v[80:83]\n"
    "ds_read_b64_tr_b16 v[146:147], v196 offset:4608\n"
    "v_mul_f32 v5, s88, v5\n"
    "v_mfma_f32_16x16x32_bf16 v[84:87], v[132:135], a[36:39], v[84:87]\n"
    "buffer_load_dwordx4 v200, s[24:27], s62 offen offset:2048 lds\n"
    "v_mfma_f32_16x16x32_bf16 v[88:91], v[132:135], a[40:43], v[88:91]\n"
    "ds_read_b64_tr_b16 v[148:149], v197 offset:4096\n"
    "v_mul_f32 v6, s88, v6\n"
    "v_mfma_f32_16x16x32_bf16 v[92:95], v[132:135], a[44:47], v[92:95]\n"
    "buffer_load_dwordx4 v201, s[24:27], s63 offen offset:3072 lds\n"
    "s_add_u32 s82, s82, 262144\n"
    "s_cmp_ge_u32 s82, 3670016\n"
    "s_cselect_b32 s82, 0, s82\n"
    "v_mfma_f32_16x16x32_bf16 v[96:99], v[136:139], a[32:35], v[96:99]\n"
    "ds_read_b64_tr_b16 v[150:151], v197 offset:4608\n"
    "v_mul_f32 v7, s88, v7\n"
    "v_mfma_f32_16x16x32_bf16 v[100:103], v[136:139], a[36:39], v[100:103]\n"
    "ds_read_b64_tr_b16 v[152:153], v198 offset:4096\n"
    "v_cvt_pk_bf16_f32 v4, v4, v5\n"
    "v_mfma_f32_16x16x32_bf16 v[104:107], v[136:139], a[40:43], v[104:107]\n"
    "ds_read_b64_tr_b16 v[154:155], v198 offset:4608\n"
    "v_cvt_pk_bf16_f32 v5, v6, v7\n"
    "v_mfma_f32_16x16x32_bf16 v[108:111], v[136:139], a[44:47], v[108:111]\n"
    "ds_read_b64_tr_b16 v[156:157], v199 offset:4096\n"
    "v_mul_f32 v20, s88, v20\n"
    "v_mfma_f32_16x16x32_bf16 v[112:115], v[140:143], a[32:35], v[112:115]\n"
    "ds_read_b64_tr_b16 v[158:159], v199 offset:4608\n"
    "v_mul_f32 v21, s88, v21\n"
    "v_mfma_f32_16x16x32_bf16 v[116:119], v[140:143], a[36:39], v[116:119]\n"
    "v_mul_f32 v22, s88, v22\n"
    "v_mul_f32 v23, s88, v23\n"
    "v_mfma_f32_16x16x32_bf16 v[120:123], v[140:143], a[40:43], v[120:123]\n"
    "v_cvt_pk_bf16_f32 v20, v20, v21\n"
    "v_cvt_pk_bf16_f32 v21, v22, v23\n"
    "v_mfma_f32_16x16x32_bf16 v[124:127], v[140:143], a[44:47], v[124:127]\n"
    "s_waitcnt lgkmcnt(6)\n"
    "v_mfma_f32_16x16x32_bf16 v[64:67], v[144:147], a[48:51], v[64:67]\n"
    "s_add_u32 s24, s83, s82\n"
    "s_addc_u32 s25, s84, 0\n"
    "s_add_u32 s38, s86, 4096\n"
    "s_mov_b32 m0, s38\n"
    "v_mfma_f32_16x16x32_bf16 v[68:71], v[144:147], a[52:55], v[68:71]\n"
    "s_waitcnt lgkmcnt(0)\n"
    "buffer_load_dwordx4 v200, s[24:27], s60 offen lds\n"
    "v_mfma_f32_16x16x32_bf16 v[72:75], v[144:147], a[56:59], v[72:75]\n"
    "s_waitcnt vmcnt(17)\n"
    "ds_read_b64_tr_b16 v[128:129], v192\n"
    "v_mul_f32 v36, s88, v36\n"
    "v_mfma_f32_16x16x32_bf16 v[76:79], v[144:147], a[60:63], v[76:79]\n"
    "buffer_load_dwordx4 v201, s[24:27], s61 offen offset:1024 lds\n"
    "v_mfma_f32_16x16x32_bf16 v[80:83], v[148:151], a[48:51], v[80:83]\n"
    "ds_read_b64_tr_b16 v[130:131], v192 offset:512\n"
    "v_mul_f32 v37, s88, v37\n"
    "v_mfma_f32_16x16x32_bf16 v[84:87], v[148:151], a[52:55], v[84:87]\n"
    "buffer_load_dwordx4 v200, s[24:27], s62 offen offset:2048 lds\n"
    "v_mfma_f32_16x16x32_bf16 v[88:91], v[148:151], a[56:59], v[88:91]\n"
    "ds_read_b64_tr_b16 v[132:133], v193\n"
    "v_mul_f32 v38, s88, v38\n"
    "v_mfma_f32_16x16x32_bf16 v[92:95], v[148:151], a[60:63], v[92:95]\n"
    "buffer_load_dwordx4 v201, s[24:27], s63 offen offset:3072 lds\n"
    "s_add_u32 s82, s82, 262144\n"
    "s_cmp_ge_u32 s82, 3670016\n"
    "s_cselect_b32 s82, 0, s82\n"
    "v_mfma_f32_16x16x32_bf16 v[96:99], v[152:155], a[48:51], v[96:99]\n"
    "ds_read_b64_tr_b16 v[134:135], v193 offset:512\n"
    "v_mul_f32 v39, s88, v39\n"
    "v_mfma_f32_16x16x32_bf16 v[100:103], v[152:155], a[52:55], v[100:103]\n"
    "ds_read_b64_tr_b16 v[136:137], v194\n"
    "v_cvt_pk_bf16_f32 v36, v36, v37\n"
    "v_mfma_f32_16x16x32_bf16 v[104:107], v[152:155], a[56:59], v[104:107]\n"
    "ds_read_b64_tr_b16 v[138:139], v194 offset:512\n"
    "v_cvt_pk_bf16_f32 v37, v38, v39\n"
    "v_mfma_f32_16x16x32_bf16 v[108:111], v[152:155], a[60:63], v[108:111]\n"
    "ds_read_b64_tr_b16 v[140:141], v195\n"
    "v_mul_f32 v52, s88, v52\n"
    "v_mfma_f32_16x16x32_bf16 v[112:115], v[156:159], a[48:51], v[112:115]\n"
    "ds_read_b64_tr_b16 v[142:143], v195 offset:512\n"
    "v_mul_f32 v53, s88, v53\n"
    "v_mfma_f32_16x16x32_bf16 v[116:119], v[156:159], a[52:55], v[116:119]\n"
    "v_mul_f32 v54, s88, v54\n"
    "v_mul_f32 v55, s88, v55\n"
    "v_mfma_f32_16x16x32_bf16 v[120:123], v[156:159], a[56:59], v[120:123]\n"
    "v_cvt_pk_bf16_f32 v52, v52, v53\n"
    "v_cvt_pk_bf16_f32 v53, v54, v55\n"
    "v_mfma_f32_16x16x32_bf16 v[124:127], v[156:159], a[60:63], v[124:127]\n"
    "s_waitcnt lgkmcnt(6)\n"
    "v_mfma_f32_16x16x32_bf16 v[64:67], v[128:131], a[64:67], v[64:67]\n"
    "s_add_u32 s24, s83, s82\n"
    "s_addc_u32 s25, s84, 0\n"
    "s_mov_b32 m0, s85\n"
    "v_mfma_f32_16x16x32_bf16 v[68:71], v[128:131], a[68:71], v[68:71]\n"
    "s_waitcnt lgkmcnt(0)\n"
    "buffer_load_dwordx4 v200, s[24:27], s60 offen lds\n"
    "v_mfma_f32_16x16x32_bf16 v[72:75], v[128:131], a[72:75], v[72:75]\n"
    "s_waitcnt vmcnt(17)\n"
    "ds_read_b64_tr_b16 v[144:145], v192 offset:4096\n"
    "v_mul_f32 v8, s88, v8\n"
    "v_mfma_f32_16x16x32_bf16 v[76:79], v[128:131], a[76:79], v[76:79]\n"
    "buffer_load_dwordx4 v201, s[24:27], s61 offen offset:1024 lds\n"
    "v_mfma_f32_16x16x32_bf16 v[80:83], v[132:135], a[64:67], v[80:83]\n"
    "ds_read_b64_tr_b16 v[146:147], v192 offset:4608\n"
    "v_mul_f32 v9, s88, v9\n"
    "v_mfma_f32_16x16x32_bf16 v[84:87], v[132:135], a[68:71], v[84:87]\n"
    "buffer_load_dwordx4 v200, s[24:27], s62 offen offset:2048 lds\n"
    "v_mfma_f32_16x16x32_bf16 v[88:91], v[132:135], a[72:75], v[88:91]\n"
    "ds_read_b64_tr_b16 v[148:149], v193 offset:4096\n"
    "v_mul_f32 v10, s88, v10\n"
    "v_mfma_f32_16x16x32_bf16 v[92:95], v[132:135], a[76:79], v[92:95]\n"
    "buffer_load_dwordx4 v201, s[24:27], s63 offen offset:3072 lds\n"
    "s_add_u32 s82, s82, 262144\n"
    "s_cmp_ge_u32 s82, 3670016\n"
    "s_cselect_b32 s82, 0, s82\n"
    "v_mfma_f32_16x16x32_bf16 v[96:99], v[136:139], a[64:67], v[96:99]\n"
    "ds_read_b64_tr_b16 v[150:151], v193 offset:4608\n"
    "v_mul_f32 v11, s88, v11\n"
    "v_mfma_f32_16x16x32_bf16 v[100:103], v[136:139], a[68:71], v[100:103]\n"
    "ds_read_b64_tr_b16 v[152:153], v194 offset:4096\n"
    "v_cvt_pk_bf16_f32 v8, v8, v9\n"
    "v_mfma_f32_16x16x32_bf16 v[104:107], v[136:139], a[72:75], v[104:107]\n"
    "ds_read_b64_tr_b16 v[154:155], v194 offset:4608\n"
    "v_cvt_pk_bf16_f32 v9, v10, v11\n"
    "v_mfma_f32_16x16x32_bf16 v[108:111], v[136:139], a[76:79], v[108:111]\n"
    "ds_read_b64_tr_b16 v[156:157], v195 offset:4096\n"
    "v_mul_f32 v24, s88, v24\n"
    "v_mfma_f32_16x16x32_bf16 v[112:115], v[140:143], a[64:67], v[112:115]\n"
    "ds_read_b64_tr_b16 v[158:159], v195 offset:4608\n"
    "v_mul_f32 v25, s88, v25\n"
    "v_mfma_f32_16x16x32_bf16 v[116:119], v[140:143], a[68:71], v[116:119]\n"
    "v_mul_f32 v26, s88, v26\n"
    "v_mul_f32 v27, s88, v27\n"
    "v_mfma_f32_16x16x32_bf16 v[120:123], v[140:143], a[72:75], v[120:123]\n"
    "v_cvt_pk_bf16_f32 v24, v24, v25\n"
    "v_cvt_pk_bf16_f32 v25, v26, v27\n"
    "v_mfma_f32_16x16x32_bf16 v[124:127], v[140:143], a[76:79], v[124:127]\n"
    "s_waitcnt lgkmcnt(6)\n"
    "v_mfma_f32_16x16x32_bf16 v[64:67], v[144:147], a[80:83], v[64:67]\n"
    "s_add_u32 s24, s83, s82\n"
    "s_addc_u32 s25, s84, 0\n"
    "s_add_u32 s38, s85, 4096\n"
    "s_mov_b32 m0, s38\n"
    "v_mfma_f32_16x16x32_bf16 v[68:71], v[144:147], a[84:87], v[68:71]\n"
    "s_waitcnt lgkmcnt(0)\n"
    "buffer_load_dwordx4 v200, s[24:27], s60 offen lds\n"
    "v_mfma_f32_16x16x32_bf16 v[72:75], v[144:147], a[88:91], v[72:75]\n"
    "s_waitcnt vmcnt(17)\n"
    "ds_read_b64_tr_b16 v[128:129], v192 offset:8192\n"
    "v_mul_f32 v40, s88, v40\n"
    "v_mfma_f32_16x16x32_bf16 v[76:79], v[144:147], a[92:95], v[76:79]\n"
    "buffer_load_dwordx4 v201, s[24:27], s61 offen offset:1024 lds\n"
    "v_mfma_f32_16x16x32_bf16 v[80:83], v[148:151], a[80:83], v[80:83]\n"
    "ds_read_b64_tr_b16 v[130:131], v192 offset:8704\n"
    "v_mul_f32 v41, s88, v41\n"
    "v_mfma_f32_16x16x32_bf16 v[84:87], v[148:151], a[84:87], v[84:87]\n"
    "buffer_load_dwordx4 v200, s[24:27], s62 offen offset:2048 lds\n"
    "v_mfma_f32_16x16x32_bf16 v[88:91], v[148:151], a[88:91], v[88:91]\n"
    "ds_read_b64_tr_b16 v[132:133], v193 offset:8192\n"
    "v_mul_f32 v42, s88, v42\n"
    "v_mfma_f32_16x16x32_bf16 v[92:95], v[148:151], a[92:95], v[92:95]\n"
    "buffer_load_dwordx4 v201, s[24:27], s63 offen offset:3072 lds\n"
    "s_add_u32 s82, s82, 262144\n"
    "s_cmp_ge_u32 s82, 3670016\n"
    "s_cselect_b32 s82, 0, s82\n"
    "v_mfma_f32_16x16x32_bf16 v[96:99], v[152:155], a[80:83], v[96:99]\n"
    "ds_read_b64_tr_b16 v[134:135], v193 offset:8704\n"
    "v_mul_f32 v43, s88, v43\n"
    "v_mfma_f32_16x16x32_bf16 v[100:103], v[152:155], a[84:87], v[100:103]\n"
    "ds_read_b64_tr_b16 v[136:137], v194 offset:8192\n"
    "v_cvt_pk_bf16_f32 v40, v40, v41\n"
    "v_mfma_f32_16x16x32_bf16 v[104:107], v[152:155], a[88:91], v[104:107]\n"
    "ds_read_b64_tr_b16 v[138:139], v194 offset:8704\n"
    "v_cvt_pk_bf16_f32 v41, v42, v43\n"
    "v_mfma_f32_16x16x32_bf16 v[108:111], v[152:155], a[92:95], v[108:111]\n"
    "ds_read_b64_tr_b16 v[140:141], v195 offset:8192\n"
    "v_mul_f32 v56, s88, v56\n"
    "v_mfma_f32_16x16x32_bf16 v[112:115], v[156:159], a[80:83], v[112:115]\n"
    "ds_read_b64_tr_b16 v[142:143], v195 offset:8704\n"
    "v_mul_f32 v57, s88, v57\n"
    "v_mfma_f32_16x16x32_bf16 v[116:119], v[156:159], a[84:87], v[116:119]\n"
    "v_mul_f32 v58, s88, v58\n"
    "v_mul_f32 v59, s88, v59\n"
    "v_mfma_f32_16x16x32_bf16 v[120:123], v[156:159], a[88:91], v[120:123]\n"
    "v_cvt_pk_bf16_f32 v56, v56, v57\n"
    "v_cvt_pk_bf16_f32 v57, v58, v59\n"
    "v_mfma_f32_16x16x32_bf16 v[124:127], v[156:159], a[92:95], v[124:127]\n"
    "s_waitcnt lgkmcnt(6)\n"
    "v_mfma_f32_16x16x32_bf16 v[64:67], v[128:131], a[96:99], v[64:67]\n"
    "s_add_u32 s24, s83, s82\n"
    "s_addc_u32 s25, s84, 0\n"
    "s_add_u32 s38, s85, 8192\n"
    "s_mov_b32 m0, s38\n"
    "v_mfma_f32_16x16x32_bf16 v[68:71], v[128:131], a[100:103], v[68:71]\n"
    "s_waitcnt lgkmcnt(0)\n"
    "buffer_load_dwordx4 v200, s[24:27], s60 offen lds\n"
    "v_mfma_f32_16x16x32_bf16 v[72:75], v[128:131], a[104:107], v[72:75]\n"
    "s_waitcnt vmcnt(17)\n"
    "ds_read_b64_tr_b16 v[144:145], v192 offset:12288\n"
    "v_mul_f32 v12, s88, v12\n"
    "v_mfma_f32_16x16x32_bf16 v[76:79], v[128:131], a[108:111], v[76:79]\n"
    "buffer_load_dwordx4 v201, s[24:27], s61 offen offset:1024 lds\n"
    "v_mfma_f32_16x16x32_bf16 v[80:83], v[132:135], a[96:99], v[80:83]\n"
    "ds_read_b64_tr_b16 v[146:147], v192 offset:12800\n"
    "v_mul_f32 v13, s88, v13\n"
    "v_mfma_f32_16x16x32_bf16 v[84:87], v[132:135], a[100:103], v[84:87]\n"
    "buffer_load_dwordx4 v200, s[24:27], s62 offen offset:2048 lds\n"
    "v_mfma_f32_16x16x32_bf16 v[88:91], v[132:135], a[104:107], v[88:91]\n"
    "ds_read_b64_tr_b16 v[148:149], v193 offset:12288\n"
    "v_mul_f32 v14, s88, v14\n"
    "v_mfma_f32_16x16x32_bf16 v[92:95], v[132:135], a[108:111], v[92:95]\n"
    "buffer_load_dwordx4 v201, s[24:27], s63 offen offset:3072 lds\n"
    "s_add_u32 s82, s82, 262144\n"
    "s_cmp_ge_u32 s82, 3670016\n"
    "s_cselect_b32 s82, 0, s82\n"
    "v_mfma_f32_16x16x32_bf16 v[96:99], v[136:139], a[96:99], v[96:99]\n"
    "ds_read_b64_tr_b16 v[150:151], v193 offset:12800\n"
    "v_mul_f32 v15, s88, v15\n"
    "v_mfma_f32_16x16x32_bf16 v[100:103], v[136:139], a[100:103], v[100:103]\n"
    "ds_read_b64_tr_b16 v[152:153], v194 offset:12288\n"
    "v_cvt_pk_bf16_f32 v12, v12, v13\n"
    "v_mfma_f32_16x16x32_bf16 v[104:107], v[136:139], a[104:107], v[104:107]\n"
    "ds_read_b64_tr_b16 v[154:155], v194 offset:12800\n"
    "v_cvt_pk_bf16_f32 v13, v14, v15\n"
    "v_mfma_f32_16x16x32_bf16 v[108:111], v[136:139], a[108:111], v[108:111]\n"
    "ds_read_b64_tr_b16 v[156:157], v195 offset:12288\n"
    "v_mul_f32 v28, s88, v28\n"
    "v_mfma_f32_16x16x32_bf16 v[112:115], v[140:143], a[96:99], v[112:115]\n"
    "ds_read_b64_tr_b16 v[158:159], v195 offset:12800\n"
    "v_mul_f32 v29, s88, v29\n"
    "v_mfma_f32_16x16x32_bf16 v[116:119], v[140:143], a[100:103], v[116:119]\n"
    "v_mul_f32 v30, s88, v30\n"
    "v_mul_f32 v31, s88, v31\n"
    "v_mfma_f32_16x16x32_bf16 v[120:123], v[140:143], a[104:107], v[120:123]\n"
    "v_cvt_pk_bf16_f32 v28, v28, v29\n"
    "v_cvt_pk_bf16_f32 v29, v30, v31\n"
    "v_mfma_f32_16x16x32_bf16 v[124:127], v[140:143], a[108:111], v[124:127]\n"
    "s_waitcnt lgkmcnt(6)\n"
    "v_mfma_f32_16x16x32_bf16 v[64:67], v[144:147], a[112:115], v[64:67]\n"
    "s_add_u32 s24, s83, s82\n"
    "s_addc_u32 s25, s84, 0\n"
    "s_add_u32 s38, s85, 12288\n"
    "s_mov_b32 m0, s38\n"
    "v_mfma_f32_16x16x32_bf16 v[68:71], v[144:147], a[116:119], v[68:71]\n"
    "s_waitcnt lgkmcnt(0)\n"
    "buffer_load_dwordx4 v200, s[24:27], s60 offen lds\n"
    "v_mfma_f32_16x16x32_bf16 v[72:75], v[144:147], a[120:123], v[72:75]\n"
    "s_waitcnt vmcnt(17)\n"
    "ds_read_b64_tr_b16 v[128:129], v196\n"
    "v_mul_f32 v44, s88, v44\n"
    "v_mfma_f32_16x16x32_bf16 v[76:79], v[144:147], a[124:127], v[76:79]\n"
    "buffer_load_dwordx4 v201, s[24:27], s61 offen offset:1024 lds\n"
    "v_mfma_f32_16x16x32_bf16 v[80:83], v[148:151], a[112:115], v[80:83]\n"
    "ds_read_b64_tr_b16 v[130:131], v196 offset:512\n"
    "v_mul_f32 v45, s88, v45\n"
    "v_mfma_f32_16x16x32_bf16 v[84:87], v[148:151], a[116:119], v[84:87]\n"
    "buffer_load_dwordx4 v200, s[24:27], s62 offen offset:2048 lds\n"
    "v_mfma_f32_16x16x32_bf16 v[88:91], v[148:151], a[120:123], v[88:91]\n"
    "ds_read_b64_tr_b16 v[132:133], v197\n"
    "v_mul_f32 v46, s88, v46\n"
    "v_mfma_f32_16x16x32_bf16 v[92:95], v[148:151], a[124:127], v[92:95]\n"
    "buffer_load_dwordx4 v201, s[24:27], s63 offen offset:3072 lds\n"
    "s_add_u32 s82, s82, 262144\n"
    "s_cmp_ge_u32 s82, 3670016\n"
    "s_cselect_b32 s82, 0, s82\n"
    "v_mfma_f32_16x16x32_bf16 v[96:99], v[152:155], a[112:115], v[96:99]\n"
    "ds_read_b64_tr_b16 v[134:135], v197 offset:512\n"
    "v_mul_f32 v47, s88, v47\n"
    "v_mfma_f32_16x16x32_bf16 v[100:103], v[152:155], a[116:119], v[100:103]\n"
    "ds_read_b64_tr_b16 v[136:137], v198\n"
    "v_cvt_pk_bf16_f32 v44, v44, v45\n"
    "v_mfma_f32_16x16x32_bf16 v[104:107], v[152:155], a[120:123], v[104:107]\n"
    "ds_read_b64_tr_b16 v[138:139], v198 offset:512\n"
    "v_cvt_pk_bf16_f32 v45, v46, v47\n"
    "v_mfma_f32_16x16x32_bf16 v[108:111], v[152:155], a[124:127], v[108:111]\n"
    "ds_read_b64_tr_b16 v[140:141], v199\n"
    "v_mul_f32 v60, s88, v60\n"
    "v_mfma_f32_16x16x32_bf16 v[112:115], v[156:159], a[112:115], v[112:115]\n"
    "ds_read_b64_tr_b16 v[142:143], v199 offset:512\n"
    "v_mul_f32 v61, s88, v61\n"
    "v_mfma_f32_16x16x32_bf16 v[116:119], v[156:159], a[116:119], v[116:119]\n"
    "v_mul_f32 v62, s88, v62\n"
    "v_mul_f32 v63, s88, v63\n"
    "v_mfma_f32_16x16x32_bf16 v[120:123], v[156:159], a[120:123], v[120:123]\n"
    "v_cvt_pk_bf16_f32 v60, v60, v61\n"
    "v_cvt_pk_bf16_f32 v61, v62, v63\n"
    "v_mfma_f32_16x16x32_bf16 v[124:127], v[156:159], a[124:127], v[124:127]\n"
    "s_waitcnt lgkmcnt(6)\n"
    "v_mfma_f32_16x16x32_bf16 v[64:67], v[128:131], a[128:131], v[64:67]\n"
    "s_waitcnt vmcnt(16)\n"
    "ds_read_b64_tr_b16 v[144:145], v196 offset:4096\n"
    "v_mfma_f32_16x16x32_bf16 v[68:71], v[128:131], a[132:135], v[68:71]\n"
    "ds_read_b64_tr_b16 v[146:147], v196 offset:4608\n"
    "s_mov_b32 m0, s72\n"
    "v_mfma_f32_16x16x32_bf16 v[72:75], v[128:131], a[136:139], v[72:75]\n"
    "ds_read_b64_tr_b16 v[148:149], v197 offset:4096\n"
    "buffer_load_dwordx4 v234, s[20:23], s40 offen nt lds\n"
    "v_mfma_f32_16x16x32_bf16 v[76:79], v[128:131], a[140:143], v[76:79]\n"
    "ds_read_b64_tr_b16 v[150:151], v197 offset:4608\n"
    "s_waitcnt lgkmcnt(8)\n"
    "v_mfma_f32_16x16x32_bf16 v[80:83], v[132:135], a[128:131], v[80:83]\n"
    "ds_read_b64_tr_b16 v[152:153], v198 offset:4096\n"
    "v_mfma_f32_16x16x32_bf16 v[84:87], v[132:135], a[132:135], v[84:87]\n"
    "ds_read_b64_tr_b16 v[154:155], v198 offset:4608\n"
    "v_mfma_f32_16x16x32_bf16 v[88:91], v[132:135], a[136:139], v[88:91]\n"
    "ds_read_b64_tr_b16 v[156:157], v199 offset:4096\n"
    "buffer_load_dwordx4 v234, s[20:23], s41 offen offset:1024 nt lds\n"
    "v_mfma_f32_16x16x32_bf16 v[92:95], v[132:135], a[140:143], v[92:95]\n"
    "ds_read_b64_tr_b16 v[158:159], v199 offset:4608\n"
    "s_waitcnt lgkmcnt(10)\n"
    "v_mfma_f32_16x16x32_bf16 v[96:99], v[136:139], a[128:131], v[96:99]\n"
    "v_mfma_f32_16x16x32_bf16 v[100:103], v[136:139], a[132:135], v[100:103]\n"
    "v_mfma_f32_16x16x32_bf16 v[104:107], v[136:139], a[136:139], v[104:107]\n"
    "buffer_load_dwordx4 v234, s[20:23], s42 offen offset:2048 nt lds\n"
    "v_mfma_f32_16x16x32_bf16 v[108:111], v[136:139], a[140:143], v[108:111]\n"
    "s_waitcnt lgkmcnt(8)\n"
    "v_mfma_f32_16x16x32_bf16 v[112:115], v[140:143], a[128:131], v[112:115]\n"
    "v_mfma_f32_16x16x32_bf16 v[116:119], v[140:143], a[132:135], v[116:119]\n"
    "v_mfma_f32_16x16x32_bf16 v[120:123], v[140:143], a[136:139], v[120:123]\n"
    "buffer_load_dwordx4 v234, s[20:23], s43 offen offset:3072 nt lds\n"
    "v_mfma_f32_16x16x32_bf16 v[124:127], v[140:143], a[140:143], v[124:127]\n"
    "s_waitcnt lgkmcnt(6)\n"
    "v_mfma_f32_16x16x32_bf16 v[64:67], v[144:147], a[144:147], v[64:67]\n"
    "s_waitcnt vmcnt(16)\n"
    "ds_read_b64_tr_b16 v[128:129], v192\n"
    "v_mfma_f32_16x16x32_bf16 v[68:71], v[144:147], a[148:151], v[68:71]\n"
    "ds_read_b64_tr_b16 v[130:131], v192 offset:512\n"
    "s_mov_b32 m0, s73\n"
    "v_mfma_f32_16x16x32_bf16 v[72:75], v[144:147], a[152:155], v[72:75]\n"
    "ds_read_b64_tr_b16 v[132:133], v193\n"
    "buffer_load_dwordx4 v234, s[20:23], s44 offen nt lds\n"
    "v_mfma_f32_16x16x32_bf16 v[76:79], v[144:147], a[156:159], v[76:79]\n"
    "ds_read_b64_tr_b16 v[134:135], v193 offset:512\n"
    "s_waitcnt lgkmcnt(8)\n"
    "v_mfma_f32_16x16x32_bf16 v[80:83], v[148:151], a[144:147], v[80:83]\n"
    "ds_read_b64_tr_b16 v[136:137], v194\n"
    "v_mfma_f32_16x16x32_bf16 v[84:87], v[148:151], a[148:151], v[84:87]\n"
    "ds_read_b64_tr_b16 v[138:139], v194 offset:512\n"
    "v_mfma_f32_16x16x32_bf16 v[88:91], v[148:151], a[152:155], v[88:91]\n"
    "ds_read_b64_tr_b16 v[140:141], v195\n"
    "buffer_load_dwordx4 v234, s[20:23], s45 offen offset:1024 nt lds\n"
    "v_mfma_f32_16x16x32_bf16 v[92:95], v[148:151], a[156:159], v[92:95]\n"
    "ds_read_b64_tr_b16 v[142:143], v195 offset:512\n"
    "s_waitcnt lgkmcnt(10)\n"
    "v_mfma_f32_16x16x32_bf16 v[96:99], v[152:155], a[144:147], v[96:99]\n"
    "s_lshl_b32 s39, s36, 7\n"
    "s_add_u32 s24, s10, s39\n"
    "s_addc_u32 s25, s11, 0\n"
    "v_mfma_f32_16x16x32_bf16 v[100:103], v[152:155], a[148:151], v[100:103]\n"
    "s_and_b32 s25, s25, 0xffff\n"
    "s_mov_b32 s26, 0x10000\n"
    "s_mov_b32 s56, 0\n"
    "v_mfma_f32_16x16x32_bf16 v[104:107], v[152:155], a[152:155], v[104:107]\n"
    "buffer_load_dwordx4 v234, s[20:23], s46 offen offset:2048 nt lds\n"
    "s_mov_b32 s57, 0xc00\n"
    "s_mov_b32 s58, 0x1800\n"
    "v_mfma_f32_16x16x32_bf16 v[108:111], v[152:155], a[156:159], v[108:111]\n"
    "s_mov_b32 s59, 0x2400\n"
    "s_waitcnt lgkmcnt(8)\n"
    "v_mfma_f32_16x16x32_bf16 v[112:115], v[156:159], a[144:147], v[112:115]\n"
    "v_mfma_f32_16x16x32_bf16 v[116:119], v[156:159], a[148:151], v[116:119]\n"
    "v_mfma_f32_16x16x32_bf16 v[120:123], v[156:159], a[152:155], v[120:123]\n"
    "buffer_load_dwordx4 v234, s[20:23], s47 offen offset:3072 nt lds\n"
    "v_mfma_f32_16x16x32_bf16 v[124:127], v[156:159], a[156:159], v[124:127]\n"
    "s_waitcnt lgkmcnt(6)\n"
    "v_mfma_f32_16x16x32_bf16 v[64:67], v[128:131], a[160:163], v[64:67]\n"
    "s_waitcnt vmcnt(16)\n"
    "ds_read_b64_tr_b16 v[144:145], v192 offset:4096\n"
    "v_mfma_f32_16x16x32_bf16 v[68:71], v[128:131], a[164:167], v[68:71]\n"
    "ds_read_b64_tr_b16 v[146:147], v192 offset:4608\n"
    "s_mov_b32 m0, s74\n"
    "v_mfma_f32_16x16x32_bf16 v[72:75], v[128:131], a[168:171], v[72:75]\n"
    "ds_read_b64_tr_b16 v[148:149], v193 offset:4096\n"
    "buffer_load_dwordx4 v234, s[20:23], s48 offen nt lds\n"
    "v_mfma_f32_16x16x32_bf16 v[76:79], v[128:131], a[172:175], v[76:79]\n"
    "ds_read_b64_tr_b16 v[150:151], v193 offset:4608\n"
    "s_mov_b32 s60, 0x4000\n"
    "s_mov_b32 s61, 0x4c00\n"
    "s_waitcnt lgkmcnt(8)\n"
    "v_mfma_f32_16x16x32_bf16 v[80:83], v[132:135], a[160:163], v[80:83]\n"
    "ds_read_b64_tr_b16 v[152:153], v194 offset:4096\n"
    "s_mov_b32 s62, 0x5800\n"
    "s_mov_b32 s63, 0x6400\n"
    "v_mfma_f32_16x16x32_bf16 v[84:87], v[132:135], a[164:167], v[84:87]\n"
    "ds_read_b64_tr_b16 v[154:155], v194 offset:4608\n"
    "s_mov_b32 s64, 0x8000\n"
    "s_mov_b32 s65, 0x8c00\n"
    "v_mfma_f32_16x16x32_bf16 v[88:91], v[132:135], a[168:171], v[88:91]\n"
    "ds_read_b64_tr_b16 v[156:157], v195 offset:4096\n"
    "buffer_load_dwordx4 v234, s[20:23], s49 offen offset:1024 nt lds\n"
    "v_mfma_f32_16x16x32_bf16 v[92:95], v[132:135], a[172:175], v[92:95]\n"
    "ds_read_b64_tr_b16 v[158:159], v195 offset:4608\n"
    "s_mov_b32 s66, 0x9800\n"
    "s_waitcnt lgkmcnt(10)\n"
    "v_mfma_f32_16x16x32_bf16 v[96:99], v[136:139], a[160:163], v[96:99]\n"
    "v_mfma_f32_16x16x32_bf16 v[100:103], v[136:139], a[164:167], v[100:103]\n"
    "v_mfma_f32_16x16x32_bf16 v[104:107], v[136:139], a[168:171], v[104:107]\n"
    "buffer_load_dwordx4 v234, s[20:23], s50 offen offset:2048 nt lds\n"
    "v_mfma_f32_16x16x32_bf16 v[108:111], v[136:139], a[172:175], v[108:111]\n"
    "s_waitcnt lgkmcnt(8)\n"
    "v_mfma_f32_16x16x32_bf16 v[112:115], v[140:143], a[160:163], v[112:115]\n"
    "v_mfma_f32_16x16x32_bf16 v[116:119], v[140:143], a[164:167], v[116:119]\n"
    "v_mfma_f32_16x16x32_bf16 v[120:123], v[140:143], a[168:171], v[120:123]\n"
    "buffer_load_dwordx4 v234, s[20:23], s51 offen offset:3072 nt lds\n"
    "v_mfma_f32_16x16x32_bf16 v[124:127], v[140:143], a[172:175], v[124:127]\n"
    "s_waitcnt lgkmcnt(6)\n"
    "v_mfma_f32_16x16x32_bf16 v[64:67], v[144:147], a[176:179], v[64:67]\n"
    "s_waitcnt vmcnt(16)\n"
    "ds_read_b64_tr_b16 v[128:129], v192 offset:8192\n"
    "v_mfma_f32_16x16x32_bf16 v[68:71], v[144:147], a[180:183], v[68:71]\n"
    "ds_read_b64_tr_b16 v[130:131], v192 offset:8704\n"
    "s_mov_b32 m0, s75\n"
    "v_mfma_f32_16x16x32_bf16 v[72:75], v[144:147], a[184:187], v[72:75]\n"
    "ds_read_b64_tr_b16 v[132:133], v193 offset:8192\n"
    "buffer_load_dwordx4 v234, s[20:23], s52 offen nt lds\n"
    "v_mfma_f32_16x16x32_bf16 v[76:79], v[144:147], a[188:191], v[76:79]\n"
    "ds_read_b64_tr_b16 v[134:135], v193 offset:8704\n"
    "s_mov_b32 s67, 0xa400\n"
    "s_mov_b32 s68, 0xc000\n"
    "s_waitcnt lgkmcnt(8)\n"
    "v_mfma_f32_16x16x32_bf16 v[80:83], v[148:151], a[176:179], v[80:83]\n"
    "ds_read_b64_tr_b16 v[136:137], v194 offset:8192\n"
    "s_mov_b32 s69, 0xcc00\n"
    "s_mov_b32 s70, 0xd800\n"
    "v_mfma_f32_16x16x32_bf16 v[84:87], v[148:151], a[180:183], v[84:87]\n"
    "ds_read_b64_tr_b16 v[138:139], v194 offset:8704\n"
    "s_mov_b32 s71, 0xe400\n"
    "v_mfma_f32_16x16x32_bf16 v[88:91], v[148:151], a[184:187], v[88:91]\n"
    "ds_read_b64_tr_b16 v[140:141], v195 offset:8192\n"
    "buffer_load_dwordx4 v234, s[20:23], s53 offen offset:1024 nt lds\n"
    "v_mfma_f32_16x16x32_bf16 v[92:95], v[148:151], a[188:191], v[92:95]\n"
    "ds_read_b64_tr_b16 v[142:143], v195 offset:8704\n"
    "s_waitcnt lgkmcnt(10)\n"
    "v_mfma_f32_16x16x32_bf16 v[96:99], v[152:155], a[176:179], v[96:99]\n"
    "v_mfma_f32_16x16x32_bf16 v[100:103], v[152:155], a[180:183], v[100:103]\n"
    "v_mfma_f32_16x16x32_bf16 v[104:107], v[152:155], a[184:187], v[104:107]\n"
    "buffer_load_dwordx4 v234, s[20:23], s54 offen offset:2048 nt lds\n"
    "v_mfma_f32_16x16x32_bf16 v[108:111], v[152:155], a[188:191], v[108:111]\n"
    "s_waitcnt lgkmcnt(8)\n"
    "v_mfma_f32_16x16x32_bf16 v[112:115], v[156:159], a[176:179], v[112:115]\n"
    "v_mfma_f32_16x16x32_bf16 v[116:119], v[156:159], a[180:183], v[116:119]\n"
    "v_mfma_f32_16x16x32_bf16 v[120:123], v[156:159], a[184:187], v[120:123]\n"
    "buffer_load_dwordx4 v234, s[20:23], s55 offen offset:3072 nt lds\n"
    "v_mfma_f32_16x16x32_bf16 v[124:127], v[156:159], a[188:191], v[124:127]\n"
    "s_waitcnt lgkmcnt(0)\n"
    "s_barrier\n"
    "v_mfma_f32_16x16x32_bf16 v[64:67], v[128:131], a[192:195], v[64:67]\n"
    "s_waitcnt vmcnt(16)\n"
    "ds_read_b64_tr_b16 v[144:145], v192 offset:12288\n"
    "v_mfma_f32_16x16x32_bf16 v[68:71], v[128:131], a[196:199], v[68:71]\n"
    "ds_read_b64_tr_b16 v[146:147], v192 offset:12800\n"
    "v_mfma_f32_16x16x32_bf16 v[72:75], v[128:131], a[200:203], v[72:75]\n"
    "ds_read_b64_tr_b16 v[148:149], v193 offset:12288\n"
    "v_mfma_f32_16x16x32_bf16 v[76:79], v[128:131], a[204:207], v[76:79]\n"
    "ds_read_b64_tr_b16 v[150:151], v193 offset:12800\n"
    "v_mfma_f32_16x16x32_bf16 v[80:83], v[132:135], a[192:195], v[80:83]\n"
    "ds_read_b64_tr_b16 v[152:153], v194 offset:12288\n"
    "v_mfma_f32_16x16x32_bf16 v[84:87], v[132:135], a[196:199], v[84:87]\n"
    "ds_read_b64_tr_b16 v[154:155], v194 offset:12800\n"
    "v_mfma_f32_16x16x32_bf16 v[88:91], v[132:135], a[200:203], v[88:91]\n"
    "ds_read_b64_tr_b16 v[156:157], v195 offset:12288\n"
    "v_mfma_f32_16x16x32_bf16 v[92:95], v[132:135], a[204:207], v[92:95]\n"
    "ds_read_b64_tr_b16 v[158:159], v195 offset:12800\n"
    "v_mfma_f32_16x16x32_bf16 v[96:99], v[136:139], a[192:195], v[96:99]\n"
    "ds_write_b64 v160, v[0:1]\n"
    "v_mfma_f32_16x16x32_bf16 v[100:103], v[136:139], a[196:199], v[100:103]\n"
    "ds_write_b64 v161, v[16:17]\n"
    "v_mfma_f32_16x16x32_bf16 v[104:107], v[136:139], a[200:203], v[104:107]\n"
    "ds_write_b64 v162, v[32:33]\n"
    "v_mfma_f32_16x16x32_bf16 v[108:111], v[136:139], a[204:207], v[108:111]\n"
    "ds_write_b64 v163, v[48:49]\n"
    "v_mfma_f32_16x16x32_bf16 v[112:115], v[140:143], a[192:195], v[112:115]\n"
    "ds_write_b64 v160, v[4:5] offset:8192\n"
    "v_mfma_f32_16x16x32_bf16 v[116:119], v[140:143], a[196:199], v[116:119]\n"
    "ds_write_b64 v161, v[20:21] offset:8192\n"
    "v_mfma_f32_16x16x32_bf16 v[120:123], v[140:143], a[200:203], v[120:123]\n"
    "ds_write_b64 v162, v[36:37] offset:8192\n"
    "v_mfma_f32_16x16x32_bf16 v[124:127], v[140:143], a[204:207], v[124:127]\n"
    "ds_write_b64 v163, v[52:53] offset:8192\n"
    "s_waitcnt lgkmcnt(14)\n"
    "v_mfma_f32_16x16x32_bf16 v[64:67], v[144:147], a[208:211], v[64:67]\n"
    "v_mfma_f32_16x16x32_bf16 v[68:71], v[144:147], a[212:215], v[68:71]\n"
    "ds_write_b64 v160, v[8:9] offset:16384\n"
    "v_mfma_f32_16x16x32_bf16 v[72:75], v[144:147], a[216:219], v[72:75]\n"
    "ds_write_b64 v161, v[24:25] offset:16384\n"
    "v_mfma_f32_16x16x32_bf16 v[76:79], v[144:147], a[220:223], v[76:79]\n"
    "ds_write_b64 v162, v[40:41] offset:16384\n"
    "s_waitcnt lgkmcnt(15)\n"
    "v_mfma_f32_16x16x32_bf16 v[80:83], v[148:151], a[208:211], v[80:83]\n"
    "ds_write_b64 v163, v[56:57] offset:16384\n"
    "v_mfma_f32_16x16x32_bf16 v[84:87], v[148:151], a[212:215], v[84:87]\n"
    "ds_write_b64 v160, v[12:13] offset:24576\n"
    "v_mfma_f32_16x16x32_bf16 v[88:91], v[148:151], a[216:219], v[88:91]\n"
    "ds_write_b64 v161, v[28:29] offset:24576\n"
    "v_mfma_f32_16x16x32_bf16 v[92:95], v[148:151], a[220:223], v[92:95]\n"
    "ds_write_b64 v162, v[44:45] offset:24576\n"
    "s_waitcnt lgkmcnt(15)\n"
    "v_mfma_f32_16x16x32_bf16 v[96:99], v[152:155], a[208:211], v[96:99]\n"
    "ds_write_b64 v163, v[60:61] offset:24576\n"
    "v_mfma_f32_16x16x32_bf16 v[100:103], v[152:155], a[212:215], v[100:103]\n"
    "v_mfma_f32_16x16x32_bf16 v[104:107], v[152:155], a[216:219], v[104:107]\n"
    "v_mfma_f32_16x16x32_bf16 v[108:111], v[152:155], a[220:223], v[108:111]\n"
    "v_mfma_f32_16x16x32_bf16 v[112:115], v[156:159], a[208:211], v[112:115]\n"
    "v_mfma_f32_16x16x32_bf16 v[116:119], v[156:159], a[212:215], v[116:119]\n"
    "v_mfma_f32_16x16x32_bf16 v[120:123], v[156:159], a[216:219], v[120:123]\n"
    "v_mfma_f32_16x16x32_bf16 v[124:127], v[156:159], a[220:223], v[124:127]\n"
    "s_mov_b32 m0, s76\n"
    "s_nop 0\n"
    "buffer_load_dwordx4 v234, s[24:27], s56 offen nt lds\n"
    "s_waitcnt lgkmcnt(0)\n"
    "s_barrier\n"
    "v_xor_b32 v172, 0, v168\n"
    "ds_read_b128 a[0:3], v172\n"
    "v_xor_b32 v173, 0, v168\n"
    "ds_read_b128 a[32:35], v173 offset:8192\n"
    "v_xor_b32 v174, 0, v168\n"
    "ds_read_b128 a[64:67], v174 offset:16384\n"
    "v_xor_b32 v175, 64, v168\n"
    "ds_read_b128 a[4:7], v175\n"
    "v_xor_b32 v172, 64, v168\n"
    "ds_read_b128 a[36:39], v172 offset:8192\n"
    "v_xor_b32 v173, 64, v168\n"
    "ds_read_b128 a[68:71], v173 offset:16384\n"
    "v_pk_mul_f32 v[64:65], v[64:65], s[88:89]\n"
    "v_pk_mul_f32 v[66:67], v[66:67], s[88:89]\n"
    "v_cvt_pk_bf16_f32 v64, v64, v65\n"
    "v_cvt_pk_bf16_f32 v65, v66, v67\n"
    "v_pk_mul_f32 v[80:81], v[80:81], s[88:89]\n"
    "v_pk_mul_f32 v[82:83], v[82:83], s[88:89]\n"
    "v_cvt_pk_bf16_f32 v80, v80, v81\n"
    "v_cvt_pk_bf16_f32 v81, v82, v83\n"
    "v_pk_mul_f32 v[96:97], v[96:97], s[88:89]\n"
    "v_pk_mul_f32 v[98:99], v[98:99], s[88:89]\n"
    "v_cvt_pk_bf16_f32 v96, v96, v97\n"
    "v_cvt_pk_bf16_f32 v97, v98, v99\n"
    "v_pk_mul_f32 v[112:113], v[112:113], s[88:89]\n"
    "v_pk_mul_f32 v[114:115], v[114:115], s[88:89]\n"
    "v_cvt_pk_bf16_f32 v112, v112, v113\n"
    "v_cvt_pk_bf16_f32 v113, v114, v115\n"
    "buffer_load_dwordx4 v234, s[24:27], s57 offen offset:1024 nt lds\n"
    "v_xor_b32 v174, 128, v168\n"
    "ds_read_b128 a[8:11], v174\n"
    "v_xor_b32 v175, 128, v168\n"
    "ds_read_b128 a[40:43], v175 offset:8192\n"
    "v_xor_b32 v172, 128, v168\n"
    "ds_read_b128 a[72:75], v172 offset:16384\n"
    "v_xor_b32 v173, 192, v168\n"
    "ds_read_b128 a[12:15], v173\n"
    "v_xor_b32 v174, 192, v168\n"
    "ds_read_b128 a[44:47], v174 offset:8192\n"
    "v_xor_b32 v175, 192, v168\n"
    "ds_read_b128 a[76:79], v175 offset:16384\n"
    "v_pk_mul_f32 v[68:69], v[68:69], s[88:89]\n"
    "v_pk_mul_f32 v[70:71], v[70:71], s[88:89]\n"
    "v_cvt_pk_bf16_f32 v68, v68, v69\n"
    "v_cvt_pk_bf16_f32 v69, v70, v71\n"
    "v_pk_mul_f32 v[84:85], v[84:85], s[88:89]\n"
    "v_pk_mul_f32 v[86:87], v[86:87], s[88:89]\n"
    "v_cvt_pk_bf16_f32 v84, v84, v85\n"
    "v_cvt_pk_bf16_f32 v85, v86, v87\n"
    "v_pk_mul_f32 v[100:101], v[100:101], s[88:89]\n"
    "v_pk_mul_f32 v[102:103], v[102:103], s[88:89]\n"
    "v_cvt_pk_bf16_f32 v100, v100, v101\n"
    "v_cvt_pk_bf16_f32 v101, v102, v103\n"
    "v_pk_mul_f32 v[116:117], v[116:117], s[88:89]\n"
    "v_pk_mul_f32 v[118:119], v[118:119], s[88:89]\n"
    "v_cvt_pk_bf16_f32 v116, v116, v117\n"
    "v_cvt_pk_bf16_f32 v117, v118, v119\n"
    "buffer_load_dwordx4 v234, s[24:27], s58 offen offset:2048 nt lds\n"
    "v_xor_b32 v172, 256, v168\n"
    "ds_read_b128 a[16:19], v172\n"
    "v_xor_b32 v173, 256, v168\n"
    "ds_read_b128 a[48:51], v173 offset:8192\n"
    "v_xor_b32 v174, 256, v168\n"
    "ds_read_b128 a[80:83], v174 offset:16384\n"
    "v_xor_b32 v175, 320, v168\n"
    "ds_read_b128 a[20:23], v175\n"
    "v_xor_b32 v172, 320, v168\n"
    "ds_read_b128 a[52:55], v172 offset:8192\n"
    "v_xor_b32 v173, 320, v168\n"
    "ds_read_b128 a[84:87], v173 offset:16384\n"
    "v_pk_mul_f32 v[72:73], v[72:73], s[88:89]\n"
    "v_pk_mul_f32 v[74:75], v[74:75], s[88:89]\n"
    "v_cvt_pk_bf16_f32 v72, v72, v73\n"
    "v_cvt_pk_bf16_f32 v73, v74, v75\n"
    "v_pk_mul_f32 v[88:89], v[88:89], s[88:89]\n"
    "v_pk_mul_f32 v[90:91], v[90:91], s[88:89]\n"
    "v_cvt_pk_bf16_f32 v88, v88, v89\n"
    "v_cvt_pk_bf16_f32 v89, v90, v91\n"
    "v_pk_mul_f32 v[104:105], v[104:105], s[88:89]\n"
    "v_pk_mul_f32 v[106:107], v[106:107], s[88:89]\n"
    "v_cvt_pk_bf16_f32 v104, v104, v105\n"
    "v_cvt_pk_bf16_f32 v105, v106, v107\n"
    "v_pk_mul_f32 v[120:121], v[120:121], s[88:89]\n"
    "v_pk_mul_f32 v[122:123], v[122:123], s[88:89]\n"
    "v_cvt_pk_bf16_f32 v120, v120, v121\n"
    "v_cvt_pk_bf16_f32 v121, v122, v123\n"
    "buffer_load_dwordx4 v234, s[24:27], s59 offen offset:3072 nt lds\n"
    "v_xor_b32 v174, 384, v168\n"
    "ds_read_b128 a[24:27], v174\n"
    "v_xor_b32 v175, 384, v168\n"
    "ds_read_b128 a[56:59], v175 offset:8192\n"
    "v_xor_b32 v172, 384, v168\n"
    "ds_read_b128 a[88:91], v172 offset:16384\n"
    "v_xor_b32 v173, 448, v168\n"
    "ds_read_b128 a[28:31], v173\n"
    "v_xor_b32 v174, 448, v168\n"
    "ds_read_b128 a[60:63], v174 offset:8192\n"
    "v_xor_b32 v175, 448, v168\n"
    "ds_read_b128 a[92:95], v175 offset:16384\n"
    "v_pk_mul_f32 v[76:77], v[76:77], s[88:89]\n"
    "v_pk_mul_f32 v[78:79], v[78:79], s[88:89]\n"
    "v_cvt_pk_bf16_f32 v76, v76, v77\n"
    "v_cvt_pk_bf16_f32 v77, v78, v79\n"
    "v_pk_mul_f32 v[92:93], v[92:93], s[88:89]\n"
    "v_pk_mul_f32 v[94:95], v[94:95], s[88:89]\n"
    "v_cvt_pk_bf16_f32 v92, v92, v93\n"
    "v_cvt_pk_bf16_f32 v93, v94, v95\n"
    "v_pk_mul_f32 v[108:109], v[108:109], s[88:89]\n"
    "v_pk_mul_f32 v[110:111], v[110:111], s[88:89]\n"
    "v_cvt_pk_bf16_f32 v108, v108, v109\n"
    "v_cvt_pk_bf16_f32 v109, v110, v111\n"
    "v_pk_mul_f32 v[124:125], v[124:125], s[88:89]\n"
    "v_pk_mul_f32 v[126:127], v[126:127], s[88:89]\n"
    "v_cvt_pk_bf16_f32 v124, v124, v125\n"
    "v_cvt_pk_bf16_f32 v125, v126, v127\n"
    "s_mov_b32 m0, s77\n"
    "s_nop 0\n"
    "buffer_load_dwordx4 v234, s[24:27], s60 offen nt lds\n"
    "s_waitcnt lgkmcnt(0)\n"
    "s_barrier\n"
    "ds_write_b64 v160, v[64:65]\n"
    "ds_write_b64 v161, v[80:81]\n"
    "ds_write_b64 v162, v[96:97]\n"
    "ds_write_b64 v163, v[112:113]\n"
    "ds_write_b64 v160, v[68:69] offset:8192\n"
    "ds_write_b64 v161, v[84:85] offset:8192\n"
    "ds_write_b64 v162, v[100:101] offset:8192\n"
    "ds_write_b64 v163, v[116:117] offset:8192\n"
    "ds_write_b64 v160, v[72:73] offset:16384\n"
    "ds_write_b64 v161, v[88:89] offset:16384\n"
    "ds_write_b64 v162, v[104:105] offset:16384\n"
    "ds_write_b64 v163, v[120:121] offset:16384\n"
    "s_mov_b64 exec, s[92:93]\n"
    "ds_write_b64 v164, v[76:77]\n"
    "s_mov_b64 exec, -1\n"
    "s_mov_b64 exec, s[92:93]\n"
    "ds_write_b64 v165, v[92:93]\n"
    "s_mov_b64 exec, -1\n"
    "s_mov_b64 exec, s[92:93]\n"
    "ds_write_b64 v166, v[108:109]\n"
    "s_mov_b64 exec, -1\n"
    "s_mov_b64 exec, s[92:93]\n"
    "ds_write_b64 v167, v[124:125]\n"
    "s_mov_b64 exec, -1\n"
    "buffer_load_dwordx4 v234, s[24:27], s61 offen offset:1024 nt lds\n"
    "buffer_load_dwordx4 v234, s[24:27], s62 offen offset:2048 nt lds\n"
    "s_waitcnt lgkmcnt(0)\n"
    "s_barrier\n"
    "v_xor_b32 v175, 0, v170\n"
    "ds_read_b128 a[96:99], v175\n"
    "v_mov_b32 v0, 0\n"
    "v_mov_b32 v1, 0\n"
    "v_mov_b32 v2, 0\n"
    "v_mov_b32 v3, 0\n"
    "v_xor_b32 v172, 0, v169\n"
    "ds_read_b128 a[128:131], v172\n"
    "v_mov_b32 v4, 0\n"
    "v_mov_b32 v5, 0\n"
    "v_mov_b32 v6, 0\n"
    "v_mov_b32 v7, 0\n"
    "v_xor_b32 v173, 0, v169\n"
    "ds_read_b128 a[160:163], v173 offset:8192\n"
    "v_mov_b32 v8, 0\n"
    "v_mov_b32 v9, 0\n"
    "v_mov_b32 v10, 0\n"
    "v_mov_b32 v11, 0\n"
    "v_xor_b32 v174, 0, v171\n"
    "ds_read_b128 a[192:195], v174\n"
    "v_mov_b32 v12, 0\n"
    "v_mov_b32 v13, 0\n"
    "v_mov_b32 v14, 0\n"
    "v_mov_b32 v15, 0\n"
    "buffer_load_dwordx4 v234, s[24:27], s63 offen offset:3072 nt lds\n"
    "v_xor_b32 v175, 64, v170\n"
    "ds_read_b128 a[100:103], v175\n"
    "v_mov_b32 v16, 0\n"
    "v_mov_b32 v17, 0\n"
    "v_mov_b32 v18, 0\n"
    "v_mov_b32 v19, 0\n"
    "v_xor_b32 v172, 64, v169\n"
    "ds_read_b128 a[132:135], v172\n"
    "v_mov_b32 v20, 0\n"
    "v_mov_b32 v21, 0\n"
    "v_mov_b32 v22, 0\n"
    "v_mov_b32 v23, 0\n"
    "v_xor_b32 v173, 64, v169\n"
    "ds_read_b128 a[164:167], v173 offset:8192\n"
    "v_mov_b32 v24, 0\n"
    "v_mov_b32 v25, 0\n"
    "v_mov_b32 v26, 0\n"
    "v_mov_b32 v27, 0\n"
    "v_xor_b32 v174, 64, v171\n"
    "ds_read_b128 a[196:199], v174\n"
    "v_mov_b32 v28, 0\n"
    "v_mov_b32 v29, 0\n"
    "v_mov_b32 v30, 0\n"
    "v_mov_b32 v31, 0\n"
    "s_mov_b32 m0, s78\n"
    "s_nop 0\n"
    "buffer_load_dwordx4 v234, s[24:27], s64 offen nt lds\n"
    "v_xor_b32 v175, 128, v170\n"
    "ds_read_b128 a[104:107], v175\n"
    "v_mov_b32 v32, 0\n"
    "v_mov_b32 v33, 0\n"
    "v_mov_b32 v34, 0\n"
    "v_mov_b32 v35, 0\n"
    "v_xor_b32 v172, 128, v169\n"
    "ds_read_b128 a[136:139], v172\n"
    "v_mov_b32 v36, 0\n"
    "v_mov_b32 v37, 0\n"
    "v_mov_b32 v38, 0\n"
    "v_mov_b32 v39, 0\n"
    "v_xor_b32 v173, 128, v169\n"
    "ds_read_b128 a[168:171], v173 offset:8192\n"
    "v_mov_b32 v40, 0\n"
    "v_mov_b32 v41, 0\n"
    "v_mov_b32 v42, 0\n"
    "v_mov_b32 v43, 0\n"
    "v_xor_b32 v174, 128, v171\n"
    "ds_read_b128 a[200:203], v174\n"
    "v_mov_b32 v44, 0\n"
    "v_mov_b32 v45, 0\n"
    "v_mov_b32 v46, 0\n"
    "v_mov_b32 v47, 0\n"
    "buffer_load_dwordx4 v234, s[24:27], s65 offen offset:1024 nt lds\n"
    "v_xor_b32 v175, 192, v170\n"
    "ds_read_b128 a[108:111], v175\n"
    "v_mov_b32 v48, 0\n"
    "v_mov_b32 v49, 0\n"
    "v_mov_b32 v50, 0\n"
    "v_mov_b32 v51, 0\n"
    "v_xor_b32 v172, 192, v169\n"
    "ds_read_b128 a[140:143], v172\n"
    "v_mov_b32 v52, 0\n"
    "v_mov_b32 v53, 0\n"
    "v_mov_b32 v54, 0\n"
    "v_mov_b32 v55, 0\n"
    "v_xor_b32 v173, 192, v169\n"
    "ds_read_b128 a[172:175], v173 offset:8192\n"
    "v_mov_b32 v56, 0\n"
    "v_mov_b32 v57, 0\n"
    "v_mov_b32 v58, 0\n"
    "v_mov_b32 v59, 0\n"
    "v_xor_b32 v174, 192, v171\n"
    "ds_read_b128 a[204:207], v174\n"
    "v_mov_b32 v60, 0\n"
    "v_mov_b32 v61, 0\n"
    "v_mov_b32 v62, 0\n"
    "v_mov_b32 v63, 0\n"
    "buffer_load_dwordx4 v234, s[24:27], s66 offen offset:2048 nt lds\n"
    "v_xor_b32 v175, 256, v170\n"
    "ds_read_b128 a[112:115], v175\n"
    "v_mov_b32 v64, 0\n"
    "v_mov_b32 v65, 0\n"
    "v_mov_b32 v66, 0\n"
    "v_mov_b32 v67, 0\n"
    "v_xor_b32 v172, 256, v169\n"
    "ds_read_b128 a[144:147], v172\n"
    "v_mov_b32 v68, 0\n"
    "v_mov_b32 v69, 0\n"
    "v_mov_b32 v70, 0\n"
    "v_mov_b32 v71, 0\n"
    "v_xor_b32 v173, 256, v169\n"
    "ds_read_b128 a[176:179], v173 offset:8192\n"
    "v_mov_b32 v72, 0\n"
    "v_mov_b32 v73, 0\n"
    "v_mov_b32 v74, 0\n"
    "v_mov_b32 v75, 0\n"
    "v_xor_b32 v174, 256, v171\n"
    "ds_read_b128 a[208:211], v174\n"
    "v_mov_b32 v76, 0\n"
    "v_mov_b32 v77, 0\n"
    "v_mov_b32 v78, 0\n"
    "v_mov_b32 v79, 0\n"
    "buffer_load_dwordx4 v234, s[24:27], s67 offen offset:3072 nt lds\n"
    "v_xor_b32 v175, 320, v170\n"
    "ds_read_b128 a[116:119], v175\n"
    "v_mov_b32 v80, 0\n"
    "v_mov_b32 v81, 0\n"
    "v_mov_b32 v82, 0\n"
    "v_mov_b32 v83, 0\n"
    "v_xor_b32 v172, 320, v169\n"
    "ds_read_b128 a[148:151], v172\n"
    "v_mov_b32 v84, 0\n"
    "v_mov_b32 v85, 0\n"
    "v_mov_b32 v86, 0\n"
    "v_mov_b32 v87, 0\n"
    "v_xor_b32 v173, 320, v169\n"
    "ds_read_b128 a[180:183], v173 offset:8192\n"
    "v_mov_b32 v88, 0\n"
    "v_mov_b32 v89, 0\n"
    "v_mov_b32 v90, 0\n"
    "v_mov_b32 v91, 0\n"
    "v_xor_b32 v174, 320, v171\n"
    "ds_read_b128 a[212:215], v174\n"
    "v_mov_b32 v92, 0\n"
    "v_mov_b32 v93, 0\n"
    "v_mov_b32 v94, 0\n"
    "v_mov_b32 v95, 0\n"
    "s_mov_b32 m0, s79\n"
    "s_nop 0\n"
    "buffer_load_dwordx4 v234, s[24:27], s68 offen nt lds\n"
    "v_xor_b32 v175, 384, v170\n"
    "ds_read_b128 a[120:123], v175\n"
    "v_mov_b32 v96, 0\n"
    "v_mov_b32 v97, 0\n"
    "v_mov_b32 v98, 0\n"
    "v_mov_b32 v99, 0\n"
    "v_xor_b32 v172, 384, v169\n"
    "ds_read_b128 a[152:155], v172\n"
    "v_mov_b32 v100, 0\n"
    "v_mov_b32 v101, 0\n"
    "v_mov_b32 v102, 0\n"
    "v_mov_b32 v103, 0\n"
    "v_xor_b32 v173, 384, v169\n"
    "ds_read_b128 a[184:187], v173 offset:8192\n"
    "v_mov_b32 v104, 0\n"
    "v_mov_b32 v105, 0\n"
    "v_mov_b32 v106, 0\n"
    "v_mov_b32 v107, 0\n"
    "v_xor_b32 v174, 384, v171\n"
    "ds_read_b128 a[216:219], v174\n"
    "v_mov_b32 v108, 0\n"
    "v_mov_b32 v109, 0\n"
    "v_mov_b32 v110, 0\n"
    "v_mov_b32 v111, 0\n"
    "buffer_load_dwordx4 v234, s[24:27], s69 offen offset:1024 nt lds\n"
    "v_xor_b32 v175, 448, v170\n"
    "ds_read_b128 a[124:127], v175\n"
    "v_xor_b32 v172, 448, v169\n"
    "ds_read_b128 a[156:159], v172\n"
    "v_xor_b32 v173, 448, v169\n"
    "ds_read_b128 a[188:191], v173 offset:8192\n"
    "v_xor_b32 v174, 448, v171\n"
    "ds_read_b128 a[220:223], v174\n"
    "buffer_load_dwordx4 v234, s[24:27], s70 offen offset:2048 nt lds\n"
    "buffer_load_dwordx4 v234, s[24:27], s71 offen offset:3072 nt lds\n"
    "s_waitcnt lgkmcnt(0)\n"
    "s_barrier\n"
    "s_waitcnt vmcnt(16)\n"
    "ds_read_b128 v[112:115], v223\n"
    "ds_read_b128 v[116:119], v224\n"
    "ds_read_b128 v[120:123], v223 offset:1024\n"
    "ds_read_b128 v[124:127], v224 offset:1024\n"
    "ds_read_b128 v[128:131], v223 offset:2048\n"
    "ds_read_b128 v[132:135], v224 offset:2048\n"
    "ds_read_b128 v[136:139], v223 offset:3072\n"
    "ds_read_b128 v[140:143], v224 offset:3072\n"
    "ds_read_b128 v[144:147], v223 offset:8192\n"
    "ds_read_b128 v[148:151], v224 offset:8192\n"
    "ds_read_b128 v[152:155], v223 offset:9216\n"
    "ds_read_b128 v[156:159], v224 offset:9216\n"
    "ds_read_b128 v[160:163], v223 offset:10240\n"
    "ds_read_b128 v[164:167], v224 offset:10240\n"
    "ds_read_b128 v[168:171], v223 offset:11264\n"
    "ds_read_b128 v[172:175], v224 offset:11264\n"
    "s_add_u32 s20, s20, 65536\n"
    "s_addc_u32 s21, s21, 0\n"
    "s_add_u32 s24, s24, 65536\n"
    "s_addc_u32 s25, s25, 0\n"
    "s_mov_b32 s37, 15\n"
    "L_loop_%=:\n"
    "s_waitcnt lgkmcnt(12)\n"
    "v_mfma_f32_16x16x32_bf16 v[176:179], v[112:115], a[0:3], 0\n"
    "v_mfma_f32_16x16x32_bf16 v[176:179], v[116:119], a[4:7], v[176:179]\n"
    "v_mfma_f32_16x16x32_bf16 v[176:179], v[120:123], a[8:11], v[176:179]\n"
    "v_mfma_f32_16x16x32_bf16 v[176:179], v[124:127], a[12:15], v[176:179]\n"
    "s_waitcnt lgkmcnt(8)\n"
    "v_mfma_f32_16x16x32_bf16 v[176:179], v[128:131], a[16:19], v[176:179]\n"
    "v_mfma_f32_16x16x32_bf16 v[176:179], v[132:135], a[20:23], v[176:179]\n"
    "v_mfma_f32_16x16x32_bf16 v[176:179], v[136:139], a[24:27], v[176:179]\n"
    "v_mfma_f32_16x16x32_bf16 v[176:179], v[140:143], a[28:31], v[176:179]\n"
    "s_waitcnt lgkmcnt(4)\n"
    "v_mfma_f32_16x16x32_bf16 v[180:183], v[144:147], a[0:3], 0\n"
    "s_mov_b32 m0, s72\n"
    "v_mfma_f32_16x16x32_bf16 v[180:183], v[148:151], a[4:7], v[180:183]\n"
    "buffer_load_dwordx4 v234, s[20:23], s40 offen nt lds\n"
    "v_mfma_f32_16x16x32_bf16 v[180:183], v[152:155], a[8:11], v[180:183]\n"
    "v_mfma_f32_16x16x32_bf16 v[180:183], v[156:159], a[12:15], v[180:183]\n"
    "s_nop 1\n"
    "v_exp_f32 v176, v176\n"
    "s_waitcnt lgkmcnt(0)\n"
    "v_mfma_f32_16x16x32_bf16 v[180:183], v[160:163], a[16:19], v[180:183]\n"
    "v_exp_f32 v177, v177\n"
    "v_mfma_f32_16x16x32_bf16 v[180:183], v[164:167], a[20:23], v[180:183]\n"
    "v_exp_f32 v178, v178\n"
    "v_mfma_f32_16x16x32_bf16 v[180:183], v[168:171], a[24:27], v[180:183]\n"
    "v_exp_f32 v179, v179\n"
    "v_mfma_f32_16x16x32_bf16 v[180:183], v[172:175], a[28:31], v[180:183]\n"
    "v_cvt_pk_bf16_f32 v192, v176, v177\n"
    "v_cvt_pk_bf16_f32 v193, v178, v179\n"
    "v_mfma_f32_16x16x32_bf16 v[184:187], v[112:115], a[32:35], 0\n"
    "ds_write_b64 v229, v[192:193]\n"
    "v_mfma_f32_16x16x32_bf16 v[184:187], v[116:119], a[36:39], v[184:187]\n"
    "buffer_load_dwordx4 v234, s[20:23], s41 offen offset:1024 nt lds\n"
    "v_mfma_f32_16x16x32_bf16 v[184:187], v[120:123], a[40:43], v[184:187]\n"
    "v_mfma_f32_16x16x32_bf16 v[184:187], v[124:127], a[44:47], v[184:187]\n"
    "v_exp_f32 v180, v180\n"
    "v_mfma_f32_16x16x32_bf16 v[184:187], v[128:131], a[48:51], v[184:187]\n"
    "v_exp_f32 v181, v181\n"
    "v_mfma_f32_16x16x32_bf16 v[184:187], v[132:135], a[52:55], v[184:187]\n"
    "v_exp_f32 v182, v182\n"
    "v_mfma_f32_16x16x32_bf16 v[184:187], v[136:139], a[56:59], v[184:187]\n"
    "v_exp_f32 v183, v183\n"
    "v_mfma_f32_16x16x32_bf16 v[184:187], v[140:143], a[60:63], v[184:187]\n"
    "v_cvt_pk_bf16_f32 v194, v180, v181\n"
    "v_cvt_pk_bf16_f32 v195, v182, v183\n"
    "v_mfma_f32_16x16x32_bf16 v[188:191], v[144:147], a[32:35], 0\n"
    "ds_write_b64 v229, v[194:195] offset:8\n"
    "v_mfma_f32_16x16x32_bf16 v[188:191], v[148:151], a[36:39], v[188:191]\n"
    "buffer_load_dwordx4 v234, s[20:23], s42 offen offset:2048 nt lds\n"
    "v_mfma_f32_16x16x32_bf16 v[188:191], v[152:155], a[40:43], v[188:191]\n"
    "buffer_load_dwordx4 v234, s[20:23], s43 offen offset:3072 nt lds\n"
    "v_mfma_f32_16x16x32_bf16 v[188:191], v[156:159], a[44:47], v[188:191]\n"
    "v_exp_f32 v184, v184\n"
    "v_mfma_f32_16x16x32_bf16 v[188:191], v[160:163], a[48:51], v[188:191]\n"
    "v_exp_f32 v185, v185\n"
    "v_mfma_f32_16x16x32_bf16 v[188:191], v[164:167], a[52:55], v[188:191]\n"
    "v_exp_f32 v186, v186\n"
    "v_mfma_f32_16x16x32_bf16 v[188:191], v[168:171], a[56:59], v[188:191]\n"
    "v_exp_f32 v187, v187\n"
    "v_mfma_f32_16x16x32_bf16 v[188:191], v[172:175], a[60:63], v[188:191]\n"
    "v_cvt_pk_bf16_f32 v196, v184, v185\n"
    "v_cvt_pk_bf16_f32 v197, v186, v187\n"
    "v_mfma_f32_16x16x32_bf16 v[176:179], v[112:115], a[64:67], 0\n"
    "ds_write_b64 v229, v[196:197] offset:4096\n"
    "s_mov_b32 m0, s73\n"
    "v_mfma_f32_16x16x32_bf16 v[176:179], v[116:119], a[68:71], v[176:179]\n"
    "buffer_load_dwordx4 v234, s[20:23], s44 offen nt lds\n"
    "v_mfma_f32_16x16x32_bf16 v[176:179], v[120:123], a[72:75], v[176:179]\n"
    "v_mfma_f32_16x16x32_bf16 v[176:179], v[124:127], a[76:79], v[176:179]\n"
    "v_exp_f32 v188, v188\n"
    "v_mfma_f32_16x16x32_bf16 v[176:179], v[128:131], a[80:83], v[176:179]\n"
    "v_exp_f32 v189, v189\n"
    "v_mfma_f32_16x16x32_bf16 v[176:179], v[132:135], a[84:87], v[176:179]\n"
    "v_exp_f32 v190, v190\n"
    "v_mfma_f32_16x16x32_bf16 v[176:179], v[136:139], a[88:91], v[176:179]\n"
    "v_exp_f32 v191, v191\n"
    "v_mfma_f32_16x16x32_bf16 v[176:179], v[140:143], a[92:95], v[176:179]\n"
    "v_cvt_pk_bf16_f32 v198, v188, v189\n"
    "v_cvt_pk_bf16_f32 v199, v190, v191\n"
    "v_mfma_f32_16x16x32_bf16 v[180:183], v[144:147], a[64:67], 0\n"
    "ds_write_b64 v229, v[198:199] offset:4104\n"
    "v_mfma_f32_16x16x32_bf16 v[180:183], v[148:151], a[68:71], v[180:183]\n"
    "buffer_load_dwordx4 v234, s[20:23], s45 offen offset:1024 nt lds\n"
    "v_mfma_f32_16x16x32_bf16 v[180:183], v[152:155], a[72:75], v[180:183]\n"
    "buffer_load_dwordx4 v234, s[20:23], s46 offen offset:2048 nt lds\n"
    "v_mfma_f32_16x16x32_bf16 v[180:183], v[156:159], a[76:79], v[180:183]\n"
    "v_exp_f32 v176, v176\n"
    "v_mfma_f32_16x16x32_bf16 v[180:183], v[160:163], a[80:83], v[180:183]\n"
    "v_exp_f32 v177, v177\n"
    "v_mfma_f32_16x16x32_bf16 v[180:183], v[164:167], a[84:87], v[180:183]\n"
    "v_exp_f32 v178, v178\n"
    "v_mfma_f32_16x16x32_bf16 v[180:183], v[168:171], a[88:91], v[180:183]\n"
    "v_exp_f32 v179, v179\n"
    "v_mfma_f32_16x16x32_bf16 v[180:183], v[172:175], a[92:95], v[180:183]\n"
    "v_cvt_pk_bf16_f32 v192, v176, v177\n"
    "v_cvt_pk_bf16_f32 v193, v178, v179\n"
    "v_mfma_f32_16x16x32_bf16 v[184:187], v[112:115], a[96:99], 0\n"
    "ds_write_b64 v229, v[192:193] offset:8192\n"
    "v_mfma_f32_16x16x32_bf16 v[184:187], v[116:119], a[100:103], v[184:187]\n"
    "buffer_load_dwordx4 v234, s[20:23], s47 offen offset:3072 nt lds\n"
    "v_mfma_f32_16x16x32_bf16 v[184:187], v[120:123], a[104:107], v[184:187]\n"
    "v_mfma_f32_16x16x32_bf16 v[184:187], v[124:127], a[108:111], v[184:187]\n"
    "v_exp_f32 v180, v180\n"
    "v_mfma_f32_16x16x32_bf16 v[184:187], v[128:131], a[112:115], v[184:187]\n"
    "v_exp_f32 v181, v181\n"
    "v_mfma_f32_16x16x32_bf16 v[184:187], v[132:135], a[116:119], v[184:187]\n"
    "v_exp_f32 v182, v182\n"
    "v_mfma_f32_16x16x32_bf16 v[184:187], v[136:139], a[120:123], v[184:187]\n"
    "v_exp_f32 v183, v183\n"
    "v_mfma_f32_16x16x32_bf16 v[184:187], v[140:143], a[124:127], v[184:187]\n"
    "v_cvt_pk_bf16_f32 v194, v180, v181\n"
    "v_cvt_pk_bf16_f32 v195, v182, v183\n"
    "v_mfma_f32_16x16x32_bf16 v[188:191], v[144:147], a[96:99], 0\n"
    "ds_write_b64 v229, v[194:195] offset:8200\n"
    "s_mov_b32 m0, s74\n"
    "v_mfma_f32_16x16x32_bf16 v[188:191], v[148:151], a[100:103], v[188:191]\n"
    "buffer_load_dwordx4 v234, s[20:23], s48 offen nt lds\n"
    "v_mfma_f32_16x16x32_bf16 v[188:191], v[152:155], a[104:107], v[188:191]\n"
    "buffer_load_dwordx4 v234, s[20:23], s49 offen offset:1024 nt lds\n"
    "v_mfma_f32_16x16x32_bf16 v[188:191], v[156:159], a[108:111], v[188:191]\n"
    "v_exp_f32 v184, v184\n"
    "v_mfma_f32_16x16x32_bf16 v[188:191], v[160:163], a[112:115], v[188:191]\n"
    "v_exp_f32 v185, v185\n"
    "v_mfma_f32_16x16x32_bf16 v[188:191], v[164:167], a[116:119], v[188:191]\n"
    "v_exp_f32 v186, v186\n"
    "v_mfma_f32_16x16x32_bf16 v[188:191], v[168:171], a[120:123], v[188:191]\n"
    "v_exp_f32 v187, v187\n"
    "v_mfma_f32_16x16x32_bf16 v[188:191], v[172:175], a[124:127], v[188:191]\n"
    "v_cvt_pk_bf16_f32 v196, v184, v185\n"
    "v_cvt_pk_bf16_f32 v197, v186, v187\n"
    "v_mfma_f32_16x16x32_bf16 v[176:179], v[112:115], a[128:131], 0\n"
    "ds_write_b64 v229, v[196:197] offset:12288\n"
    "v_mfma_f32_16x16x32_bf16 v[176:179], v[116:119], a[132:135], v[176:179]\n"
    "buffer_load_dwordx4 v234, s[20:23], s50 offen offset:2048 nt lds\n"
    "v_mfma_f32_16x16x32_bf16 v[176:179], v[120:123], a[136:139], v[176:179]\n"
    "v_mfma_f32_16x16x32_bf16 v[176:179], v[124:127], a[140:143], v[176:179]\n"
    "v_exp_f32 v188, v188\n"
    "v_mfma_f32_16x16x32_bf16 v[176:179], v[128:131], a[144:147], v[176:179]\n"
    "v_exp_f32 v189, v189\n"
    "v_mfma_f32_16x16x32_bf16 v[176:179], v[132:135], a[148:151], v[176:179]\n"
    "v_exp_f32 v190, v190\n"
    "v_mfma_f32_16x16x32_bf16 v[176:179], v[136:139], a[152:155], v[176:179]\n"
    "v_exp_f32 v191, v191\n"
    "v_mfma_f32_16x16x32_bf16 v[176:179], v[140:143], a[156:159], v[176:179]\n"
    "v_cvt_pk_bf16_f32 v198, v188, v189\n"
    "v_cvt_pk_bf16_f32 v199, v190, v191\n"
    "v_mfma_f32_16x16x32_bf16 v[180:183], v[144:147], a[128:131], 0\n"
    "ds_write_b64 v229, v[198:199] offset:12296\n"
    "v_mfma_f32_16x16x32_bf16 v[180:183], v[148:151], a[132:135], v[180:183]\n"
    "buffer_load_dwordx4 v234, s[20:23], s51 offen offset:3072 nt lds\n"
    "v_mfma_f32_16x16x32_bf16 v[180:183], v[152:155], a[136:139], v[180:183]\n"
    "v_mfma_f32_16x16x32_bf16 v[180:183], v[156:159], a[140:143], v[180:183]\n"
    "v_exp_f32 v176, v176\n"
    "v_mfma_f32_16x16x32_bf16 v[180:183], v[160:163], a[144:147], v[180:183]\n"
    "v_exp_f32 v177, v177\n"
    "v_mfma_f32_16x16x32_bf16 v[180:183], v[164:167], a[148:151], v[180:183]\n"
    "v_exp_f32 v178, v178\n"
    "v_mfma_f32_16x16x32_bf16 v[180:183], v[168:171], a[152:155], v[180:183]\n"
    "v_exp_f32 v179, v179\n"
    "v_mfma_f32_16x16x32_bf16 v[180:183], v[172:175], a[156:159], v[180:183]\n"
    "v_cvt_pk_bf16_f32 v192, v176, v177\n"
    "v_cvt_pk_bf16_f32 v193, v178, v179\n"
    "v_mfma_f32_16x16x32_bf16 v[184:187], v[112:115], a[160:163], 0\n"
    "ds_write_b64 v229, v[192:193] offset:16384\n"
    "s_mov_b32 m0, s75\n"
    "v_mfma_f32_16x16x32_bf16 v[184:187], v[116:119], a[164:167], v[184:187]\n"
    "buffer_load_dwordx4 v234, s[20:23], s52 offen nt lds\n"
    "v_mfma_f32_16x16x32_bf16 v[184:187], v[120:123], a[168:171], v[184:187]\n"
    "buffer_load_dwordx4 v234, s[20:23], s53 offen offset:1024 nt lds\n"
    "v_mfma_f32_16x16x32_bf16 v[184:187], v[124:127], a[172:175], v[184:187]\n"
    "v_exp_f32 v180, v180\n"
    "v_mfma_f32_16x16x32_bf16 v[184:187], v[128:131], a[176:179], v[184:187]\n"
    "v_exp_f32 v181, v181\n"
    "v_mfma_f32_16x16x32_bf16 v[184:187], v[132:135], a[180:183], v[184:187]\n"
    "v_exp_f32 v182, v182\n"
    "v_mfma_f32_16x16x32_bf16 v[184:187], v[136:139], a[184:187], v[184:187]\n"
    "v_exp_f32 v183, v183\n"
    "v_mfma_f32_16x16x32_bf16 v[184:187], v[140:143], a[188:191], v[184:187]\n"
    "v_cvt_pk_bf16_f32 v194, v180, v181\n"
    "v_cvt_pk_bf16_f32 v195, v182, v183\n"
    "v_mfma_f32_16x16x32_bf16 v[188:191], v[144:147], a[160:163], 0\n"
    "ds_write_b64 v229, v[194:195] offset:16392\n"
    "v_mfma_f32_16x16x32_bf16 v[188:191], v[148:151], a[164:167], v[188:191]\n"
    "buffer_load_dwordx4 v234, s[20:23], s54 offen offset:2048 nt lds\n"
    "v_mfma_f32_16x16x32_bf16 v[188:191], v[152:155], a[168:171], v[188:191]\n"
    "v_mfma_f32_16x16x32_bf16 v[188:191], v[156:159], a[172:175], v[188:191]\n"
    "v_exp_f32 v184, v184\n"
    "v_mfma_f32_16x16x32_bf16 v[188:191], v[160:163], a[176:179], v[188:191]\n"
    "v_exp_f32 v185, v185\n"
    "v_mfma_f32_16x16x32_bf16 v[188:191], v[164:167], a[180:183], v[188:191]\n"
    "v_exp_f32 v186, v186\n"
    "v_mfma_f32_16x16x32_bf16 v[188:191], v[168:171], a[184:187], v[188:191]\n"
    "v_exp_f32 v187, v187\n"
    "v_mfma_f32_16x16x32_bf16 v[188:191], v[172:175], a[188:191], v[188:191]\n"
    "v_cvt_pk_bf16_f32 v196, v184, v185\n"
    "v_cvt_pk_bf16_f32 v197, v186, v187\n"
    "v_mfma_f32_16x16x32_bf16 v[176:179], v[112:115], a[192:195], 0\n"
    "ds_write_b64 v229, v[196:197] offset:20480\n"
    "v_mfma_f32_16x16x32_bf16 v[176:179], v[116:119], a[196:199], v[176:179]\n"
    "buffer_load_dwordx4 v234, s[20:23], s55 offen offset:3072 nt lds\n"
    "v_mfma_f32_16x16x32_bf16 v[176:179], v[120:123], a[200:203], v[176:179]\n"
    "s_waitcnt vmcnt(16)\n"
    "ds_read_b64_tr_b16 v[112:113], v225\n"
    "v_mfma_f32_16x16x32_bf16 v[176:179], v[124:127], a[204:207], v[176:179]\n"
    "ds_read_b64_tr_b16 v[114:115], v225 offset:2048\n"
    "v_mfma_f32_16x16x32_bf16 v[176:179], v[128:131], a[208:211], v[176:179]\n"
    "ds_read_b64_tr_b16 v[116:117], v225 offset:4096\n"
    "v_mfma_f32_16x16x32_bf16 v[176:179], v[132:135], a[212:215], v[176:179]\n"
    "ds_read_b64_tr_b16 v[118:119], v225 offset:6144\n"
    "v_mfma_f32_16x16x32_bf16 v[176:179], v[136:139], a[216:219], v[176:179]\n"
    "ds_read_b64_tr_b16 v[120:121], v225 offset:8192\n"
    "v_mfma_f32_16x16x32_bf16 v[176:179], v[140:143], a[220:223], v[176:179]\n"
    "s_waitcnt lgkmcnt(6)\n"
    "s_barrier\n"
    "ds_read_b64_tr_b16 v[122:123], v225 offset:10240\n"
    "v_mfma_f32_16x16x32_bf16 v[180:183], v[144:147], a[192:195], 0\n"
    "ds_read_b128 v[200:203], v230\n"
    "ds_read_b128 v[212:215], v233\n"
    "v_mfma_f32_16x16x32_bf16 v[180:183], v[148:151], a[196:199], v[180:183]\n"
    "ds_read_b128 v[204:207], v231\n"
    "v_mfma_f32_16x16x32_bf16 v[180:183], v[152:155], a[200:203], v[180:183]\n"
    "ds_read_b128 v[208:211], v232\n"
    "v_mfma_f32_16x16x32_bf16 v[180:183], v[156:159], a[204:207], v[180:183]\n"
    "ds_read_b64_tr_b16 v[124:125], v225 offset:12288\n"
    "v_mfma_f32_16x16x32_bf16 v[180:183], v[160:163], a[208:211], v[180:183]\n"
    "ds_read_b64_tr_b16 v[126:127], v225 offset:14336\n"
    "v_mfma_f32_16x16x32_bf16 v[180:183], v[164:167], a[212:215], v[180:183]\n"
    "ds_read_b64_tr_b16 v[128:129], v226\n"
    "ds_read_b64_tr_b16 v[130:131], v226 offset:2048\n"
    "v_mfma_f32_16x16x32_bf16 v[180:183], v[168:171], a[216:219], v[180:183]\n"
    "ds_read_b64_tr_b16 v[132:133], v226 offset:4096\n"
    "ds_read_b64_tr_b16 v[134:135], v226 offset:6144\n"
    "v_mfma_f32_16x16x32_bf16 v[180:183], v[172:175], a[220:223], v[180:183]\n"
    "s_add_u32 s20, s20, 65536\n"
    "s_addc_u32 s21, s21, 0\n"
    "ds_read_b64_tr_b16 v[136:137], v226 offset:8192\n"
    "ds_read_b64_tr_b16 v[138:139], v226 offset:10240\n"
    "s_waitcnt lgkmcnt(11)\n"
    "v_mfma_f32_16x16x32_bf16 a[224:227], a[252:255], v[200:203], a[224:227]\n"
    "ds_read_b64_tr_b16 v[140:141], v226 offset:12288\n"
    "ds_read_b64_tr_b16 v[142:143], v226 offset:14336\n"
    "s_waitcnt lgkmcnt(8)\n"
    "v_mfma_f32_16x16x32_bf16 v[0:3], v[112:115], v[200:203], v[0:3]\n"
    "ds_read_b64_tr_b16 v[144:145], v227\n"
    "ds_read_b64_tr_b16 v[146:147], v227 offset:2048\n"
    "v_mfma_f32_16x16x32_bf16 v[0:3], v[116:119], v[204:207], v[0:3]\n"
    "ds_read_b64_tr_b16 v[148:149], v227 offset:4096\n"
    "ds_read_b64_tr_b16 v[150:151], v227 offset:6144\n"
    "v_mfma_f32_16x16x32_bf16 v[0:3], v[120:123], v[208:211], v[0:3]\n"
    "ds_read_b64_tr_b16 v[152:153], v227 offset:8192\n"
    "ds_read_b64_tr_b16 v[154:155], v227 offset:10240\n"
    "v_mfma_f32_16x16x32_bf16 v[0:3], v[124:127], v[212:215], v[0:3]\n"
    "ds_read_b64_tr_b16 v[156:157], v227 offset:12288\n"
    "ds_read_b64_tr_b16 v[158:159], v227 offset:14336\n"
    "s_waitcnt lgkmcnt(8)\n"
    "v_mfma_f32_16x16x32_bf16 v[4:7], v[128:131], v[200:203], v[4:7]\n"
    "ds_read_b64_tr_b16 v[160:161], v228\n"
    "ds_read_b64_tr_b16 v[162:163], v228 offset:2048\n"
    "v_mfma_f32_16x16x32_bf16 v[4:7], v[132:135], v[204:207], v[4:7]\n"
    "ds_read_b64_tr_b16 v[164:165], v228 offset:4096\n"
    "ds_read_b64_tr_b16 v[166:167], v228 offset:6144\n"
    "v_mfma_f32_16x16x32_bf16 v[4:7], v[136:139], v[208:211], v[4:7]\n"
    "ds_read_b64_tr_b16 v[168:169], v228 offset:8192\n"
    "ds_read_b64_tr_b16 v[170:171], v228 offset:10240\n"
    "v_mfma_f32_16x16x32_bf16 v[4:7], v[140:143], v[212:215], v[4:7]\n"
    "ds_read_b64_tr_b16 v[172:173], v228 offset:12288\n"
    "ds_read_b64_tr_b16 v[174:175], v228 offset:14336\n"
    "s_waitcnt lgkmcnt(8)\n"
    "v_mfma_f32_16x16x32_bf16 v[8:11], v[144:147], v[200:203], v[8:11]\n"
    "ds_read_b128 v[236:239], v230 offset:4096\n"
    "v_mfma_f32_16x16x32_bf16 v[8:11], v[148:151], v[204:207], v[8:11]\n"
    "ds_read_b128 v[240:243], v231 offset:4096\n"
    "v_mfma_f32_16x16x32_bf16 v[8:11], v[152:155], v[208:211], v[8:11]\n"
    "ds_read_b128 v[244:247], v232 offset:4096\n"
    "ds_read_b128 v[248:251], v233 offset:4096\n"
    "v_mfma_f32_16x16x32_bf16 v[8:11], v[156:159], v[212:215], v[8:11]\n"
    "s_waitcnt lgkmcnt(4)\n"
    "v_mfma_f32_16x16x32_bf16 v[12:15], v[160:163], v[200:203], v[12:15]\n"
    "s_mov_b32 m0, s76\n"
    "v_mfma_f32_16x16x32_bf16 v[12:15], v[164:167], v[204:207], v[12:15]\n"
    "buffer_load_dwordx4 v234, s[24:27], s56 offen nt lds\n"
    "v_mfma_f32_16x16x32_bf16 v[12:15], v[168:171], v[208:211], v[12:15]\n"
    "v_mfma_f32_16x16x32_bf16 v[12:15], v[172:175], v[212:215], v[12:15]\n"
    "s_waitcnt lgkmcnt(3)\n"
    "v_mfma_f32_16x16x32_bf16 a[228:231], a[252:255], v[236:239], a[228:231]\n"
    "v_exp_f32 v188, v188\n"
    "s_waitcnt lgkmcnt(0)\n"
    "v_mfma_f32_16x16x32_bf16 v[16:19], v[112:115], v[236:239], v[16:19]\n"
    "ds_read_b128 v[200:203], v230 offset:8192\n"
    "v_mfma_f32_16x16x32_bf16 v[16:19], v[116:119], v[240:243], v[16:19]\n"
    "ds_read_b128 v[204:207], v231 offset:8192\n"
    "v_mfma_f32_16x16x32_bf16 v[16:19], v[120:123], v[244:247], v[16:19]\n"
    "ds_read_b128 v[208:211], v232 offset:8192\n"
    "v_mfma_f32_16x16x32_bf16 v[16:19], v[124:127], v[248:251], v[16:19]\n"
    "ds_read_b128 v[212:215], v233 offset:8192\n"
    "v_mfma_f32_16x16x32_bf16 v[20:23], v[128:131], v[236:239], v[20:23]\n"
    "buffer_load_dwordx4 v234, s[24:27], s57 offen offset:1024 nt lds\n"
    "v_mfma_f32_16x16x32_bf16 v[20:23], v[132:135], v[240:243], v[20:23]\n"
    "v_exp_f32 v189, v189\n"
    "v_mfma_f32_16x16x32_bf16 v[20:23], v[136:139], v[244:247], v[20:23]\n"
    "v_exp_f32 v190, v190\n"
    "v_mfma_f32_16x16x32_bf16 v[20:23], v[140:143], v[248:251], v[20:23]\n"
    "v_exp_f32 v191, v191\n"
    "v_mfma_f32_16x16x32_bf16 v[24:27], v[144:147], v[236:239], v[24:27]\n"
    "buffer_load_dwordx4 v234, s[24:27], s58 offen offset:2048 nt lds\n"
    "v_mfma_f32_16x16x32_bf16 v[24:27], v[148:151], v[240:243], v[24:27]\n"
    "v_cvt_pk_bf16_f32 v198, v188, v189\n"
    "v_cvt_pk_bf16_f32 v199, v190, v191\n"
    "v_mfma_f32_16x16x32_bf16 v[24:27], v[152:155], v[244:247], v[24:27]\n"
    "ds_write_b64 v229, v[198:199] offset:20488\n"
    "v_mfma_f32_16x16x32_bf16 v[24:27], v[156:159], v[248:251], v[24:27]\n"
    "v_exp_f32 v176, v176\n"
    "v_mfma_f32_16x16x32_bf16 v[28:31], v[160:163], v[236:239], v[28:31]\n"
    "v_exp_f32 v177, v177\n"
    "v_mfma_f32_16x16x32_bf16 v[28:31], v[164:167], v[240:243], v[28:31]\n"
    "v_exp_f32 v178, v178\n"
    "v_mfma_f32_16x16x32_bf16 v[28:31], v[168:171], v[244:247], v[28:31]\n"
    "buffer_load_dwordx4 v234, s[24:27], s59 offen offset:3072 nt lds\n"
    "v_mfma_f32_16x16x32_bf16 v[28:31], v[172:175], v[248:251], v[28:31]\n"
    "v_exp_f32 v179, v179\n"
    "s_waitcnt lgkmcnt(4)\n"
    "v_mfma_f32_16x16x32_bf16 a[232:235], a[252:255], v[200:203], a[232:235]\n"
    "v_cvt_pk_bf16_f32 v192, v176, v177\n"
    "v_cvt_pk_bf16_f32 v193, v178, v179\n"
    "s_waitcnt lgkmcnt(1)\n"
    "v_mfma_f32_16x16x32_bf16 v[32:35], v[112:115], v[200:203], v[32:35]\n"
    "ds_read_b128 v[236:239], v230 offset:12288\n"
    "v_mfma_f32_16x16x32_bf16 v[32:35], v[116:119], v[204:207], v[32:35]\n"
    "ds_read_b128 v[240:243], v231 offset:12288\n"
    "v_mfma_f32_16x16x32_bf16 v[32:35], v[120:123], v[208:211], v[32:35]\n"
    "ds_read_b128 v[244:247], v232 offset:12288\n"
    "v_mfma_f32_16x16x32_bf16 v[32:35], v[124:127], v[212:215], v[32:35]\n"
    "ds_read_b128 v[248:251], v233 offset:12288\n"
    "s_mov_b32 m0, s77\n"
    "v_mfma_f32_16x16x32_bf16 v[36:39], v[128:131], v[200:203], v[36:39]\n"
    "buffer_load_dwordx4 v234, s[24:27], s60 offen nt lds\n"
    "v_mfma_f32_16x16x32_bf16 v[36:39], v[132:135], v[204:207], v[36:39]\n"
    "ds_write_b64 v229, v[192:193] offset:24576\n"
    "v_mfma_f32_16x16x32_bf16 v[36:39], v[136:139], v[208:211], v[36:39]\n"
    "v_exp_f32 v180, v180\n"
    "v_mfma_f32_16x16x32_bf16 v[36:39], v[140:143], v[212:215], v[36:39]\n"
    "v_exp_f32 v181, v181\n"
    "v_mfma_f32_16x16x32_bf16 v[40:43], v[144:147], v[200:203], v[40:43]\n"
    "v_exp_f32 v182, v182\n"
    "v_mfma_f32_16x16x32_bf16 v[40:43], v[148:151], v[204:207], v[40:43]\n"
    "buffer_load_dwordx4 v234, s[24:27], s61 offen offset:1024 nt lds\n"
    "v_mfma_f32_16x16x32_bf16 v[40:43], v[152:155], v[208:211], v[40:43]\n"
    "v_exp_f32 v183, v183\n"
    "v_mfma_f32_16x16x32_bf16 v[40:43], v[156:159], v[212:215], v[40:43]\n"
    "v_cvt_pk_bf16_f32 v194, v180, v181\n"
    "v_cvt_pk_bf16_f32 v195, v182, v183\n"
    "v_mfma_f32_16x16x32_bf16 v[44:47], v[160:163], v[200:203], v[44:47]\n"
    "ds_write_b64 v229, v[194:195] offset:24584\n"
    "v_mfma_f32_16x16x32_bf16 v[44:47], v[164:167], v[204:207], v[44:47]\n"
    "v_mfma_f32_16x16x32_bf16 v[44:47], v[168:171], v[208:211], v[44:47]\n"
    "v_mfma_f32_16x16x32_bf16 v[44:47], v[172:175], v[212:215], v[44:47]\n"
    "buffer_load_dwordx4 v234, s[24:27], s62 offen offset:2048 nt lds\n"
    "s_waitcnt lgkmcnt(5)\n"
    "v_mfma_f32_16x16x32_bf16 a[236:239], a[252:255], v[236:239], a[236:239]\n"
    "s_waitcnt lgkmcnt(2)\n"
    "v_mfma_f32_16x16x32_bf16 v[48:51], v[112:115], v[236:239], v[48:51]\n"
    "ds_read_b128 v[200:203], v230 offset:16384\n"
    "v_mfma_f32_16x16x32_bf16 v[48:51], v[116:119], v[240:243], v[48:51]\n"
    "ds_read_b128 v[204:207], v231 offset:16384\n"
    "v_mfma_f32_16x16x32_bf16 v[48:51], v[120:123], v[244:247], v[48:51]\n"
    "ds_read_b128 v[208:211], v232 offset:16384\n"
    "v_mfma_f32_16x16x32_bf16 v[48:51], v[124:127], v[248:251], v[48:51]\n"
    "ds_read_b128 v[212:215], v233 offset:16384\n"
    "v_mfma_f32_16x16x32_bf16 v[52:55], v[128:131], v[236:239], v[52:55]\n"
    "buffer_load_dwordx4 v234, s[24:27], s63 offen offset:3072 nt lds\n"
    "v_mfma_f32_16x16x32_bf16 v[52:55], v[132:135], v[240:243], v[52:55]\n"
    "v_mfma_f32_16x16x32_bf16 v[52:55], v[136:139], v[244:247], v[52:55]\n"
    "v_mfma_f32_16x16x32_bf16 v[52:55], v[140:143], v[248:251], v[52:55]\n"
    "v_mfma_f32_16x16x32_bf16 v[56:59], v[144:147], v[236:239], v[56:59]\n"
    "v_mfma_f32_16x16x32_bf16 v[56:59], v[148:151], v[240:243], v[56:59]\n"
    "s_mov_b32 m0, s78\n"
    "v_mfma_f32_16x16x32_bf16 v[56:59], v[152:155], v[244:247], v[56:59]\n"
    "buffer_load_dwordx4 v234, s[24:27], s64 offen nt lds\n"
    "v_mfma_f32_16x16x32_bf16 v[56:59], v[156:159], v[248:251], v[56:59]\n"
    "v_mfma_f32_16x16x32_bf16 v[60:63], v[160:163], v[236:239], v[60:63]\n"
    "v_mfma_f32_16x16x32_bf16 v[60:63], v[164:167], v[240:243], v[60:63]\n"
    "v_mfma_f32_16x16x32_bf16 v[60:63], v[168:171], v[244:247], v[60:63]\n"
    "v_mfma_f32_16x16x32_bf16 v[60:63], v[172:175], v[248:251], v[60:63]\n"
    "s_waitcnt lgkmcnt(3)\n"
    "v_mfma_f32_16x16x32_bf16 a[240:243], a[252:255], v[200:203], a[240:243]\n"
    "s_waitcnt lgkmcnt(0)\n"
    "s_barrier\n"
    "buffer_load_dwordx4 v234, s[24:27], s65 offen offset:1024 nt lds\n"
    "v_mfma_f32_16x16x32_bf16 v[64:67], v[112:115], v[200:203], v[64:67]\n"
    "ds_read_b128 v[236:239], v230 offset:20480\n"
    "v_mfma_f32_16x16x32_bf16 v[64:67], v[116:119], v[204:207], v[64:67]\n"
    "ds_read_b128 v[240:243], v231 offset:20480\n"
    "v_mfma_f32_16x16x32_bf16 v[64:67], v[120:123], v[208:211], v[64:67]\n"
    "ds_read_b128 v[244:247], v232 offset:20480\n"
    "v_mfma_f32_16x16x32_bf16 v[64:67], v[124:127], v[212:215], v[64:67]\n"
    "ds_read_b128 v[248:251], v233 offset:20480\n"
    "v_mfma_f32_16x16x32_bf16 v[68:71], v[128:131], v[200:203], v[68:71]\n"
    "v_mfma_f32_16x16x32_bf16 v[68:71], v[132:135], v[204:207], v[68:71]\n"
    "buffer_load_dwordx4 v234, s[24:27], s66 offen offset:2048 nt lds\n"
    "v_mfma_f32_16x16x32_bf16 v[68:71], v[136:139], v[208:211], v[68:71]\n"
    "v_mfma_f32_16x16x32_bf16 v[68:71], v[140:143], v[212:215], v[68:71]\n"
    "v_mfma_f32_16x16x32_bf16 v[72:75], v[144:147], v[200:203], v[72:75]\n"
    "v_mfma_f32_16x16x32_bf16 v[72:75], v[148:151], v[204:207], v[72:75]\n"
    "v_mfma_f32_16x16x32_bf16 v[72:75], v[152:155], v[208:211], v[72:75]\n"
    "v_mfma_f32_16x16x32_bf16 v[72:75], v[156:159], v[212:215], v[72:75]\n"
    "buffer_load_dwordx4 v234, s[24:27], s67 offen offset:3072 nt lds\n"
    "v_mfma_f32_16x16x32_bf16 v[76:79], v[160:163], v[200:203], v[76:79]\n"
    "v_mfma_f32_16x16x32_bf16 v[76:79], v[164:167], v[204:207], v[76:79]\n"
    "v_mfma_f32_16x16x32_bf16 v[76:79], v[168:171], v[208:211], v[76:79]\n"
    "v_mfma_f32_16x16x32_bf16 v[76:79], v[172:175], v[212:215], v[76:79]\n"
    "s_waitcnt lgkmcnt(3)\n"
    "v_mfma_f32_16x16x32_bf16 a[244:247], a[252:255], v[236:239], a[244:247]\n"
    "s_waitcnt lgkmcnt(0)\n"
    "v_mfma_f32_16x16x32_bf16 v[80:83], v[112:115], v[236:239], v[80:83]\n"
    "ds_read_b128 v[200:203], v230 offset:24576\n"
    "v_mfma_f32_16x16x32_bf16 v[80:83], v[116:119], v[240:243], v[80:83]\n"
    "ds_read_b128 v[204:207], v231 offset:24576\n"
    "v_mfma_f32_16x16x32_bf16 v[80:83], v[120:123], v[244:247], v[80:83]\n"
    "ds_read_b128 v[208:211], v232 offset:24576\n"
    "v_mfma_f32_16x16x32_bf16 v[80:83], v[124:127], v[248:251], v[80:83]\n"
    "ds_read_b128 v[212:215], v233 offset:24576\n"
    "s_mov_b32 m0, s79\n"
    "v_mfma_f32_16x16x32_bf16 v[84:87], v[128:131], v[236:239], v[84:87]\n"
    "buffer_load_dwordx4 v234, s[24:27], s68 offen nt lds\n"
    "v_mfma_f32_16x16x32_bf16 v[84:87], v[132:135], v[240:243], v[84:87]\n"
    "v_mfma_f32_16x16x32_bf16 v[84:87], v[136:139], v[244:247], v[84:87]\n"
    "buffer_load_dwordx4 v234, s[24:27], s69 offen offset:1024 nt lds\n"
    "v_mfma_f32_16x16x32_bf16 v[84:87], v[140:143], v[248:251], v[84:87]\n"
    "v_mfma_f32_16x16x32_bf16 v[88:91], v[144:147], v[236:239], v[88:91]\n"
    "v_mfma_f32_16x16x32_bf16 v[88:91], v[148:151], v[240:243], v[88:91]\n"
    "v_mfma_f32_16x16x32_bf16 v[88:91], v[152:155], v[244:247], v[88:91]\n"
    "v_mfma_f32_16x16x32_bf16 v[88:91], v[156:159], v[248:251], v[88:91]\n"
    "v_mfma_f32_16x16x32_bf16 v[92:95], v[160:163], v[236:239], v[92:95]\n"
    "buffer_load_dwordx4 v234, s[24:27], s70 offen offset:2048 nt lds\n"
    "v_mfma_f32_16x16x32_bf16 v[92:95], v[164:167], v[240:243], v[92:95]\n"
    "v_mfma_f32_16x16x32_bf16 v[92:95], v[168:171], v[244:247], v[92:95]\n"
    "v_mfma_f32_16x16x32_bf16 v[92:95], v[172:175], v[248:251], v[92:95]\n"
    "s_waitcnt lgkmcnt(3)\n"
    "v_mfma_f32_16x16x32_bf16 a[248:251], a[252:255], v[200:203], a[248:251]\n"
    "s_waitcnt lgkmcnt(0)\n"
    "v_mfma_f32_16x16x32_bf16 v[96:99], v[112:115], v[200:203], v[96:99]\n"
    "s_waitcnt vmcnt(15)\n"
    "ds_read_b128 v[112:115], v223\n"
    "v_mfma_f32_16x16x32_bf16 v[96:99], v[116:119], v[204:207], v[96:99]\n"
    "buffer_load_dwordx4 v234, s[24:27], s71 offen offset:3072 nt lds\n"
    "ds_read_b128 v[116:119], v224\n"
    "v_mfma_f32_16x16x32_bf16 v[96:99], v[120:123], v[208:211], v[96:99]\n"
    "ds_read_b128 v[120:123], v223 offset:1024\n"
    "v_mfma_f32_16x16x32_bf16 v[96:99], v[124:127], v[212:215], v[96:99]\n"
    "ds_read_b128 v[124:127], v224 offset:1024\n"
    "v_mfma_f32_16x16x32_bf16 v[100:103], v[128:131], v[200:203], v[100:103]\n"
    "ds_read_b128 v[128:131], v223 offset:2048\n"
    "v_mfma_f32_16x16x32_bf16 v[100:103], v[132:135], v[204:207], v[100:103]\n"
    "ds_read_b128 v[132:135], v224 offset:2048\n"
    "v_mfma_f32_16x16x32_bf16 v[100:103], v[136:139], v[208:211], v[100:103]\n"
    "ds_read_b128 v[136:139], v223 offset:3072\n"
    "v_mfma_f32_16x16x32_bf16 v[100:103], v[140:143], v[212:215], v[100:103]\n"
    "ds_read_b128 v[140:143], v224 offset:3072\n"
    "v_mfma_f32_16x16x32_bf16 v[104:107], v[144:147], v[200:203], v[104:107]\n"
    "ds_read_b128 v[144:147], v223 offset:8192\n"
    "v_mfma_f32_16x16x32_bf16 v[104:107], v[148:151], v[204:207], v[104:107]\n"
    "ds_read_b128 v[148:151], v224 offset:8192\n"
    "v_mfma_f32_16x16x32_bf16 v[104:107], v[152:155], v[208:211], v[104:107]\n"
    "ds_read_b128 v[152:155], v223 offset:9216\n"
    "v_mfma_f32_16x16x32_bf16 v[104:107], v[156:159], v[212:215], v[104:107]\n"
    "ds_read_b128 v[156:159], v224 offset:9216\n"
    "v_mfma_f32_16x16x32_bf16 v[108:111], v[160:163], v[200:203], v[108:111]\n"
    "ds_read_b128 v[160:163], v223 offset:10240\n"
    "v_mfma_f32_16x16x32_bf16 v[108:111], v[164:167], v[204:207], v[108:111]\n"
    "ds_read_b128 v[164:167], v224 offset:10240\n"
    "v_mfma_f32_16x16x32_bf16 v[108:111], v[168:171], v[208:211], v[108:111]\n"
    "ds_read_b128 v[168:171], v223 offset:11264\n"
    "v_mfma_f32_16x16x32_bf16 v[108:111], v[172:175], v[212:215], v[108:111]\n"
    "s_add_u32 s24, s24, 65536\n"
    "s_addc_u32 s25, s25, 0\n"
    "ds_read_b128 v[172:175], v224 offset:11264\n"
    "s_sub_u32 s37, s37, 1\n"
    "s_cmp_lg_u32 s37, 0\n"
    "s_cbranch_scc1 L_loop_%=\n"
    "s_mul_i32 s80, s36, 7168\n"
    "s_add_u32 s83, s12, s80\n"
    "s_addc_u32 s84, s13, 0\n"
    "s_sub_u32 s83, s83, 2688\n"
    "s_subb_u32 s84, s84, 0\n"
    "s_and_b32 s84, s84, 0xffff\n"
    "v_lshrrev_b32 v247, 3, v254\n"
    "v_and_b32 v248, 7, v254\n"
    "v_and_b32 v249, 2, v247\n"
    "v_xor_b32 v248, v248, v249\n"
    "s_and_b32 s80, s36, 1\n"
    "s_lshl_b32 s80, s80, 2\n"
    "v_xor_b32 v248, s80, v248\n"
    "v_lshlrev_b32 v248, 4, v248\n"
    "v_mul_u32_u24 v247, 896, v247\n"
    "v_add_u32 v235, v247, v248\n"
    "s_mov_b32 s30, 0xea80\n"
    "s_mov_b32 s31, 0x20000\n"
    "s_mov_b32 s40, 0xa80\n"
    "s_mov_b32 s41, 0x700\n"
    "s_mov_b32 s42, 0x380\n"
    "s_mov_b32 s43, 0\n"
    "s_mov_b32 s44, 0x7a80\n"
    "s_mov_b32 s45, 0x7700\n"
    "s_mov_b32 s46, 0x7380\n"
    "s_mov_b32 s47, 0x7000\n"
    "s_mov_b32 s48, 0xc80\n"
    "s_mov_b32 s49, 0x900\n"
    "s_mov_b32 s50, 0x580\n"
    "s_mov_b32 s51, 0x7c80\n"
    "s_mov_b32 s52, 0x7900\n"
    "s_mov_b32 s53, 0x7580\n"
    "s_lshl_b32 s80, s36, 12\n"
    "s_add_u32 s85, s80, 0\n"
    "s_add_u32 s86, s80, 32768\n"
    "s_add_u32 s87, s80, 65536\n"
    "s_add_u32 s80, s17, 0\n"
    "s_and_b32 s80, s80, 7\n"
    "s_lshl_b32 s81, s99, 2\n"
    "s_xor_b32 s81, s81, s80\n"
    "s_mul_i32 s72, s81, 57344\n"
    "s_lshr_b32 s81, s80, 2\n"
    "s_mul_i32 s64, s81, 28672\n"
    "s_and_b32 s80, s80, 3\n"
    "s_xor_b32 s80, s80, s81\n"
    "s_lshl_b32 s56, s80, 7\n"
    "s_add_u32 s80, s17, 1\n"
    "s_and_b32 s80, s80, 7\n"
    "s_lshl_b32 s81, s99, 2\n"
    "s_xor_b32 s81, s81, s80\n"
    "s_mul_i32 s73, s81, 57344\n"
    "s_lshr_b32 s81, s80, 2\n"
    "s_mul_i32 s65, s81, 28672\n"
    "s_and_b32 s80, s80, 3\n"
    "s_xor_b32 s80, s80, s81\n"
    "s_lshl_b32 s57, s80, 7\n"
    "s_add_u32 s80, s17, 2\n"
    "s_and_b32 s80, s80, 7\n"
    "s_lshl_b32 s81, s99, 2\n"
    "s_xor_b32 s81, s81, s80\n"
    "s_mul_i32 s74, s81, 57344\n"
    "s_lshr_b32 s81, s80, 2\n"
    "s_mul_i32 s66, s81, 28672\n"
    "s_and_b32 s80, s80, 3\n"
    "s_xor_b32 s80, s80, s81\n"
    "s_lshl_b32 s58, s80, 7\n"
    "s_add_u32 s80, s17, 3\n"
    "s_and_b32 s80, s80, 7\n"
    "s_lshl_b32 s81, s99, 2\n"
    "s_xor_b32 s81, s81, s80\n"
    "s_mul_i32 s75, s81, 57344\n"
    "s_lshr_b32 s81, s80, 2\n"
    "s_mul_i32 s67, s81, 28672\n"
    "s_and_b32 s80, s80, 3\n"
    "s_xor_b32 s80, s80, s81\n"
    "s_lshl_b32 s59, s80, 7\n"
    "s_add_u32 s80, s17, 4\n"
    "s_and_b32 s80, s80, 7\n"
    "s_lshl_b32 s81, s99, 2\n"
    "s_xor_b32 s81, s81, s80\n"
    "s_mul_i32 s76, s81, 57344\n"
    "s_lshr_b32 s81, s80, 2\n"
    "s_mul_i32 s68, s81, 28672\n"
    "s_and_b32 s80, s80, 3\n"
    "s_xor_b32 s80, s80, s81\n"
    "s_lshl_b32 s60, s80, 7\n"
    "s_add_u32 s80, s17, 5\n"
    "s_and_b32 s80, s80, 7\n"
    "s_lshl_b32 s81, s99, 2\n"
    "s_xor_b32 s81, s81, s80\n"
    "s_mul_i32 s77, s81, 57344\n"
    "s_lshr_b32 s81, s80, 2\n"
    "s_mul_i32 s69, s81, 28672\n"
    "s_and_b32 s80, s80, 3\n"
    "s_xor_b32 s80, s80, s81\n"
    "s_lshl_b32 s61, s80, 7\n"
    "s_add_u32 s80, s17, 6\n"
    "s_and_b32 s80, s80, 7\n"
    "s_lshl_b32 s81, s99, 2\n"
    "s_xor_b32 s81, s81, s80\n"
    "s_mul_i32 s78, s81, 57344\n"
    "s_lshr_b32 s81, s80, 2\n"
    "s_mul_i32 s70, s81, 28672\n"
    "s_and_b32 s80, s80, 3\n"
    "s_xor_b32 s80, s80, s81\n"
    "s_lshl_b32 s62, s80, 7\n"
    "s_add_u32 s80, s17, 7\n"
    "s_and_b32 s80, s80, 7\n"
    "s_lshl_b32 s81, s99, 2\n"
    "s_xor_b32 s81, s81, s80\n"
    "s_mul_i32 s79, s81, 57344\n"
    "s_lshr_b32 s81, s80, 2\n"
    "s_mul_i32 s71, s81, 28672\n"
    "s_and_b32 s80, s80, 3\n"
    "s_xor_b32 s80, s80, s81\n"
    "s_lshl_b32 s63, s80, 7\n"
    "v_and_b32 v247, 15, v254\n"
    "v_lshrrev_b32 v248, 4, v254\n"
    "v_lshlrev_b32 v249, 4, v247\n"
    "v_add_u32 v224, 159744, v249\n"
    "s_lshl_b32 s80, s36, 2\n"
    "v_add_u32 v223, s80, v224\n"
    "v_lshrrev_b32 v249, 1, v248\n"
    "s_lshl_b32 s80, s36, 3\n"
    "v_add_u32 v249, s80, v249\n"
    "v_xor_b32 v249, v249, v247\n"
    "v_lshlrev_b32 v249, 4, v249\n"
    "v_and_b32 v248, 1, v248\n"
    "v_lshlrev_b32 v248, 3, v248\n"
    "v_add_u32 v249, v249, v248\n"
    "v_lshlrev_b32 v247, 9, v247\n"
    "v_add_u32 v249, v249, v247\n"
    "v_add_u32 v234, 98304, v249\n"
    "s_waitcnt lgkmcnt(12)\n"
    "v_mfma_f32_16x16x32_bf16 v[176:179], v[112:115], a[0:3], 0\n"
    "v_mfma_f32_16x16x32_bf16 v[176:179], v[116:119], a[4:7], v[176:179]\n"
    "v_mfma_f32_16x16x32_bf16 v[176:179], v[120:123], a[8:11], v[176:179]\n"
    "v_mfma_f32_16x16x32_bf16 v[176:179], v[124:127], a[12:15], v[176:179]\n"
    "s_waitcnt lgkmcnt(8)\n"
    "v_mfma_f32_16x16x32_bf16 v[176:179], v[128:131], a[16:19], v[176:179]\n"
    "v_mfma_f32_16x16x32_bf16 v[176:179], v[132:135], a[20:23], v[176:179]\n"
    "v_mfma_f32_16x16x32_bf16 v[176:179], v[136:139], a[24:27], v[176:179]\n"
    "v_mfma_f32_16x16x32_bf16 v[176:179], v[140:143], a[28:31], v[176:179]\n"
    "s_waitcnt lgkmcnt(4)\n"
    "v_mfma_f32_16x16x32_bf16 v[180:183], v[144:147], a[0:3], 0\n"
    "v_mfma_f32_16x16x32_bf16 v[180:183], v[148:151], a[4:7], v[180:183]\n"
    "v_mfma_f32_16x16x32_bf16 v[180:183], v[152:155], a[8:11], v[180:183]\n"
    "v_mfma_f32_16x16x32_bf16 v[180:183], v[156:159], a[12:15], v[180:183]\n"
    "s_nop 3\n"
    "v_exp_f32 v176, v176\n"
    "s_waitcnt lgkmcnt(0)\n"
    "v_mfma_f32_16x16x32_bf16 v[180:183], v[160:163], a[16:19], v[180:183]\n"
    "v_exp_f32 v177, v177\n"
    "v_mfma_f32_16x16x32_bf16 v[180:183], v[164:167], a[20:23], v[180:183]\n"
    "v_exp_f32 v178, v178\n"
    "v_mfma_f32_16x16x32_bf16 v[180:183], v[168:171], a[24:27], v[180:183]\n"
    "v_exp_f32 v179, v179\n"
    "v_mfma_f32_16x16x32_bf16 v[180:183], v[172:175], a[28:31], v[180:183]\n"
    "v_cvt_pk_bf16_f32 v192, v176, v177\n"
    "v_cvt_pk_bf16_f32 v193, v178, v179\n"
    "v_mfma_f32_16x16x32_bf16 v[184:187], v[112:115], a[32:35], 0\n"
    "ds_write_b64 v229, v[192:193]\n"
    "v_mfma_f32_16x16x32_bf16 v[184:187], v[116:119], a[36:39], v[184:187]\n"
    "v_mfma_f32_16x16x32_bf16 v[184:187], v[120:123], a[40:43], v[184:187]\n"
    "v_mfma_f32_16x16x32_bf16 v[184:187], v[124:127], a[44:47], v[184:187]\n"
    "s_barrier\n"
    "v_exp_f32 v180, v180\n"
    "v_mfma_f32_16x16x32_bf16 v[184:187], v[128:131], a[48:51], v[184:187]\n"
    "v_exp_f32 v181, v181\n"
    "v_mfma_f32_16x16x32_bf16 v[184:187], v[132:135], a[52:55], v[184:187]\n"
    "v_exp_f32 v182, v182\n"
    "s_add_u32 s28, s83, s72\n"
    "s_addc_u32 s29, s84, 0\n"
    "v_mfma_f32_16x16x32_bf16 v[184:187], v[136:139], a[56:59], v[184:187]\n"
    "v_exp_f32 v183, v183\n"
    "v_mfma_f32_16x16x32_bf16 v[184:187], v[140:143], a[60:63], v[184:187]\n"
    "v_cvt_pk_bf16_f32 v194, v180, v181\n"
    "v_cvt_pk_bf16_f32 v195, v182, v183\n"
    "v_mfma_f32_16x16x32_bf16 v[188:191], v[144:147], a[32:35], 0\n"
    "ds_write_b64 v229, v[194:195] offset:8\n"
    "v_mfma_f32_16x16x32_bf16 v[188:191], v[148:151], a[36:39], v[188:191]\n"
    "v_mfma_f32_16x16x32_bf16 a[224:227], a[252:255], v[192:195], a[224:227]\n"
    "v_mfma_f32_16x16x32_bf16 v[188:191], v[152:155], a[40:43], v[188:191]\n"
    "v_mfma_f32_16x16x32_bf16 v[188:191], v[156:159], a[44:47], v[188:191]\n"
    "v_exp_f32 v184, v184\n"
    "v_mfma_f32_16x16x32_bf16 v[188:191], v[160:163], a[48:51], v[188:191]\n"
    "v_exp_f32 v185, v185\n"
    "v_mfma_f32_16x16x32_bf16 v[188:191], v[164:167], a[52:55], v[188:191]\n"
    "v_exp_f32 v186, v186\n"
    "v_mfma_f32_16x16x32_bf16 v[188:191], v[168:171], a[56:59], v[188:191]\n"
    "v_exp_f32 v187, v187\n"
    "v_mfma_f32_16x16x32_bf16 v[188:191], v[172:175], a[60:63], v[188:191]\n"
    "v_cvt_pk_bf16_f32 v196, v184, v185\n"
    "v_cvt_pk_bf16_f32 v197, v186, v187\n"
    "v_mfma_f32_16x16x32_bf16 v[176:179], v[112:115], a[64:67], 0\n"
    "ds_write_b64 v229, v[196:197] offset:4096\n"
    "s_mov_b32 m0, s85\n"
    "v_mfma_f32_16x16x32_bf16 v[176:179], v[116:119], a[68:71], v[176:179]\n"
    "buffer_load_dwordx4 v235, s[28:31], s40 offen lds\n"
    "v_mfma_f32_16x16x32_bf16 v[176:179], v[120:123], a[72:75], v[176:179]\n"
    "v_mfma_f32_16x16x32_bf16 v[176:179], v[124:127], a[76:79], v[176:179]\n"
    "v_exp_f32 v188, v188\n"
    "v_mfma_f32_16x16x32_bf16 v[176:179], v[128:131], a[80:83], v[176:179]\n"
    "v_exp_f32 v189, v189\n"
    "v_mfma_f32_16x16x32_bf16 v[176:179], v[132:135], a[84:87], v[176:179]\n"
    "v_exp_f32 v190, v190\n"
    "v_mfma_f32_16x16x32_bf16 v[176:179], v[136:139], a[88:91], v[176:179]\n"
    "v_exp_f32 v191, v191\n"
    "v_mfma_f32_16x16x32_bf16 v[176:179], v[140:143], a[92:95], v[176:179]\n"
    "v_cvt_pk_bf16_f32 v198, v188, v189\n"
    "v_cvt_pk_bf16_f32 v199, v190, v191\n"
    "v_mfma_f32_16x16x32_bf16 v[180:183], v[144:147], a[64:67], 0\n"
    "ds_write_b64 v229, v[198:199] offset:4104\n"
    "v_mfma_f32_16x16x32_bf16 v[180:183], v[148:151], a[68:71], v[180:183]\n"
    "v_mfma_f32_16x16x32_bf16 a[228:231], a[252:255], v[196:199], a[228:231]\n"
    "v_mfma_f32_16x16x32_bf16 v[180:183], v[152:155], a[72:75], v[180:183]\n"
    "buffer_load_dwordx4 v235, s[28:31], s41 offen offset:1024 lds\n"
    "v_mfma_f32_16x16x32_bf16 v[180:183], v[156:159], a[76:79], v[180:183]\n"
    "v_exp_f32 v176, v176\n"
    "v_mfma_f32_16x16x32_bf16 v[180:183], v[160:163], a[80:83], v[180:183]\n"
    "v_exp_f32 v177, v177\n"
    "v_mfma_f32_16x16x32_bf16 v[180:183], v[164:167], a[84:87], v[180:183]\n"
    "v_exp_f32 v178, v178\n"
    "v_mfma_f32_16x16x32_bf16 v[180:183], v[168:171], a[88:91], v[180:183]\n"
    "v_exp_f32 v179, v179\n"
    "v_mfma_f32_16x16x32_bf16 v[180:183], v[172:175], a[92:95], v[180:183]\n"
    "v_cvt_pk_bf16_f32 v192, v176, v177\n"
    "v_cvt_pk_bf16_f32 v193, v178, v179\n"
    "v_mfma_f32_16x16x32_bf16 v[184:187], v[112:115], a[96:99], 0\n"
    "ds_write_b64 v229, v[192:193] offset:8192\n"
    "v_mfma_f32_16x16x32_bf16 v[184:187], v[116:119], a[100:103], v[184:187]\n"
    "buffer_load_dwordx4 v235, s[28:31], s42 offen offset:2048 lds\n"
    "v_mfma_f32_16x16x32_bf16 v[184:187], v[120:123], a[104:107], v[184:187]\n"
    "v_mfma_f32_16x16x32_bf16 v[184:187], v[124:127], a[108:111], v[184:187]\n"
    "v_exp_f32 v180, v180\n"
    "v_mfma_f32_16x16x32_bf16 v[184:187], v[128:131], a[112:115], v[184:187]\n"
    "v_exp_f32 v181, v181\n"
    "v_mfma_f32_16x16x32_bf16 v[184:187], v[132:135], a[116:119], v[184:187]\n"
    "v_exp_f32 v182, v182\n"
    "v_mfma_f32_16x16x32_bf16 v[184:187], v[136:139], a[120:123], v[184:187]\n"
    "v_exp_f32 v183, v183\n"
    "v_mfma_f32_16x16x32_bf16 v[184:187], v[140:143], a[124:127], v[184:187]\n"
    "v_cvt_pk_bf16_f32 v194, v180, v181\n"
    "v_cvt_pk_bf16_f32 v195, v182, v183\n"
    "v_mfma_f32_16x16x32_bf16 v[188:191], v[144:147], a[96:99], 0\n"
    "ds_write_b64 v229, v[194:195] offset:8200\n"
    "v_mfma_f32_16x16x32_bf16 v[188:191], v[148:151], a[100:103], v[188:191]\n"
    "v_mfma_f32_16x16x32_bf16 a[232:235], a[252:255], v[192:195], a[232:235]\n"
    "v_mfma_f32_16x16x32_bf16 v[188:191], v[152:155], a[104:107], v[188:191]\n"
    "buffer_load_dwordx4 v235, s[28:31], s43 offen offset:3072 lds\n"
    "v_mfma_f32_16x16x32_bf16 v[188:191], v[156:159], a[108:111], v[188:191]\n"
    "v_exp_f32 v184, v184\n"
    "v_mfma_f32_16x16x32_bf16 v[188:191], v[160:163], a[112:115], v[188:191]\n"
    "v_exp_f32 v185, v185\n"
    "v_mfma_f32_16x16x32_bf16 v[188:191], v[164:167], a[116:119], v[188:191]\n"
    "v_exp_f32 v186, v186\n"
    "v_mfma_f32_16x16x32_bf16 v[188:191], v[168:171], a[120:123], v[188:191]\n"
    "v_exp_f32 v187, v187\n"
    "v_mfma_f32_16x16x32_bf16 v[188:191], v[172:175], a[124:127], v[188:191]\n"
    "v_cvt_pk_bf16_f32 v196, v184, v185\n"
    "v_cvt_pk_bf16_f32 v197, v186, v187\n"
    "v_mfma_f32_16x16x32_bf16 v[176:179], v[112:115], a[128:131], 0\n"
    "ds_write_b64 v229, v[196:197] offset:12288\n"
    "s_add_u32 s38, s85, 16384\n"
    "s_mov_b32 m0, s38\n"
    "v_mfma_f32_16x16x32_bf16 v[176:179], v[116:119], a[132:135], v[176:179]\n"
    "buffer_load_dwordx4 v235, s[28:31], s44 offen lds\n"
    "v_mfma_f32_16x16x32_bf16 v[176:179], v[120:123], a[136:139], v[176:179]\n"
    "v_mfma_f32_16x16x32_bf16 v[176:179], v[124:127], a[140:143], v[176:179]\n"
    "v_exp_f32 v188, v188\n"
    "v_mfma_f32_16x16x32_bf16 v[176:179], v[128:131], a[144:147], v[176:179]\n"
    "v_exp_f32 v189, v189\n"
    "v_mfma_f32_16x16x32_bf16 v[176:179], v[132:135], a[148:151], v[176:179]\n"
    "v_exp_f32 v190, v190\n"
    "v_mfma_f32_16x16x32_bf16 v[176:179], v[136:139], a[152:155], v[176:179]\n"
    "v_exp_f32 v191, v191\n"
    "v_mfma_f32_16x16x32_bf16 v[176:179], v[140:143], a[156:159], v[176:179]\n"
    "v_cvt_pk_bf16_f32 v198, v188, v189\n"
    "v_cvt_pk_bf16_f32 v199, v190, v191\n"
    "v_mfma_f32_16x16x32_bf16 v[180:183], v[144:147], a[128:131], 0\n"
    "ds_write_b64 v229, v[198:199] offset:12296\n"
    "v_mfma_f32_16x16x32_bf16 v[180:183], v[148:151], a[132:135], v[180:183]\n"
    "v_mfma_f32_16x16x32_bf16 a[236:239], a[252:255], v[196:199], a[236:239]\n"
    "v_mfma_f32_16x16x32_bf16 v[180:183], v[152:155], a[136:139], v[180:183]\n"
    "buffer_load_dwordx4 v235, s[28:31], s45 offen offset:1024 lds\n"
    "v_mfma_f32_16x16x32_bf16 v[180:183], v[156:159], a[140:143], v[180:183]\n"
    "v_exp_f32 v176, v176\n"
    "v_mfma_f32_16x16x32_bf16 v[180:183], v[160:163], a[144:147], v[180:183]\n"
    "v_exp_f32 v177, v177\n"
    "v_mfma_f32_16x16x32_bf16 v[180:183], v[164:167], a[148:151], v[180:183]\n"
    "v_exp_f32 v178, v178\n"
    "v_mfma_f32_16x16x32_bf16 v[180:183], v[168:171], a[152:155], v[180:183]\n"
    "v_exp_f32 v179, v179\n"
    "v_mfma_f32_16x16x32_bf16 v[180:183], v[172:175], a[156:159], v[180:183]\n"
    "v_cvt_pk_bf16_f32 v192, v176, v177\n"
    "v_cvt_pk_bf16_f32 v193, v178, v179\n"
    "v_mfma_f32_16x16x32_bf16 v[184:187], v[112:115], a[160:163], 0\n"
    "ds_write_b64 v229, v[192:193] offset:16384\n"
    "v_mfma_f32_16x16x32_bf16 v[184:187], v[116:119], a[164:167], v[184:187]\n"
    "buffer_load_dwordx4 v235, s[28:31], s46 offen offset:2048 lds\n"
    "v_mfma_f32_16x16x32_bf16 v[184:187], v[120:123], a[168:171], v[184:187]\n"
    "v_mfma_f32_16x16x32_bf16 v[184:187], v[124:127], a[172:175], v[184:187]\n"
    "v_exp_f32 v180, v180\n"
    "v_mfma_f32_16x16x32_bf16 v[184:187], v[128:131], a[176:179], v[184:187]\n"
    "v_exp_f32 v181, v181\n"
    "v_mfma_f32_16x16x32_bf16 v[184:187], v[132:135], a[180:183], v[184:187]\n"
    "v_exp_f32 v182, v182\n"
    "v_mfma_f32_16x16x32_bf16 v[184:187], v[136:139], a[184:187], v[184:187]\n"
    "v_exp_f32 v183, v183\n"
    "v_mfma_f32_16x16x32_bf16 v[184:187], v[140:143], a[188:191], v[184:187]\n"
    "v_cvt_pk_bf16_f32 v194, v180, v181\n"
    "v_cvt_pk_bf16_f32 v195, v182, v183\n"
    "v_mfma_f32_16x16x32_bf16 v[188:191], v[144:147], a[160:163], 0\n"
    "ds_write_b64 v229, v[194:195] offset:16392\n"
    "v_mfma_f32_16x16x32_bf16 v[188:191], v[148:151], a[164:167], v[188:191]\n"
    "v_mfma_f32_16x16x32_bf16 a[240:243], a[252:255], v[192:195], a[240:243]\n"
    "v_mfma_f32_16x16x32_bf16 v[188:191], v[152:155], a[168:171], v[188:191]\n"
    "buffer_load_dwordx4 v235, s[28:31], s47 offen offset:3072 lds\n"
    "s_add_u32 s28, s83, s73\n"
    "s_addc_u32 s29, s84, 0\n"
    "v_mfma_f32_16x16x32_bf16 v[188:191], v[156:159], a[172:175], v[188:191]\n"
    "v_exp_f32 v184, v184\n"
    "v_mfma_f32_16x16x32_bf16 v[188:191], v[160:163], a[176:179], v[188:191]\n"
    "v_exp_f32 v185, v185\n"
    "v_mfma_f32_16x16x32_bf16 v[188:191], v[164:167], a[180:183], v[188:191]\n"
    "v_exp_f32 v186, v186\n"
    "v_mfma_f32_16x16x32_bf16 v[188:191], v[168:171], a[184:187], v[188:191]\n"
    "v_exp_f32 v187, v187\n"
    "v_mfma_f32_16x16x32_bf16 v[188:191], v[172:175], a[188:191], v[188:191]\n"
    "v_cvt_pk_bf16_f32 v196, v184, v185\n"
    "v_cvt_pk_bf16_f32 v197, v186, v187\n"
    "v_mfma_f32_16x16x32_bf16 v[176:179], v[112:115], a[192:195], 0\n"
    "ds_write_b64 v229, v[196:197] offset:20480\n"
    "v_mfma_f32_16x16x32_bf16 v[176:179], v[116:119], a[196:199], v[176:179]\n"
    "s_waitcnt vmcnt(8)\n"
    "ds_read_b64_tr_b16 v[112:113], v225\n"
    "v_mfma_f32_16x16x32_bf16 v[176:179], v[120:123], a[200:203], v[176:179]\n"
    "ds_read_b64_tr_b16 v[114:115], v225 offset:2048\n"
    "v_mfma_f32_16x16x32_bf16 v[176:179], v[124:127], a[204:207], v[176:179]\n"
    "ds_read_b64_tr_b16 v[116:117], v225 offset:4096\n"
    "v_mfma_f32_16x16x32_bf16 v[176:179], v[128:131], a[208:211], v[176:179]\n"
    "ds_read_b64_tr_b16 v[118:119], v225 offset:6144\n"
    "v_mfma_f32_16x16x32_bf16 v[176:179], v[132:135], a[212:215], v[176:179]\n"
    "ds_read_b64_tr_b16 v[120:121], v225 offset:8192\n"
    "v_mfma_f32_16x16x32_bf16 v[176:179], v[136:139], a[216:219], v[176:179]\n"
    "ds_read_b64_tr_b16 v[122:123], v225 offset:10240\n"
    "v_mfma_f32_16x16x32_bf16 v[176:179], v[140:143], a[220:223], v[176:179]\n"
    "s_waitcnt lgkmcnt(7)\n"
    "s_barrier\n"
    "ds_read_b64_tr_b16 v[124:125], v225 offset:12288\n"
    "v_mfma_f32_16x16x32_bf16 v[180:183], v[144:147], a[192:195], 0\n"
    "ds_read_b128 v[200:203], v230\n"
    "ds_read_b128 v[212:215], v233\n"
    "v_mfma_f32_16x16x32_bf16 v[180:183], v[148:151], a[196:199], v[180:183]\n"
    "ds_read_b128 v[204:207], v231\n"
    "v_mfma_f32_16x16x32_bf16 v[180:183], v[152:155], a[200:203], v[180:183]\n"
    "ds_read_b128 v[208:211], v232\n"
    "v_mfma_f32_16x16x32_bf16 v[180:183], v[156:159], a[204:207], v[180:183]\n"
    "ds_read_b64_tr_b16 v[126:127], v225 offset:14336\n"
    "v_mfma_f32_16x16x32_bf16 v[180:183], v[160:163], a[208:211], v[180:183]\n"
    "ds_read_b64_tr_b16 v[128:129], v226\n"
    "ds_read_b64_tr_b16 v[130:131], v226 offset:2048\n"
    "v_mfma_f32_16x16x32_bf16 v[180:183], v[164:167], a[212:215], v[180:183]\n"
    "ds_read_b64_tr_b16 v[132:133], v226 offset:4096\n"
    "ds_read_b64_tr_b16 v[134:135], v226 offset:6144\n"
    "v_mfma_f32_16x16x32_bf16 v[180:183], v[168:171], a[216:219], v[180:183]\n"
    "ds_read_b64_tr_b16 v[136:137], v226 offset:8192\n"
    "ds_read_b64_tr_b16 v[138:139], v226 offset:10240\n"
    "v_mfma_f32_16x16x32_bf16 v[180:183], v[172:175], a[220:223], v[180:183]\n"
    "ds_read_b64_tr_b16 v[140:141], v226 offset:12288\n"
    "ds_read_b64_tr_b16 v[142:143], v226 offset:14336\n"
    "s_waitcnt lgkmcnt(8)\n"
    "v_mfma_f32_16x16x32_bf16 v[0:3], v[112:115], v[200:203], v[0:3]\n"
    "ds_read_b64_tr_b16 v[144:145], v227\n"
    "ds_read_b64_tr_b16 v[146:147], v227 offset:2048\n"
    "v_mfma_f32_16x16x32_bf16 v[0:3], v[116:119], v[204:207], v[0:3]\n"
    "ds_read_b64_tr_b16 v[148:149], v227 offset:4096\n"
    "ds_read_b64_tr_b16 v[150:151], v227 offset:6144\n"
    "v_mfma_f32_16x16x32_bf16 v[0:3], v[120:123], v[208:211], v[0:3]\n"
    "ds_read_b64_tr_b16 v[152:153], v227 offset:8192\n"
    "ds_read_b64_tr_b16 v[154:155], v227 offset:10240\n"
    "v_mfma_f32_16x16x32_bf16 v[0:3], v[124:127], v[212:215], v[0:3]\n"
    "ds_read_b64_tr_b16 v[156:157], v227 offset:12288\n"
    "ds_read_b64_tr_b16 v[158:159], v227 offset:14336\n"
    "s_waitcnt lgkmcnt(8)\n"
    "v_mfma_f32_16x16x32_bf16 v[4:7], v[128:131], v[200:203], v[4:7]\n"
    "ds_read_b64_tr_b16 v[160:161], v228\n"
    "ds_read_b64_tr_b16 v[162:163], v228 offset:2048\n"
    "v_mfma_f32_16x16x32_bf16 v[4:7], v[132:135], v[204:207], v[4:7]\n"
    "ds_read_b64_tr_b16 v[164:165], v228 offset:4096\n"
    "ds_read_b64_tr_b16 v[166:167], v228 offset:6144\n"
    "v_mfma_f32_16x16x32_bf16 v[4:7], v[136:139], v[208:211], v[4:7]\n"
    "ds_read_b64_tr_b16 v[168:169], v228 offset:8192\n"
    "ds_read_b64_tr_b16 v[170:171], v228 offset:10240\n"
    "v_mfma_f32_16x16x32_bf16 v[4:7], v[140:143], v[212:215], v[4:7]\n"
    "ds_read_b64_tr_b16 v[172:173], v228 offset:12288\n"
    "ds_read_b64_tr_b16 v[174:175], v228 offset:14336\n"
    "s_waitcnt lgkmcnt(8)\n"
    "v_mfma_f32_16x16x32_bf16 v[8:11], v[144:147], v[200:203], v[8:11]\n"
    "ds_read_b128 v[236:239], v230 offset:4096\n"
    "v_mfma_f32_16x16x32_bf16 v[8:11], v[148:151], v[204:207], v[8:11]\n"
    "ds_read_b128 v[240:243], v231 offset:4096\n"
    "v_mfma_f32_16x16x32_bf16 v[8:11], v[152:155], v[208:211], v[8:11]\n"
    "ds_read_b128 v[244:247], v232 offset:4096\n"
    "ds_read_b128 v[248:251], v233 offset:4096\n"
    "v_mfma_f32_16x16x32_bf16 v[8:11], v[156:159], v[212:215], v[8:11]\n"
    "v_exp_f32 v188, v188\n"
    "s_waitcnt lgkmcnt(4)\n"
    "v_mfma_f32_16x16x32_bf16 v[12:15], v[160:163], v[200:203], v[12:15]\n"
    "v_exp_f32 v189, v189\n"
    "v_mfma_f32_16x16x32_bf16 v[12:15], v[164:167], v[204:207], v[12:15]\n"
    "v_exp_f32 v190, v190\n"
    "v_mfma_f32_16x16x32_bf16 v[12:15], v[168:171], v[208:211], v[12:15]\n"
    "v_exp_f32 v191, v191\n"
    "v_mfma_f32_16x16x32_bf16 v[12:15], v[172:175], v[212:215], v[12:15]\n"
    "v_cvt_pk_bf16_f32 v198, v188, v189\n"
    "v_cvt_pk_bf16_f32 v199, v190, v191\n"
    "s_waitcnt lgkmcnt(0)\n"
    "v_mfma_f32_16x16x32_bf16 v[16:19], v[112:115], v[236:239], v[16:19]\n"
    "ds_read_b128 v[200:203], v230 offset:8192\n"
    "v_mfma_f32_16x16x32_bf16 v[16:19], v[116:119], v[240:243], v[16:19]\n"
    "ds_read_b128 v[204:207], v231 offset:8192\n"
    "v_mfma_f32_16x16x32_bf16 v[16:19], v[120:123], v[244:247], v[16:19]\n"
    "ds_read_b128 v[208:211], v232 offset:8192\n"
    "v_mfma_f32_16x16x32_bf16 v[16:19], v[124:127], v[248:251], v[16:19]\n"
    "ds_read_b128 v[212:215], v233 offset:8192\n"
    "v_mfma_f32_16x16x32_bf16 v[20:23], v[128:131], v[236:239], v[20:23]\n"
    "ds_write_b64 v229, v[198:199] offset:20488\n"
    "v_mfma_f32_16x16x32_bf16 v[20:23], v[132:135], v[240:243], v[20:23]\n"
    "v_mfma_f32_16x16x32_bf16 a[244:247], a[252:255], v[196:199], a[244:247]\n"
    "v_mfma_f32_16x16x32_bf16 v[20:23], v[136:139], v[244:247], v[20:23]\n"
    "v_exp_f32 v176, v176\n"
    "v_mfma_f32_16x16x32_bf16 v[20:23], v[140:143], v[248:251], v[20:23]\n"
    "v_exp_f32 v177, v177\n"
    "v_mfma_f32_16x16x32_bf16 v[24:27], v[144:147], v[236:239], v[24:27]\n"
    "v_exp_f32 v178, v178\n"
    "v_mfma_f32_16x16x32_bf16 v[24:27], v[148:151], v[240:243], v[24:27]\n"
    "v_exp_f32 v179, v179\n"
    "v_mfma_f32_16x16x32_bf16 v[24:27], v[152:155], v[244:247], v[24:27]\n"
    "v_cvt_pk_bf16_f32 v192, v176, v177\n"
    "v_cvt_pk_bf16_f32 v193, v178, v179\n"
    "v_mfma_f32_16x16x32_bf16 v[24:27], v[156:159], v[248:251], v[24:27]\n"
    "ds_write_b64 v229, v[192:193] offset:24576\n"
    "v_mfma_f32_16x16x32_bf16 v[28:31], v[160:163], v[236:239], v[28:31]\n"
    "v_exp_f32 v180, v180\n"
    "v_mfma_f32_16x16x32_bf16 v[28:31], v[164:167], v[240:243], v[28:31]\n"
    "v_exp_f32 v181, v181\n"
    "v_mfma_f32_16x16x32_bf16 v[28:31], v[168:171], v[244:247], v[28:31]\n"
    "v_exp_f32 v182, v182\n"
    "v_mfma_f32_16x16x32_bf16 v[28:31], v[172:175], v[248:251], v[28:31]\n"
    "v_exp_f32 v183, v183\n"
    "s_waitcnt lgkmcnt(2)\n"
    "v_mfma_f32_16x16x32_bf16 v[32:35], v[112:115], v[200:203], v[32:35]\n"
    "ds_read_b128 v[236:239], v230 offset:12288\n"
    "v_cvt_pk_bf16_f32 v194, v180, v181\n"
    "v_mfma_f32_16x16x32_bf16 v[32:35], v[116:119], v[204:207], v[32:35]\n"
    "ds_read_b128 v[240:243], v231 offset:12288\n"
    "v_cvt_pk_bf16_f32 v195, v182, v183\n"
    "v_mfma_f32_16x16x32_bf16 v[32:35], v[120:123], v[208:211], v[32:35]\n"
    "ds_read_b128 v[244:247], v232 offset:12288\n"
    "v_xor_b32 v225, 0, v234\n"
    "v_mfma_f32_16x16x32_bf16 v[32:35], v[124:127], v[212:215], v[32:35]\n"
    "ds_read_b128 v[248:251], v233 offset:12288\n"
    "v_xor_b32 v226, 32, v234\n"
    "v_mfma_f32_16x16x32_bf16 v[36:39], v[128:131], v[200:203], v[36:39]\n"
    "ds_write_b64 v229, v[194:195] offset:24584\n"
    "v_mfma_f32_16x16x32_bf16 v[36:39], v[132:135], v[204:207], v[36:39]\n"
    "v_mfma_f32_16x16x32_bf16 a[248:251], a[252:255], v[192:195], a[248:251]\n"
    "s_mov_b32 m0, s86\n"
    "v_mfma_f32_16x16x32_bf16 v[36:39], v[136:139], v[208:211], v[36:39]\n"
    "buffer_load_dwordx4 v235, s[28:31], s40 offen lds\n"
    "v_mfma_f32_16x16x32_bf16 v[36:39], v[140:143], v[212:215], v[36:39]\n"
    "v_xor_b32 v227, 64, v234\n"
    "v_xor_b32 v228, 96, v234\n"
    "v_mfma_f32_16x16x32_bf16 v[40:43], v[144:147], v[200:203], v[40:43]\n"
    "v_mfma_f32_16x16x32_bf16 v[40:43], v[148:151], v[204:207], v[40:43]\n"
    "v_accvgpr_read_b32 v216, a224\n"
    "ds_write_b32 v223, v216\n"
    "v_mfma_f32_16x16x32_bf16 v[40:43], v[152:155], v[208:211], v[40:43]\n"
    "v_accvgpr_read_b32 v217, a228\n"
    "ds_write_b32 v223, v217 offset:256\n"
    "v_mfma_f32_16x16x32_bf16 v[40:43], v[156:159], v[212:215], v[40:43]\n"
    "v_accvgpr_read_b32 v218, a232\n"
    "ds_write_b32 v223, v218 offset:512\n"
    "v_mfma_f32_16x16x32_bf16 v[44:47], v[160:163], v[200:203], v[44:47]\n"
    "buffer_load_dwordx4 v235, s[28:31], s41 offen offset:1024 lds\n"
    "v_mfma_f32_16x16x32_bf16 v[44:47], v[164:167], v[204:207], v[44:47]\n"
    "v_accvgpr_read_b32 v219, a236\n"
    "ds_write_b32 v223, v219 offset:768\n"
    "v_mfma_f32_16x16x32_bf16 v[44:47], v[168:171], v[208:211], v[44:47]\n"
    "v_accvgpr_read_b32 v220, a240\n"
    "ds_write_b32 v223, v220 offset:1024\n"
    "v_mfma_f32_16x16x32_bf16 v[44:47], v[172:175], v[212:215], v[44:47]\n"
    "v_accvgpr_read_b32 v221, a244\n"
    "ds_write_b32 v223, v221 offset:1280\n"
    "s_waitcnt lgkmcnt(7)\n"
    "v_mfma_f32_16x16x32_bf16 v[48:51], v[112:115], v[236:239], v[48:51]\n"
    "ds_read_b128 v[200:203], v230 offset:16384\n"
    "v_accvgpr_read_b32 v222, a248\n"
    "v_mfma_f32_16x16x32_bf16 v[48:51], v[116:119], v[240:243], v[48:51]\n"
    "ds_read_b128 v[204:207], v231 offset:16384\n"
    "v_mfma_f32_16x16x32_bf16 v[48:51], v[120:123], v[244:247], v[48:51]\n"
    "ds_read_b128 v[208:211], v232 offset:16384\n"
    "v_mfma_f32_16x16x32_bf16 v[48:51], v[124:127], v[248:251], v[48:51]\n"
    "ds_read_b128 v[212:215], v233 offset:16384\n"
    "v_mfma_f32_16x16x32_bf16 v[52:55], v[128:131], v[236:239], v[52:55]\n"
    "buffer_load_dwordx4 v235, s[28:31], s42 offen offset:2048 lds\n"
    "v_mfma_f32_16x16x32_bf16 v[52:55], v[132:135], v[240:243], v[52:55]\n"
    "ds_write_b32 v223, v222 offset:1536\n"
    "v_mfma_f32_16x16x32_bf16 v[52:55], v[136:139], v[244:247], v[52:55]\n"
    "v_mfma_f32_16x16x32_bf16 v[52:55], v[140:143], v[248:251], v[52:55]\n"
    "v_mfma_f32_16x16x32_bf16 v[56:59], v[144:147], v[236:239], v[56:59]\n"
    "v_mfma_f32_16x16x32_bf16 v[56:59], v[148:151], v[240:243], v[56:59]\n"
    "s_waitcnt lgkmcnt(0)\n"
    "s_barrier\n"
    "v_mfma_f32_16x16x32_bf16 v[56:59], v[152:155], v[244:247], v[56:59]\n"
    "buffer_load_dwordx4 v235, s[28:31], s43 offen offset:3072 lds\n"
    "v_mfma_f32_16x16x32_bf16 v[56:59], v[156:159], v[248:251], v[56:59]\n"
    "ds_read_b128 v[176:179], v224\n"
    "v_mfma_f32_16x16x32_bf16 v[60:63], v[160:163], v[236:239], v[60:63]\n"
    "ds_read_b128 v[180:183], v224 offset:256\n"
    "v_mfma_f32_16x16x32_bf16 v[60:63], v[164:167], v[240:243], v[60:63]\n"
    "ds_read_b128 v[184:187], v224 offset:512\n"
    "v_mfma_f32_16x16x32_bf16 v[60:63], v[168:171], v[244:247], v[60:63]\n"
    "ds_read_b128 v[188:191], v224 offset:768\n"
    "v_mfma_f32_16x16x32_bf16 v[60:63], v[172:175], v[248:251], v[60:63]\n"
    "s_add_u32 s38, s86, 16384\n"
    "s_mov_b32 m0, s38\n"
    "ds_read_b128 v[192:195], v224 offset:1024\n"
    "v_mfma_f32_16x16x32_bf16 v[64:67], v[112:115], v[200:203], v[64:67]\n"
    "s_barrier\n"
    "buffer_load_dwordx4 v235, s[28:31], s44 offen lds\n"
    "v_mfma_f32_16x16x32_bf16 v[64:67], v[116:119], v[204:207], v[64:67]\n"
    "ds_read_b128 v[236:239], v230 offset:20480\n"
    "s_waitcnt lgkmcnt(5)\n"
    "v_add_f32 v176, v176, v177\n"
    "v_mfma_f32_16x16x32_bf16 v[64:67], v[120:123], v[208:211], v[64:67]\n"
    "ds_read_b128 v[240:243], v231 offset:20480\n"
    "v_add_f32 v178, v178, v179\n"
    "v_mfma_f32_16x16x32_bf16 v[64:67], v[124:127], v[212:215], v[64:67]\n"
    "ds_read_b128 v[244:247], v232 offset:20480\n"
    "v_add_f32 v216, v176, v178\n"
    "v_mfma_f32_16x16x32_bf16 v[68:71], v[128:131], v[200:203], v[68:71]\n"
    "ds_read_b128 v[248:251], v233 offset:20480\n"
    "s_waitcnt lgkmcnt(7)\n"
    "v_add_f32 v180, v180, v181\n"
    "v_mfma_f32_16x16x32_bf16 v[68:71], v[132:135], v[204:207], v[68:71]\n"
    "v_rcp_f32 v216, v216\n"
    "v_mfma_f32_16x16x32_bf16 v[68:71], v[136:139], v[208:211], v[68:71]\n"
    "buffer_load_dwordx4 v235, s[28:31], s45 offen offset:1024 lds\n"
    "v_mfma_f32_16x16x32_bf16 v[68:71], v[140:143], v[212:215], v[68:71]\n"
    "v_add_f32 v182, v182, v183\n"
    "v_add_f32 v217, v180, v182\n"
    "v_mfma_f32_16x16x32_bf16 v[72:75], v[144:147], v[200:203], v[72:75]\n"
    "v_rcp_f32 v217, v217\n"
    "v_mfma_f32_16x16x32_bf16 v[72:75], v[148:151], v[204:207], v[72:75]\n"
    "s_waitcnt lgkmcnt(6)\n"
    "v_add_f32 v184, v184, v185\n"
    "v_add_f32 v186, v186, v187\n"
    "v_mfma_f32_16x16x32_bf16 v[72:75], v[152:155], v[208:211], v[72:75]\n"
    "v_add_f32 v218, v184, v186\n"
    "s_waitcnt lgkmcnt(5)\n"
    "v_add_f32 v188, v188, v189\n"
    "v_mfma_f32_16x16x32_bf16 v[72:75], v[156:159], v[212:215], v[72:75]\n"
    "v_rcp_f32 v218, v218\n"
    "v_mfma_f32_16x16x32_bf16 v[76:79], v[160:163], v[200:203], v[76:79]\n"
    "buffer_load_dwordx4 v235, s[28:31], s46 offen offset:2048 lds\n"
    "v_mfma_f32_16x16x32_bf16 v[76:79], v[164:167], v[204:207], v[76:79]\n"
    "v_add_f32 v190, v190, v191\n"
    "v_add_f32 v219, v188, v190\n"
    "v_mfma_f32_16x16x32_bf16 v[76:79], v[168:171], v[208:211], v[76:79]\n"
    "v_rcp_f32 v219, v219\n"
    "v_mfma_f32_16x16x32_bf16 v[76:79], v[172:175], v[212:215], v[76:79]\n"
    "s_waitcnt lgkmcnt(4)\n"
    "v_add_f32 v192, v192, v193\n"
    "v_add_f32 v194, v194, v195\n"
    "s_waitcnt lgkmcnt(0)\n"
    "v_mfma_f32_16x16x32_bf16 v[80:83], v[112:115], v[236:239], v[80:83]\n"
    "ds_read_b128 v[200:203], v230 offset:24576\n"
    "v_add_f32 v220, v192, v194\n"
    "v_mfma_f32_16x16x32_bf16 v[80:83], v[116:119], v[240:243], v[80:83]\n"
    "ds_read_b128 v[204:207], v231 offset:24576\n"
    "v_mfma_f32_16x16x32_bf16 v[80:83], v[120:123], v[244:247], v[80:83]\n"
    "ds_read_b128 v[208:211], v232 offset:24576\n"
    "v_mfma_f32_16x16x32_bf16 v[80:83], v[124:127], v[248:251], v[80:83]\n"
    "ds_read_b128 v[212:215], v233 offset:24576\n"
    "v_mfma_f32_16x16x32_bf16 v[84:87], v[128:131], v[236:239], v[84:87]\n"
    "buffer_load_dwordx4 v235, s[28:31], s47 offen offset:3072 lds\n"
    "s_add_u32 s28, s83, s74\n"
    "s_addc_u32 s29, s84, 0\n"
    "v_mfma_f32_16x16x32_bf16 v[84:87], v[132:135], v[240:243], v[84:87]\n"
    "v_rcp_f32 v220, v220\n"
    "v_mfma_f32_16x16x32_bf16 v[84:87], v[136:139], v[244:247], v[84:87]\n"
    "ds_read_b128 v[196:199], v224 offset:1280\n"
    "v_mfma_f32_16x16x32_bf16 v[84:87], v[140:143], v[248:251], v[84:87]\n"
    "s_mov_b32 m0, s87\n"
    "ds_read_b128 v[176:179], v224 offset:1536\n"
    "v_mfma_f32_16x16x32_bf16 v[88:91], v[144:147], v[236:239], v[88:91]\n"
    "buffer_load_dwordx4 v235, s[28:31], s40 offen lds\n"
    "v_mfma_f32_16x16x32_bf16 v[88:91], v[148:151], v[240:243], v[88:91]\n"
    "v_mfma_f32_16x16x32_bf16 v[88:91], v[152:155], v[244:247], v[88:91]\n"
    "v_mfma_f32_16x16x32_bf16 v[88:91], v[156:159], v[248:251], v[88:91]\n"
    "s_waitcnt lgkmcnt(1)\n"
    "v_add_f32 v196, v196, v197\n"
    "v_add_f32 v198, v198, v199\n"
    "v_mfma_f32_16x16x32_bf16 v[92:95], v[160:163], v[236:239], v[92:95]\n"
    "buffer_load_dwordx4 v235, s[28:31], s41 offen offset:1024 lds\n"
    "v_mfma_f32_16x16x32_bf16 v[92:95], v[164:167], v[240:243], v[92:95]\n"
    "v_add_f32 v221, v196, v198\n"
    "s_waitcnt lgkmcnt(0)\n"
    "v_add_f32 v176, v176, v177\n"
    "v_mfma_f32_16x16x32_bf16 v[92:95], v[168:171], v[244:247], v[92:95]\n"
    "v_rcp_f32 v221, v221\n"
    "v_mfma_f32_16x16x32_bf16 v[92:95], v[172:175], v[248:251], v[92:95]\n"
    "v_add_f32 v178, v178, v179\n"
    "v_add_f32 v222, v176, v178\n"
    "v_mfma_f32_16x16x32_bf16 v[96:99], v[112:115], v[200:203], v[96:99]\n"
    "buffer_load_dwordx4 v235, s[28:31], s42 offen offset:2048 lds\n"
    "v_mfma_f32_16x16x32_bf16 v[96:99], v[116:119], v[204:207], v[96:99]\n"
    "v_rcp_f32 v222, v222\n"
    "v_mfma_f32_16x16x32_bf16 v[96:99], v[120:123], v[208:211], v[96:99]\n"
    "v_mfma_f32_16x16x32_bf16 v[96:99], v[124:127], v[212:215], v[96:99]\n"
    "v_mfma_f32_16x16x32_bf16 v[100:103], v[128:131], v[200:203], v[100:103]\n"
    "buffer_load_dwordx4 v235, s[28:31], s43 offen offset:3072 lds\n"
    "v_mfma_f32_16x16x32_bf16 v[100:103], v[132:135], v[204:207], v[100:103]\n"
    "v_mfma_f32_16x16x32_bf16 v[100:103], v[136:139], v[208:211], v[100:103]\n"
    "v_mfma_f32_16x16x32_bf16 v[100:103], v[140:143], v[212:215], v[100:103]\n"
    "s_add_u32 s38, s87, 16384\n"
    "s_mov_b32 m0, s38\n"
    "v_mfma_f32_16x16x32_bf16 v[104:107], v[144:147], v[200:203], v[104:107]\n"
    "buffer_load_dwordx4 v235, s[28:31], s44 offen lds\n"
    "v_mfma_f32_16x16x32_bf16 v[104:107], v[148:151], v[204:207], v[104:107]\n"
    "v_mfma_f32_16x16x32_bf16 v[104:107], v[152:155], v[208:211], v[104:107]\n"
    "v_mfma_f32_16x16x32_bf16 v[104:107], v[156:159], v[212:215], v[104:107]\n"
    "v_mfma_f32_16x16x32_bf16 v[108:111], v[160:163], v[200:203], v[108:111]\n"
    "buffer_load_dwordx4 v235, s[28:31], s45 offen offset:1024 lds\n"
    "v_mfma_f32_16x16x32_bf16 v[108:111], v[164:167], v[204:207], v[108:111]\n"
    "v_mfma_f32_16x16x32_bf16 v[108:111], v[168:171], v[208:211], v[108:111]\n"
    "v_mfma_f32_16x16x32_bf16 v[108:111], v[172:175], v[212:215], v[108:111]\n"
    "buffer_load_dwordx4 v235, s[28:31], s46 offen offset:2048 lds\n"
    "buffer_load_dwordx4 v235, s[28:31], s47 offen offset:3072 lds\n"
    "v_min3_f32 v239, v216, v217, v218\n"
    "v_min3_f32 v239, v239, v219, v220\n"
    "v_min3_f32 v239, v239, v221, v222\n"
    "v_max3_f32 v240, v216, v217, v218\n"
    "v_max3_f32 v240, v240, v219, v220\n"
    "v_max3_f32 v240, v240, v221, v222\n"
    "v_add_f32 v243, v216, v217\n"
    "v_add_f32 v243, v243, v218\n"
    "v_add_f32 v243, v243, v219\n"
    "v_add_f32 v243, v243, v220\n"
    "v_add_f32 v243, v243, v221\n"
    "v_add_f32 v243, v243, v222\n"
    "s_mov_b32 s80, 0x2b800000\n"
    "s_mov_b32 s81, 0x53800000\n"
    "v_cmp_nle_f32 vcc, s80, v239\n"
    "s_nop 3\n"
    "s_mov_b64 s[92:93], vcc\n"
    "v_cmp_nge_f32 vcc, s81, v240\n"
    "s_nop 3\n"
    "s_or_b64 s[92:93], s[92:93], vcc\n"
    "v_cmp_u_f32 vcc, v243, v243\n"
    "s_nop 3\n"
    "s_or_b64 s[92:93], s[92:93], vcc\n"
    "s_cbranch_scc1 L_safe_%=\n"
    "L_epi_%=:\n"
    "s_barrier\n"
    "v_pk_mul_f32 v[0:1], v[0:1], v[216:217] op_sel:[0,0] op_sel_hi:[1,0]\n"
    "v_pk_mul_f32 v[2:3], v[2:3], v[216:217] op_sel:[0,0] op_sel_hi:[1,0]\n"
    "v_cvt_pk_bf16_f32 v0, v0, v1\n"
    "v_cvt_pk_bf16_f32 v1, v2, v3\n"
    "ds_write_b64 v225, v[0:1]\n"
    "v_pk_mul_f32 v[4:5], v[4:5], v[216:217] op_sel:[0,0] op_sel_hi:[1,0]\n"
    "v_pk_mul_f32 v[6:7], v[6:7], v[216:217] op_sel:[0,0] op_sel_hi:[1,0]\n"
    "v_cvt_pk_bf16_f32 v4, v4, v5\n"
    "v_cvt_pk_bf16_f32 v5, v6, v7\n"
    "ds_write_b64 v226, v[4:5]\n"
    "v_pk_mul_f32 v[8:9], v[8:9], v[216:217] op_sel:[0,0] op_sel_hi:[1,0]\n"
    "v_pk_mul_f32 v[10:11], v[10:11], v[216:217] op_sel:[0,0] op_sel_hi:[1,0]\n"
    "v_cvt_pk_bf16_f32 v8, v8, v9\n"
    "v_cvt_pk_bf16_f32 v9, v10, v11\n"
    "ds_write_b64 v227, v[8:9]\n"
    "v_pk_mul_f32 v[12:13], v[12:13], v[216:217] op_sel:[0,0] op_sel_hi:[1,0]\n"
    "v_pk_mul_f32 v[14:15], v[14:15], v[216:217] op_sel:[0,0] op_sel_hi:[1,0]\n"
    "v_cvt_pk_bf16_f32 v12, v12, v13\n"
    "v_cvt_pk_bf16_f32 v13, v14, v15\n"
    "ds_write_b64 v228, v[12:13]\n"
    "v_pk_mul_f32 v[16:17], v[16:17], v[216:217] op_sel:[0,1] op_sel_hi:[1,1]\n"
    "v_pk_mul_f32 v[18:19], v[18:19], v[216:217] op_sel:[0,1] op_sel_hi:[1,1]\n"
    "v_cvt_pk_bf16_f32 v16, v16, v17\n"
    "v_cvt_pk_bf16_f32 v17, v18, v19\n"
    "ds_write_b64 v225, v[16:17] offset:8192\n"
    "v_pk_mul_f32 v[20:21], v[20:21], v[216:217] op_sel:[0,1] op_sel_hi:[1,1]\n"
    "v_pk_mul_f32 v[22:23], v[22:23], v[216:217] op_sel:[0,1] op_sel_hi:[1,1]\n"
    "v_cvt_pk_bf16_f32 v20, v20, v21\n"
    "v_cvt_pk_bf16_f32 v21, v22, v23\n"
    "ds_write_b64 v226, v[20:21] offset:8192\n"
    "v_pk_mul_f32 v[24:25], v[24:25], v[216:217] op_sel:[0,1] op_sel_hi:[1,1]\n"
    "v_pk_mul_f32 v[26:27], v[26:27], v[216:217] op_sel:[0,1] op_sel_hi:[1,1]\n"
    "v_cvt_pk_bf16_f32 v24, v24, v25\n"
    "v_cvt_pk_bf16_f32 v25, v26, v27\n"
    "ds_write_b64 v227, v[24:25] offset:8192\n"
    "v_pk_mul_f32 v[28:29], v[28:29], v[216:217] op_sel:[0,1] op_sel_hi:[1,1]\n"
    "v_pk_mul_f32 v[30:31], v[30:31], v[216:217] op_sel:[0,1] op_sel_hi:[1,1]\n"
    "v_cvt_pk_bf16_f32 v28, v28, v29\n"
    "v_cvt_pk_bf16_f32 v29, v30, v31\n"
    "ds_write_b64 v228, v[28:29] offset:8192\n"
    "v_pk_mul_f32 v[32:33], v[32:33], v[218:219] op_sel:[0,0] op_sel_hi:[1,0]\n"
    "v_pk_mul_f32 v[34:35], v[34:35], v[218:219] op_sel:[0,0] op_sel_hi:[1,0]\n"
    "v_cvt_pk_bf16_f32 v32, v32, v33\n"
    "v_cvt_pk_bf16_f32 v33, v34, v35\n"
    "ds_write_b64 v225, v[32:33] offset:16384\n"
    "v_pk_mul_f32 v[36:37], v[36:37], v[218:219] op_sel:[0,0] op_sel_hi:[1,0]\n"
    "v_pk_mul_f32 v[38:39], v[38:39], v[218:219] op_sel:[0,0] op_sel_hi:[1,0]\n"
    "v_cvt_pk_bf16_f32 v36, v36, v37\n"
    "v_cvt_pk_bf16_f32 v37, v38, v39\n"
    "ds_write_b64 v226, v[36:37] offset:16384\n"
    "v_pk_mul_f32 v[40:41], v[40:41], v[218:219] op_sel:[0,0] op_sel_hi:[1,0]\n"
    "v_pk_mul_f32 v[42:43], v[42:43], v[218:219] op_sel:[0,0] op_sel_hi:[1,0]\n"
    "v_cvt_pk_bf16_f32 v40, v40, v41\n"
    "v_cvt_pk_bf16_f32 v41, v42, v43\n"
    "ds_write_b64 v227, v[40:41] offset:16384\n"
    "v_pk_mul_f32 v[44:45], v[44:45], v[218:219] op_sel:[0,0] op_sel_hi:[1,0]\n"
    "v_pk_mul_f32 v[46:47], v[46:47], v[218:219] op_sel:[0,0] op_sel_hi:[1,0]\n"
    "v_cvt_pk_bf16_f32 v44, v44, v45\n"
    "v_cvt_pk_bf16_f32 v45, v46, v47\n"
    "ds_write_b64 v228, v[44:45] offset:16384\n"
    "v_pk_mul_f32 v[48:49], v[48:49], v[218:219] op_sel:[0,1] op_sel_hi:[1,1]\n"
    "v_pk_mul_f32 v[50:51], v[50:51], v[218:219] op_sel:[0,1] op_sel_hi:[1,1]\n"
    "v_cvt_pk_bf16_f32 v48, v48, v49\n"
    "v_cvt_pk_bf16_f32 v49, v50, v51\n"
    "ds_write_b64 v225, v[48:49] offset:24576\n"
    "v_pk_mul_f32 v[52:53], v[52:53], v[218:219] op_sel:[0,1] op_sel_hi:[1,1]\n"
    "v_pk_mul_f32 v[54:55], v[54:55], v[218:219] op_sel:[0,1] op_sel_hi:[1,1]\n"
    "v_cvt_pk_bf16_f32 v52, v52, v53\n"
    "v_cvt_pk_bf16_f32 v53, v54, v55\n"
    "ds_write_b64 v226, v[52:53] offset:24576\n"
    "v_pk_mul_f32 v[56:57], v[56:57], v[218:219] op_sel:[0,1] op_sel_hi:[1,1]\n"
    "v_pk_mul_f32 v[58:59], v[58:59], v[218:219] op_sel:[0,1] op_sel_hi:[1,1]\n"
    "v_cvt_pk_bf16_f32 v56, v56, v57\n"
    "v_cvt_pk_bf16_f32 v57, v58, v59\n"
    "ds_write_b64 v227, v[56:57] offset:24576\n"
    "v_pk_mul_f32 v[60:61], v[60:61], v[218:219] op_sel:[0,1] op_sel_hi:[1,1]\n"
    "v_pk_mul_f32 v[62:63], v[62:63], v[218:219] op_sel:[0,1] op_sel_hi:[1,1]\n"
    "v_cvt_pk_bf16_f32 v60, v60, v61\n"
    "v_cvt_pk_bf16_f32 v61, v62, v63\n"
    "ds_write_b64 v228, v[60:61] offset:24576\n"
    "v_pk_mul_f32 v[64:65], v[64:65], v[220:221] op_sel:[0,0] op_sel_hi:[1,0]\n"
    "v_pk_mul_f32 v[66:67], v[66:67], v[220:221] op_sel:[0,0] op_sel_hi:[1,0]\n"
    "v_cvt_pk_bf16_f32 v64, v64, v65\n"
    "v_cvt_pk_bf16_f32 v65, v66, v67\n"
    "ds_write_b64 v225, v[64:65] offset:32768\n"
    "v_pk_mul_f32 v[68:69], v[68:69], v[220:221] op_sel:[0,0] op_sel_hi:[1,0]\n"
    "v_pk_mul_f32 v[70:71], v[70:71], v[220:221] op_sel:[0,0] op_sel_hi:[1,0]\n"
    "v_cvt_pk_bf16_f32 v68, v68, v69\n"
    "v_cvt_pk_bf16_f32 v69, v70, v71\n"
    "ds_write_b64 v226, v[68:69] offset:32768\n"
    "v_pk_mul_f32 v[72:73], v[72:73], v[220:221] op_sel:[0,0] op_sel_hi:[1,0]\n"
    "v_pk_mul_f32 v[74:75], v[74:75], v[220:221] op_sel:[0,0] op_sel_hi:[1,0]\n"
    "v_cvt_pk_bf16_f32 v72, v72, v73\n"
    "v_cvt_pk_bf16_f32 v73, v74, v75\n"
    "ds_write_b64 v227, v[72:73] offset:32768\n"
    "v_pk_mul_f32 v[76:77], v[76:77], v[220:221] op_sel:[0,0] op_sel_hi:[1,0]\n"
    "v_pk_mul_f32 v[78:79], v[78:79], v[220:221] op_sel:[0,0] op_sel_hi:[1,0]\n"
    "v_cvt_pk_bf16_f32 v76, v76, v77\n"
    "v_cvt_pk_bf16_f32 v77, v78, v79\n"
    "ds_write_b64 v228, v[76:77] offset:32768\n"
    "v_pk_mul_f32 v[80:81], v[80:81], v[220:221] op_sel:[0,1] op_sel_hi:[1,1]\n"
    "v_pk_mul_f32 v[82:83], v[82:83], v[220:221] op_sel:[0,1] op_sel_hi:[1,1]\n"
    "v_cvt_pk_bf16_f32 v80, v80, v81\n"
    "v_cvt_pk_bf16_f32 v81, v82, v83\n"
    "ds_write_b64 v225, v[80:81] offset:40960\n"
    "v_pk_mul_f32 v[84:85], v[84:85], v[220:221] op_sel:[0,1] op_sel_hi:[1,1]\n"
    "v_pk_mul_f32 v[86:87], v[86:87], v[220:221] op_sel:[0,1] op_sel_hi:[1,1]\n"
    "v_cvt_pk_bf16_f32 v84, v84, v85\n"
    "v_cvt_pk_bf16_f32 v85, v86, v87\n"
    "ds_write_b64 v226, v[84:85] offset:40960\n"
    "v_pk_mul_f32 v[88:89], v[88:89], v[220:221] op_sel:[0,1] op_sel_hi:[1,1]\n"
    "v_pk_mul_f32 v[90:91], v[90:91], v[220:221] op_sel:[0,1] op_sel_hi:[1,1]\n"
    "v_cvt_pk_bf16_f32 v88, v88, v89\n"
    "v_cvt_pk_bf16_f32 v89, v90, v91\n"
    "ds_write_b64 v227, v[88:89] offset:40960\n"
    "v_pk_mul_f32 v[92:93], v[92:93], v[220:221] op_sel:[0,1] op_sel_hi:[1,1]\n"
    "v_pk_mul_f32 v[94:95], v[94:95], v[220:221] op_sel:[0,1] op_sel_hi:[1,1]\n"
    "v_cvt_pk_bf16_f32 v92, v92, v93\n"
    "v_cvt_pk_bf16_f32 v93, v94, v95\n"
    "ds_write_b64 v228, v[92:93] offset:40960\n"
    "v_pk_mul_f32 v[96:97], v[96:97], v[222:223] op_sel:[0,0] op_sel_hi:[1,0]\n"
    "v_pk_mul_f32 v[98:99], v[98:99], v[222:223] op_sel:[0,0] op_sel_hi:[1,0]\n"
    "v_cvt_pk_bf16_f32 v96, v96, v97\n"
    "v_cvt_pk_bf16_f32 v97, v98, v99\n"
    "ds_write_b64 v225, v[96:97] offset:49152\n"
    "v_pk_mul_f32 v[100:101], v[100:101], v[222:223] op_sel:[0,0] op_sel_hi:[1,0]\n"
    "v_pk_mul_f32 v[102:103], v[102:103], v[222:223] op_sel:[0,0] op_sel_hi:[1,0]\n"
    "v_cvt_pk_bf16_f32 v100, v100, v101\n"
    "v_cvt_pk_bf16_f32 v101, v102, v103\n"
    "ds_write_b64 v226, v[100:101] offset:49152\n"
    "v_pk_mul_f32 v[104:105], v[104:105], v[222:223] op_sel:[0,0] op_sel_hi:[1,0]\n"
    "v_pk_mul_f32 v[106:107], v[106:107], v[222:223] op_sel:[0,0] op_sel_hi:[1,0]\n"
    "v_cvt_pk_bf16_f32 v104, v104, v105\n"
    "v_cvt_pk_bf16_f32 v105, v106, v107\n"
    "ds_write_b64 v227, v[104:105] offset:49152\n"
    "v_pk_mul_f32 v[108:109], v[108:109], v[222:223] op_sel:[0,0] op_sel_hi:[1,0]\n"
    "v_pk_mul_f32 v[110:111], v[110:111], v[222:223] op_sel:[0,0] op_sel_hi:[1,0]\n"
    "v_cvt_pk_bf16_f32 v108, v108, v109\n"
    "v_cvt_pk_bf16_f32 v109, v110, v111\n"
    "ds_write_b64 v228, v[108:109] offset:49152\n"
    "v_mov_b32 v213, v235\n"
    "v_and_b32 v244, 15, v254\n"
    "v_lshrrev_b32 v245, 4, v254\n"
    "v_and_b32 v239, 1, v245\n"
    "v_lshlrev_b32 v239, 1, v239\n"
    "v_bfe_u32 v240, v254, 3, 1\n"
    "v_add_u32 v239, v239, v240\n"
    "v_lshlrev_b32 v246, 5, v239\n"
    "v_and_b32 v239, 3, v254\n"
    "v_lshlrev_b32 v239, 3, v239\n"
    "v_add_u32 v246, v246, v239\n"
    "v_bfe_u32 v247, v254, 2, 2\n"
    "v_lshlrev_b32 v239, 12, v245\n"
    "v_lshlrev_b32 v240, 7, v247\n"
    "v_add_u32 v239, v239, v240\n"
    "s_lshl_b32 s38, s36, 5\n"
    "v_xor_b32 v240, s38, v246\n"
    "v_add_u32 v208, v239, v240\n"
    "v_add_u32 v209, 65536, v208\n"
    "v_xor_b32 v239, v245, v244\n"
    "v_lshlrev_b32 v239, 4, v239\n"
    "v_lshlrev_b32 v240, 9, v244\n"
    "v_add_u32 v239, v239, v240\n"
    "v_add_u32 v212, 98304, v239\n"
    "s_mov_b32 s32, s14\n"
    "s_and_b32 s33, s15, 0xffff\n"
    "s_mov_b32 s34, 0x70000\n"
    "s_mov_b32 s35, 0x20000\n"
    "v_lshlrev_b32 v239, 4, v254\n"
    "s_mul_i32 s38, s36, 114688\n"
    "v_add_u32 v214, s38, v239\n"
    "v_cmp_gt_u32 vcc, 8, v244\n"
    "s_nop 3\n"
    "s_mov_b64 s[92:93], vcc\n"
    "v_mov_b32 v224, 161792\n"
    "v_mov_b32 v240, 0\n"
    "v_mov_b32 v241, 0\n"
    "ds_write_b64 v224, v[240:241]\n"
    "ds_write_b64 v224, v[240:241] offset:8\n"
    "v_xor_b32 v210, s56, v212\n"
    "v_add_u32 v210, s64, v210\n"
    "v_add_u32 v215, 24576, v210\n"
    "v_cndmask_b32 v215, v224, v215, s[92:93]\n"
    "v_xor_b32 v211, 64, v210\n"
    "v_add_u32 v223, 24576, v211\n"
    "v_cndmask_b32 v223, v224, v223, s[92:93]\n"
    "s_waitcnt vmcnt(16) lgkmcnt(0)\n"
    "s_barrier\n"
    "ds_read_b128 v[160:163], v210\n"
    "ds_read_b64_tr_b16 v[112:113], v208\n"
    "ds_read_b64_tr_b16 v[114:115], v208 offset:512\n"
    "ds_read_b128 v[164:167], v210 offset:8192\n"
    "ds_read_b128 v[168:171], v210 offset:16384\n"
    "ds_read_b128 v[172:175], v215\n"
    "ds_read_b64_tr_b16 v[116:117], v208 offset:1024\n"
    "ds_read_b64_tr_b16 v[118:119], v208 offset:1536\n"
    "ds_read_b64_tr_b16 v[120:121], v208 offset:2048\n"
    "ds_read_b64_tr_b16 v[122:123], v208 offset:2560\n"
    "ds_read_b64_tr_b16 v[124:125], v208 offset:3072\n"
    "ds_read_b64_tr_b16 v[126:127], v208 offset:3584\n"
    "ds_read_b128 v[176:179], v211\n"
    "ds_read_b64_tr_b16 v[128:129], v208 offset:16384\n"
    "ds_read_b64_tr_b16 v[130:131], v208 offset:16896\n"
    "ds_read_b128 v[180:183], v211 offset:8192\n"
    "ds_read_b128 v[184:187], v211 offset:16384\n"
    "ds_read_b128 v[188:191], v223\n"
    "ds_read_b64_tr_b16 v[132:133], v208 offset:17408\n"
    "ds_read_b64_tr_b16 v[134:135], v208 offset:17920\n"
    "ds_read_b64_tr_b16 v[136:137], v208 offset:18432\n"
    "ds_read_b64_tr_b16 v[138:139], v208 offset:18944\n"
    "ds_read_b64_tr_b16 v[140:141], v208 offset:19456\n"
    "ds_read_b64_tr_b16 v[142:143], v208 offset:19968\n"
    "s_waitcnt lgkmcnt(15)\n"
    "v_mfma_f32_16x16x32_bf16 v[0:3], v[112:115], v[160:163], 0\n"
    "v_xor_b32 v210, s57, v212\n"
    "v_add_u32 v210, s65, v210\n"
    "v_mfma_f32_16x16x32_bf16 v[4:7], v[112:115], v[164:167], 0\n"
    "v_add_u32 v215, 24576, v210\n"
    "v_cndmask_b32 v215, v224, v215, s[92:93]\n"
    "s_add_u32 s28, s83, s75\n"
    "s_addc_u32 s29, s84, 0\n"
    "v_mfma_f32_16x16x32_bf16 v[8:11], v[112:115], v[168:171], 0\n"
    "v_mfma_f32_16x16x32_bf16 v[12:15], v[112:115], v[172:175], 0\n"
    "s_waitcnt vmcnt(8) lgkmcnt(0)\n"
    "s_barrier\n"
    "ds_read_b128 v[192:195], v210\n"
    "v_mfma_f32_16x16x32_bf16 v[16:19], v[116:119], v[160:163], 0\n"
    "s_mov_b32 m0, s85\n"
    "ds_read_b64_tr_b16 v[144:145], v208 offset:32768\n"
    "v_mfma_f32_16x16x32_bf16 v[20:23], v[116:119], v[164:167], 0\n"
    "buffer_load_dwordx4 v213, s[28:31], s40 offen lds\n"
    "v_mfma_f32_16x16x32_bf16 v[24:27], v[116:119], v[168:171], 0\n"
    "ds_read_b64_tr_b16 v[146:147], v208 offset:33280\n"
    "v_mfma_f32_16x16x32_bf16 v[28:31], v[116:119], v[172:175], 0\n"
    "buffer_load_dwordx4 v213, s[28:31], s41 offen offset:1024 lds\n"
    "v_mfma_f32_16x16x32_bf16 v[32:35], v[120:123], v[160:163], 0\n"
    "ds_read_b128 v[196:199], v210 offset:8192\n"
    "v_mfma_f32_16x16x32_bf16 v[36:39], v[120:123], v[164:167], 0\n"
    "buffer_load_dwordx4 v213, s[28:31], s42 offen offset:2048 lds\n"
    "v_mfma_f32_16x16x32_bf16 v[40:43], v[120:123], v[168:171], 0\n"
    "ds_read_b128 v[200:203], v210 offset:16384\n"
    "v_mfma_f32_16x16x32_bf16 v[44:47], v[120:123], v[172:175], 0\n"
    "buffer_load_dwordx4 v213, s[28:31], s43 offen offset:3072 lds\n"
    "v_mfma_f32_16x16x32_bf16 v[48:51], v[124:127], v[160:163], 0\n"
    "ds_read_b128 v[204:207], v215\n"
    "v_mfma_f32_16x16x32_bf16 v[52:55], v[124:127], v[164:167], 0\n"
    "ds_read_b64_tr_b16 v[148:149], v208 offset:33792\n"
    "v_mfma_f32_16x16x32_bf16 v[56:59], v[124:127], v[168:171], 0\n"
    "ds_read_b64_tr_b16 v[150:151], v208 offset:34304\n"
    "v_mfma_f32_16x16x32_bf16 v[60:63], v[124:127], v[172:175], 0\n"
    "ds_read_b64_tr_b16 v[152:153], v208 offset:34816\n"
    "ds_read_b64_tr_b16 v[154:155], v208 offset:35328\n"
    "ds_read_b64_tr_b16 v[156:157], v208 offset:35840\n"
    "ds_read_b64_tr_b16 v[158:159], v208 offset:36352\n"
    "v_mfma_f32_16x16x32_bf16 v[0:3], v[128:131], v[176:179], v[0:3]\n"
    "v_xor_b32 v211, 64, v210\n"
    "v_add_u32 v223, 24576, v211\n"
    "v_cndmask_b32 v223, v224, v223, s[92:93]\n"
    "s_add_u32 s38, s85, 16384\n"
    "s_mov_b32 m0, s38\n"
    "ds_read_b128 v[160:163], v211\n"
    "v_mfma_f32_16x16x32_bf16 v[4:7], v[128:131], v[180:183], v[4:7]\n"
    "buffer_load_dwordx4 v213, s[28:31], s44 offen lds\n"
    "v_mfma_f32_16x16x32_bf16 v[8:11], v[128:131], v[184:187], v[8:11]\n"
    "ds_read_b64_tr_b16 v[112:113], v208 offset:49152\n"
    "v_mfma_f32_16x16x32_bf16 v[12:15], v[128:131], v[188:191], v[12:15]\n"
    "ds_read_b64_tr_b16 v[114:115], v208 offset:49664\n"
    "v_mfma_f32_16x16x32_bf16 v[16:19], v[132:135], v[176:179], v[16:19]\n"
    "buffer_load_dwordx4 v213, s[28:31], s45 offen offset:1024 lds\n"
    "v_mfma_f32_16x16x32_bf16 v[20:23], v[132:135], v[180:183], v[20:23]\n"
    "ds_read_b128 v[164:167], v211 offset:8192\n"
    "v_mfma_f32_16x16x32_bf16 v[24:27], v[132:135], v[184:187], v[24:27]\n"
    "ds_read_b128 v[168:171], v211 offset:16384\n"
    "v_mfma_f32_16x16x32_bf16 v[28:31], v[132:135], v[188:191], v[28:31]\n"
    "buffer_load_dwordx4 v213, s[28:31], s46 offen offset:2048 lds\n"
    "v_mfma_f32_16x16x32_bf16 v[32:35], v[136:139], v[176:179], v[32:35]\n"
    "ds_read_b128 v[172:175], v223\n"
    "v_mfma_f32_16x16x32_bf16 v[36:39], v[136:139], v[180:183], v[36:39]\n"
    "ds_read_b64_tr_b16 v[116:117], v208 offset:50176\n"
    "v_mfma_f32_16x16x32_bf16 v[40:43], v[136:139], v[184:187], v[40:43]\n"
    "buffer_load_dwordx4 v213, s[28:31], s47 offen offset:3072 lds\n"
    "v_mfma_f32_16x16x32_bf16 v[44:47], v[136:139], v[188:191], v[44:47]\n"
    "ds_read_b64_tr_b16 v[118:119], v208 offset:50688\n"
    "v_mfma_f32_16x16x32_bf16 v[48:51], v[140:143], v[176:179], v[48:51]\n"
    "ds_read_b64_tr_b16 v[120:121], v208 offset:51200\n"
    "v_mfma_f32_16x16x32_bf16 v[52:55], v[140:143], v[180:183], v[52:55]\n"
    "ds_read_b64_tr_b16 v[122:123], v208 offset:51712\n"
    "v_mfma_f32_16x16x32_bf16 v[56:59], v[140:143], v[184:187], v[56:59]\n"
    "ds_read_b64_tr_b16 v[124:125], v208 offset:52224\n"
    "v_mfma_f32_16x16x32_bf16 v[60:63], v[140:143], v[188:191], v[60:63]\n"
    "ds_read_b64_tr_b16 v[126:127], v208 offset:52736\n"
    "s_waitcnt lgkmcnt(15)\n"
    "v_mfma_f32_16x16x32_bf16 v[0:3], v[144:147], v[192:195], v[0:3]\n"
    "v_xor_b32 v210, s58, v212\n"
    "v_add_u32 v210, s66, v210\n"
    "v_mfma_f32_16x16x32_bf16 v[4:7], v[144:147], v[196:199], v[4:7]\n"
    "v_add_u32 v215, 24576, v210\n"
    "v_cndmask_b32 v215, v224, v215, s[92:93]\n"
    "s_add_u32 s28, s83, s76\n"
    "s_addc_u32 s29, s84, 0\n"
    "v_mfma_f32_16x16x32_bf16 v[8:11], v[144:147], v[200:203], v[8:11]\n"
    "v_mfma_f32_16x16x32_bf16 v[12:15], v[144:147], v[204:207], v[12:15]\n"
    "s_waitcnt vmcnt(8) lgkmcnt(0)\n"
    "s_barrier\n"
    "ds_read_b128 v[176:179], v210\n"
    "v_mfma_f32_16x16x32_bf16 v[16:19], v[148:151], v[192:195], v[16:19]\n"
    "s_mov_b32 m0, s86\n"
    "ds_read_b64_tr_b16 v[128:129], v209\n"
    "v_mfma_f32_16x16x32_bf16 v[20:23], v[148:151], v[196:199], v[20:23]\n"
    "buffer_load_dwordx4 v213, s[28:31], s40 offen lds\n"
    "v_mfma_f32_16x16x32_bf16 v[24:27], v[148:151], v[200:203], v[24:27]\n"
    "ds_read_b64_tr_b16 v[130:131], v209 offset:512\n"
    "v_mfma_f32_16x16x32_bf16 v[28:31], v[148:151], v[204:207], v[28:31]\n"
    "buffer_load_dwordx4 v213, s[28:31], s41 offen offset:1024 lds\n"
    "v_mfma_f32_16x16x32_bf16 v[32:35], v[152:155], v[192:195], v[32:35]\n"
    "ds_read_b128 v[180:183], v210 offset:8192\n"
    "v_mfma_f32_16x16x32_bf16 v[36:39], v[152:155], v[196:199], v[36:39]\n"
    "buffer_load_dwordx4 v213, s[28:31], s42 offen offset:2048 lds\n"
    "v_mfma_f32_16x16x32_bf16 v[40:43], v[152:155], v[200:203], v[40:43]\n"
    "ds_read_b128 v[184:187], v210 offset:16384\n"
    "v_mfma_f32_16x16x32_bf16 v[44:47], v[152:155], v[204:207], v[44:47]\n"
    "buffer_load_dwordx4 v213, s[28:31], s43 offen offset:3072 lds\n"
    "v_mfma_f32_16x16x32_bf16 v[48:51], v[156:159], v[192:195], v[48:51]\n"
    "ds_read_b128 v[188:191], v215\n"
    "v_mfma_f32_16x16x32_bf16 v[52:55], v[156:159], v[196:199], v[52:55]\n"
    "ds_read_b64_tr_b16 v[132:133], v209 offset:1024\n"
    "v_mfma_f32_16x16x32_bf16 v[56:59], v[156:159], v[200:203], v[56:59]\n"
    "ds_read_b64_tr_b16 v[134:135], v209 offset:1536\n"
    "v_mfma_f32_16x16x32_bf16 v[60:63], v[156:159], v[204:207], v[60:63]\n"
    "ds_read_b64_tr_b16 v[136:137], v209 offset:2048\n"
    "ds_read_b64_tr_b16 v[138:139], v209 offset:2560\n"
    "ds_read_b64_tr_b16 v[140:141], v209 offset:3072\n"
    "ds_read_b64_tr_b16 v[142:143], v209 offset:3584\n"
    "v_mfma_f32_16x16x32_bf16 v[0:3], v[112:115], v[160:163], v[0:3]\n"
    "v_xor_b32 v211, 64, v210\n"
    "v_add_u32 v223, 24576, v211\n"
    "v_cndmask_b32 v223, v224, v223, s[92:93]\n"
    "s_add_u32 s38, s86, 16384\n"
    "s_mov_b32 m0, s38\n"
    "ds_read_b128 v[192:195], v211\n"
    "v_mfma_f32_16x16x32_bf16 v[4:7], v[112:115], v[164:167], v[4:7]\n"
    "buffer_load_dwordx4 v213, s[28:31], s44 offen lds\n"
    "v_mfma_f32_16x16x32_bf16 v[8:11], v[112:115], v[168:171], v[8:11]\n"
    "ds_read_b64_tr_b16 v[144:145], v209 offset:16384\n"
    "v_mfma_f32_16x16x32_bf16 v[12:15], v[112:115], v[172:175], v[12:15]\n"
    "ds_read_b64_tr_b16 v[146:147], v209 offset:16896\n"
    "v_mfma_f32_16x16x32_bf16 v[16:19], v[116:119], v[160:163], v[16:19]\n"
    "buffer_load_dwordx4 v213, s[28:31], s45 offen offset:1024 lds\n"
    "v_mfma_f32_16x16x32_bf16 v[20:23], v[116:119], v[164:167], v[20:23]\n"
    "ds_read_b128 v[196:199], v211 offset:8192\n"
    "v_mfma_f32_16x16x32_bf16 v[24:27], v[116:119], v[168:171], v[24:27]\n"
    "ds_read_b128 v[200:203], v211 offset:16384\n"
    "v_mfma_f32_16x16x32_bf16 v[28:31], v[116:119], v[172:175], v[28:31]\n"
    "buffer_load_dwordx4 v213, s[28:31], s46 offen offset:2048 lds\n"
    "v_mfma_f32_16x16x32_bf16 v[32:35], v[120:123], v[160:163], v[32:35]\n"
    "ds_read_b128 v[204:207], v223\n"
    "v_mfma_f32_16x16x32_bf16 v[36:39], v[120:123], v[164:167], v[36:39]\n"
    "ds_read_b64_tr_b16 v[148:149], v209 offset:17408\n"
    "v_mfma_f32_16x16x32_bf16 v[40:43], v[120:123], v[168:171], v[40:43]\n"
    "buffer_load_dwordx4 v213, s[28:31], s47 offen offset:3072 lds\n"
    "v_mfma_f32_16x16x32_bf16 v[44:47], v[120:123], v[172:175], v[44:47]\n"
    "ds_read_b64_tr_b16 v[150:151], v209 offset:17920\n"
    "v_mfma_f32_16x16x32_bf16 v[48:51], v[124:127], v[160:163], v[48:51]\n"
    "ds_read_b64_tr_b16 v[152:153], v209 offset:18432\n"
    "v_mfma_f32_16x16x32_bf16 v[52:55], v[124:127], v[164:167], v[52:55]\n"
    "ds_read_b64_tr_b16 v[154:155], v209 offset:18944\n"
    "v_mfma_f32_16x16x32_bf16 v[56:59], v[124:127], v[168:171], v[56:59]\n"
    "ds_read_b64_tr_b16 v[156:157], v209 offset:19456\n"
    "v_mfma_f32_16x16x32_bf16 v[60:63], v[124:127], v[172:175], v[60:63]\n"
    "ds_read_b64_tr_b16 v[158:159], v209 offset:19968\n"
    "s_waitcnt lgkmcnt(15)\n"
    "v_mfma_f32_16x16x32_bf16 v[0:3], v[128:131], v[176:179], v[0:3]\n"
    "v_xor_b32 v210, s59, v212\n"
    "v_add_u32 v210, s67, v210\n"
    "v_mfma_f32_16x16x32_bf16 v[4:7], v[128:131], v[180:183], v[4:7]\n"
    "v_add_u32 v215, 24576, v210\n"
    "v_cndmask_b32 v215, v224, v215, s[92:93]\n"
    "s_add_u32 s28, s83, s77\n"
    "s_addc_u32 s29, s84, 0\n"
    "v_mfma_f32_16x16x32_bf16 v[8:11], v[128:131], v[184:187], v[8:11]\n"
    "v_mfma_f32_16x16x32_bf16 v[12:15], v[128:131], v[188:191], v[12:15]\n"
    "s_waitcnt vmcnt(8) lgkmcnt(0)\n"
    "s_barrier\n"
    "ds_read_b128 v[160:163], v210\n"
    "v_mfma_f32_16x16x32_bf16 v[16:19], v[132:135], v[176:179], v[16:19]\n"
    "s_mov_b32 m0, s87\n"
    "ds_read_b64_tr_b16 v[112:113], v208\n"
    "v_mfma_f32_16x16x32_bf16 v[20:23], v[132:135], v[180:183], v[20:23]\n"
    "buffer_load_dwordx4 v213, s[28:31], s40 offen lds\n"
    "v_mfma_f32_16x16x32_bf16 v[24:27], v[132:135], v[184:187], v[24:27]\n"
    "ds_read_b64_tr_b16 v[114:115], v208 offset:512\n"
    "v_mfma_f32_16x16x32_bf16 v[28:31], v[132:135], v[188:191], v[28:31]\n"
    "buffer_load_dwordx4 v213, s[28:31], s41 offen offset:1024 lds\n"
    "v_mfma_f32_16x16x32_bf16 v[32:35], v[136:139], v[176:179], v[32:35]\n"
    "ds_read_b128 v[164:167], v210 offset:8192\n"
    "v_mfma_f32_16x16x32_bf16 v[36:39], v[136:139], v[180:183], v[36:39]\n"
    "buffer_load_dwordx4 v213, s[28:31], s42 offen offset:2048 lds\n"
    "v_mfma_f32_16x16x32_bf16 v[40:43], v[136:139], v[184:187], v[40:43]\n"
    "ds_read_b128 v[168:171], v210 offset:16384\n"
    "v_mfma_f32_16x16x32_bf16 v[44:47], v[136:139], v[188:191], v[44:47]\n"
    "buffer_load_dwordx4 v213, s[28:31], s43 offen offset:3072 lds\n"
    "v_mfma_f32_16x16x32_bf16 v[48:51], v[140:143], v[176:179], v[48:51]\n"
    "ds_read_b128 v[172:175], v215\n"
    "v_mfma_f32_16x16x32_bf16 v[52:55], v[140:143], v[180:183], v[52:55]\n"
    "ds_read_b64_tr_b16 v[116:117], v208 offset:1024\n"
    "v_mfma_f32_16x16x32_bf16 v[56:59], v[140:143], v[184:187], v[56:59]\n"
    "ds_read_b64_tr_b16 v[118:119], v208 offset:1536\n"
    "v_mfma_f32_16x16x32_bf16 v[60:63], v[140:143], v[188:191], v[60:63]\n"
    "ds_read_b64_tr_b16 v[120:121], v208 offset:2048\n"
    "ds_read_b64_tr_b16 v[122:123], v208 offset:2560\n"
    "ds_read_b64_tr_b16 v[124:125], v208 offset:3072\n"
    "ds_read_b64_tr_b16 v[126:127], v208 offset:3584\n"
    "v_mfma_f32_16x16x32_bf16 v[0:3], v[144:147], v[192:195], v[0:3]\n"
    "v_xor_b32 v211, 64, v210\n"
    "v_add_u32 v223, 24576, v211\n"
    "v_cndmask_b32 v223, v224, v223, s[92:93]\n"
    "s_add_u32 s38, s87, 16384\n"
    "s_mov_b32 m0, s38\n"
    "ds_read_b128 v[176:179], v211\n"
    "v_mfma_f32_16x16x32_bf16 v[4:7], v[144:147], v[196:199], v[4:7]\n"
    "buffer_load_dwordx4 v213, s[28:31], s44 offen lds\n"
    "v_mfma_f32_16x16x32_bf16 v[8:11], v[144:147], v[200:203], v[8:11]\n"
    "ds_read_b64_tr_b16 v[128:129], v208 offset:16384\n"
    "v_mfma_f32_16x16x32_bf16 v[12:15], v[144:147], v[204:207], v[12:15]\n"
    "ds_read_b64_tr_b16 v[130:131], v208 offset:16896\n"
    "v_mfma_f32_16x16x32_bf16 v[16:19], v[148:151], v[192:195], v[16:19]\n"
    "buffer_load_dwordx4 v213, s[28:31], s45 offen offset:1024 lds\n"
    "v_mfma_f32_16x16x32_bf16 v[20:23], v[148:151], v[196:199], v[20:23]\n"
    "ds_read_b128 v[180:183], v211 offset:8192\n"
    "v_mfma_f32_16x16x32_bf16 v[24:27], v[148:151], v[200:203], v[24:27]\n"
    "ds_read_b128 v[184:187], v211 offset:16384\n"
    "v_mfma_f32_16x16x32_bf16 v[28:31], v[148:151], v[204:207], v[28:31]\n"
    "buffer_load_dwordx4 v213, s[28:31], s46 offen offset:2048 lds\n"
    "v_mfma_f32_16x16x32_bf16 v[32:35], v[152:155], v[192:195], v[32:35]\n"
    "ds_read_b128 v[188:191], v223\n"
    "v_mfma_f32_16x16x32_bf16 v[36:39], v[152:155], v[196:199], v[36:39]\n"
    "ds_read_b64_tr_b16 v[132:133], v208 offset:17408\n"
    "v_mfma_f32_16x16x32_bf16 v[40:43], v[152:155], v[200:203], v[40:43]\n"
    "buffer_load_dwordx4 v213, s[28:31], s47 offen offset:3072 lds\n"
    "v_mfma_f32_16x16x32_bf16 v[44:47], v[152:155], v[204:207], v[44:47]\n"
    "ds_read_b64_tr_b16 v[134:135], v208 offset:17920\n"
    "v_mfma_f32_16x16x32_bf16 v[48:51], v[156:159], v[192:195], v[48:51]\n"
    "ds_read_b64_tr_b16 v[136:137], v208 offset:18432\n"
    "v_mfma_f32_16x16x32_bf16 v[52:55], v[156:159], v[196:199], v[52:55]\n"
    "ds_read_b64_tr_b16 v[138:139], v208 offset:18944\n"
    "v_mfma_f32_16x16x32_bf16 v[56:59], v[156:159], v[200:203], v[56:59]\n"
    "ds_read_b64_tr_b16 v[140:141], v208 offset:19456\n"
    "v_mfma_f32_16x16x32_bf16 v[60:63], v[156:159], v[204:207], v[60:63]\n"
    "ds_read_b64_tr_b16 v[142:143], v208 offset:19968\n"
    "s_waitcnt lgkmcnt(15)\n"
    "v_mfma_f32_16x16x32_bf16 v[0:3], v[112:115], v[160:163], v[0:3]\n"
    "v_xor_b32 v210, s60, v212\n"
    "v_add_u32 v210, s68, v210\n"
    "v_mfma_f32_16x16x32_bf16 v[4:7], v[112:115], v[164:167], v[4:7]\n"
    "v_add_u32 v215, 24576, v210\n"
    "v_cndmask_b32 v215, v224, v215, s[92:93]\n"
    "s_add_u32 s28, s83, s78\n"
    "s_addc_u32 s29, s84, 0\n"
    "v_mfma_f32_16x16x32_bf16 v[8:11], v[112:115], v[168:171], v[8:11]\n"
    "v_mfma_f32_16x16x32_bf16 v[12:15], v[112:115], v[172:175], v[12:15]\n"
    "s_waitcnt vmcnt(8) lgkmcnt(0)\n"
    "s_barrier\n"
    "ds_read_b128 v[192:195], v210\n"
    "v_mfma_f32_16x16x32_bf16 v[16:19], v[116:119], v[160:163], v[16:19]\n"
    "s_mov_b32 m0, s85\n"
    "ds_read_b64_tr_b16 v[144:145], v208 offset:32768\n"
    "v_mfma_f32_16x16x32_bf16 v[20:23], v[116:119], v[164:167], v[20:23]\n"
    "buffer_load_dwordx4 v213, s[28:31], s40 offen lds\n"
    "v_mfma_f32_16x16x32_bf16 v[24:27], v[116:119], v[168:171], v[24:27]\n"
    "ds_read_b64_tr_b16 v[146:147], v208 offset:33280\n"
    "v_mfma_f32_16x16x32_bf16 v[28:31], v[116:119], v[172:175], v[28:31]\n"
    "buffer_load_dwordx4 v213, s[28:31], s41 offen offset:1024 lds\n"
    "v_mfma_f32_16x16x32_bf16 v[32:35], v[120:123], v[160:163], v[32:35]\n"
    "ds_read_b128 v[196:199], v210 offset:8192\n"
    "v_mfma_f32_16x16x32_bf16 v[36:39], v[120:123], v[164:167], v[36:39]\n"
    "buffer_load_dwordx4 v213, s[28:31], s42 offen offset:2048 lds\n"
    "v_mfma_f32_16x16x32_bf16 v[40:43], v[120:123], v[168:171], v[40:43]\n"
    "ds_read_b128 v[200:203], v210 offset:16384\n"
    "v_mfma_f32_16x16x32_bf16 v[44:47], v[120:123], v[172:175], v[44:47]\n"
    "buffer_load_dwordx4 v213, s[28:31], s43 offen offset:3072 lds\n"
    "v_mfma_f32_16x16x32_bf16 v[48:51], v[124:127], v[160:163], v[48:51]\n"
    "ds_read_b128 v[204:207], v215\n"
    "v_mfma_f32_16x16x32_bf16 v[52:55], v[124:127], v[164:167], v[52:55]\n"
    "ds_read_b64_tr_b16 v[148:149], v208 offset:33792\n"
    "v_mfma_f32_16x16x32_bf16 v[56:59], v[124:127], v[168:171], v[56:59]\n"
    "ds_read_b64_tr_b16 v[150:151], v208 offset:34304\n"
    "v_mfma_f32_16x16x32_bf16 v[60:63], v[124:127], v[172:175], v[60:63]\n"
    "ds_read_b64_tr_b16 v[152:153], v208 offset:34816\n"
    "ds_read_b64_tr_b16 v[154:155], v208 offset:35328\n"
    "ds_read_b64_tr_b16 v[156:157], v208 offset:35840\n"
    "ds_read_b64_tr_b16 v[158:159], v208 offset:36352\n"
    "v_mfma_f32_16x16x32_bf16 v[0:3], v[128:131], v[176:179], v[0:3]\n"
    "v_xor_b32 v211, 64, v210\n"
    "v_add_u32 v223, 24576, v211\n"
    "v_cndmask_b32 v223, v224, v223, s[92:93]\n"
    "s_add_u32 s38, s85, 16384\n"
    "s_mov_b32 m0, s38\n"
    "ds_read_b128 v[160:163], v211\n"
    "v_mfma_f32_16x16x32_bf16 v[4:7], v[128:131], v[180:183], v[4:7]\n"
    "buffer_load_dwordx4 v213, s[28:31], s44 offen lds\n"
    "v_mfma_f32_16x16x32_bf16 v[8:11], v[128:131], v[184:187], v[8:11]\n"
    "ds_read_b64_tr_b16 v[112:113], v208 offset:49152\n"
    "v_mfma_f32_16x16x32_bf16 v[12:15], v[128:131], v[188:191], v[12:15]\n"
    "ds_read_b64_tr_b16 v[114:115], v208 offset:49664\n"
    "v_mfma_f32_16x16x32_bf16 v[16:19], v[132:135], v[176:179], v[16:19]\n"
    "buffer_load_dwordx4 v213, s[28:31], s45 offen offset:1024 lds\n"
    "v_mfma_f32_16x16x32_bf16 v[20:23], v[132:135], v[180:183], v[20:23]\n"
    "ds_read_b128 v[164:167], v211 offset:8192\n"
    "v_mfma_f32_16x16x32_bf16 v[24:27], v[132:135], v[184:187], v[24:27]\n"
    "ds_read_b128 v[168:171], v211 offset:16384\n"
    "v_mfma_f32_16x16x32_bf16 v[28:31], v[132:135], v[188:191], v[28:31]\n"
    "buffer_load_dwordx4 v213, s[28:31], s46 offen offset:2048 lds\n"
    "v_mfma_f32_16x16x32_bf16 v[32:35], v[136:139], v[176:179], v[32:35]\n"
    "ds_read_b128 v[172:175], v223\n"
    "v_mfma_f32_16x16x32_bf16 v[36:39], v[136:139], v[180:183], v[36:39]\n"
    "ds_read_b64_tr_b16 v[116:117], v208 offset:50176\n"
    "v_mfma_f32_16x16x32_bf16 v[40:43], v[136:139], v[184:187], v[40:43]\n"
    "buffer_load_dwordx4 v213, s[28:31], s47 offen offset:3072 lds\n"
    "v_mfma_f32_16x16x32_bf16 v[44:47], v[136:139], v[188:191], v[44:47]\n"
    "ds_read_b64_tr_b16 v[118:119], v208 offset:50688\n"
    "v_mfma_f32_16x16x32_bf16 v[48:51], v[140:143], v[176:179], v[48:51]\n"
    "ds_read_b64_tr_b16 v[120:121], v208 offset:51200\n"
    "v_mfma_f32_16x16x32_bf16 v[52:55], v[140:143], v[180:183], v[52:55]\n"
    "ds_read_b64_tr_b16 v[122:123], v208 offset:51712\n"
    "v_mfma_f32_16x16x32_bf16 v[56:59], v[140:143], v[184:187], v[56:59]\n"
    "ds_read_b64_tr_b16 v[124:125], v208 offset:52224\n"
    "v_mfma_f32_16x16x32_bf16 v[60:63], v[140:143], v[188:191], v[60:63]\n"
    "ds_read_b64_tr_b16 v[126:127], v208 offset:52736\n"
    "s_waitcnt lgkmcnt(15)\n"
    "v_mfma_f32_16x16x32_bf16 v[0:3], v[144:147], v[192:195], v[0:3]\n"
    "v_xor_b32 v210, s61, v212\n"
    "v_add_u32 v210, s69, v210\n"
    "v_mfma_f32_16x16x32_bf16 v[4:7], v[144:147], v[196:199], v[4:7]\n"
    "v_add_u32 v215, 24576, v210\n"
    "v_cndmask_b32 v215, v224, v215, s[92:93]\n"
    "s_add_u32 s28, s83, s79\n"
    "s_addc_u32 s29, s84, 0\n"
    "v_mfma_f32_16x16x32_bf16 v[8:11], v[144:147], v[200:203], v[8:11]\n"
    "v_mfma_f32_16x16x32_bf16 v[12:15], v[144:147], v[204:207], v[12:15]\n"
    "s_waitcnt vmcnt(8) lgkmcnt(0)\n"
    "s_barrier\n"
    "ds_read_b128 v[176:179], v210\n"
    "v_mfma_f32_16x16x32_bf16 v[16:19], v[148:151], v[192:195], v[16:19]\n"
    "s_mov_b32 m0, s86\n"
    "ds_read_b64_tr_b16 v[128:129], v209\n"
    "v_mfma_f32_16x16x32_bf16 v[20:23], v[148:151], v[196:199], v[20:23]\n"
    "buffer_load_dwordx4 v213, s[28:31], s40 offen lds\n"
    "v_mfma_f32_16x16x32_bf16 v[24:27], v[148:151], v[200:203], v[24:27]\n"
    "ds_read_b64_tr_b16 v[130:131], v209 offset:512\n"
    "v_mfma_f32_16x16x32_bf16 v[28:31], v[148:151], v[204:207], v[28:31]\n"
    "buffer_load_dwordx4 v213, s[28:31], s41 offen offset:1024 lds\n"
    "v_mfma_f32_16x16x32_bf16 v[32:35], v[152:155], v[192:195], v[32:35]\n"
    "ds_read_b128 v[180:183], v210 offset:8192\n"
    "v_mfma_f32_16x16x32_bf16 v[36:39], v[152:155], v[196:199], v[36:39]\n"
    "buffer_load_dwordx4 v213, s[28:31], s42 offen offset:2048 lds\n"
    "v_mfma_f32_16x16x32_bf16 v[40:43], v[152:155], v[200:203], v[40:43]\n"
    "ds_read_b128 v[184:187], v210 offset:16384\n"
    "v_mfma_f32_16x16x32_bf16 v[44:47], v[152:155], v[204:207], v[44:47]\n"
    "buffer_load_dwordx4 v213, s[28:31], s43 offen offset:3072 lds\n"
    "v_mfma_f32_16x16x32_bf16 v[48:51], v[156:159], v[192:195], v[48:51]\n"
    "ds_read_b128 v[188:191], v215\n"
    "v_mfma_f32_16x16x32_bf16 v[52:55], v[156:159], v[196:199], v[52:55]\n"
    "ds_read_b64_tr_b16 v[132:133], v209 offset:1024\n"
    "v_mfma_f32_16x16x32_bf16 v[56:59], v[156:159], v[200:203], v[56:59]\n"
    "ds_read_b64_tr_b16 v[134:135], v209 offset:1536\n"
    "v_mfma_f32_16x16x32_bf16 v[60:63], v[156:159], v[204:207], v[60:63]\n"
    "ds_read_b64_tr_b16 v[136:137], v209 offset:2048\n"
    "ds_read_b64_tr_b16 v[138:139], v209 offset:2560\n"
    "ds_read_b64_tr_b16 v[140:141], v209 offset:3072\n"
    "ds_read_b64_tr_b16 v[142:143], v209 offset:3584\n"
    "v_mfma_f32_16x16x32_bf16 v[0:3], v[112:115], v[160:163], v[0:3]\n"
    "v_xor_b32 v211, 64, v210\n"
    "v_add_u32 v223, 24576, v211\n"
    "v_cndmask_b32 v223, v224, v223, s[92:93]\n"
    "s_add_u32 s38, s86, 16384\n"
    "s_mov_b32 m0, s38\n"
    "ds_read_b128 v[192:195], v211\n"
    "v_mfma_f32_16x16x32_bf16 v[4:7], v[112:115], v[164:167], v[4:7]\n"
    "buffer_load_dwordx4 v213, s[28:31], s44 offen lds\n"
    "v_mfma_f32_16x16x32_bf16 v[8:11], v[112:115], v[168:171], v[8:11]\n"
    "ds_read_b64_tr_b16 v[144:145], v209 offset:16384\n"
    "v_mfma_f32_16x16x32_bf16 v[12:15], v[112:115], v[172:175], v[12:15]\n"
    "ds_read_b64_tr_b16 v[146:147], v209 offset:16896\n"
    "v_mfma_f32_16x16x32_bf16 v[16:19], v[116:119], v[160:163], v[16:19]\n"
    "buffer_load_dwordx4 v213, s[28:31], s45 offen offset:1024 lds\n"
    "v_mfma_f32_16x16x32_bf16 v[20:23], v[116:119], v[164:167], v[20:23]\n"
    "ds_read_b128 v[196:199], v211 offset:8192\n"
    "v_mfma_f32_16x16x32_bf16 v[24:27], v[116:119], v[168:171], v[24:27]\n"
    "ds_read_b128 v[200:203], v211 offset:16384\n"
    "v_mfma_f32_16x16x32_bf16 v[28:31], v[116:119], v[172:175], v[28:31]\n"
    "buffer_load_dwordx4 v213, s[28:31], s46 offen offset:2048 lds\n"
    "v_mfma_f32_16x16x32_bf16 v[32:35], v[120:123], v[160:163], v[32:35]\n"
    "ds_read_b128 v[204:207], v223\n"
    "v_mfma_f32_16x16x32_bf16 v[36:39], v[120:123], v[164:167], v[36:39]\n"
    "ds_read_b64_tr_b16 v[148:149], v209 offset:17408\n"
    "v_mfma_f32_16x16x32_bf16 v[40:43], v[120:123], v[168:171], v[40:43]\n"
    "buffer_load_dwordx4 v213, s[28:31], s47 offen offset:3072 lds\n"
    "v_mfma_f32_16x16x32_bf16 v[44:47], v[120:123], v[172:175], v[44:47]\n"
    "ds_read_b64_tr_b16 v[150:151], v209 offset:17920\n"
    "v_mfma_f32_16x16x32_bf16 v[48:51], v[124:127], v[160:163], v[48:51]\n"
    "ds_read_b64_tr_b16 v[152:153], v209 offset:18432\n"
    "v_mfma_f32_16x16x32_bf16 v[52:55], v[124:127], v[164:167], v[52:55]\n"
    "ds_read_b64_tr_b16 v[154:155], v209 offset:18944\n"
    "v_mfma_f32_16x16x32_bf16 v[56:59], v[124:127], v[168:171], v[56:59]\n"
    "ds_read_b64_tr_b16 v[156:157], v209 offset:19456\n"
    "v_mfma_f32_16x16x32_bf16 v[60:63], v[124:127], v[172:175], v[60:63]\n"
    "ds_read_b64_tr_b16 v[158:159], v209 offset:19968\n"
    "s_waitcnt lgkmcnt(15)\n"
    "v_mfma_f32_16x16x32_bf16 v[0:3], v[128:131], v[176:179], v[0:3]\n"
    "v_xor_b32 v210, s62, v212\n"
    "v_add_u32 v210, s70, v210\n"
    "v_mfma_f32_16x16x32_bf16 v[4:7], v[128:131], v[180:183], v[4:7]\n"
    "v_add_u32 v215, 24576, v210\n"
    "v_cndmask_b32 v215, v224, v215, s[92:93]\n"
    "s_add_u32 s28, s83, s72\n"
    "s_addc_u32 s29, s84, 0\n"
    "v_mfma_f32_16x16x32_bf16 v[8:11], v[128:131], v[184:187], v[8:11]\n"
    "v_mfma_f32_16x16x32_bf16 v[12:15], v[128:131], v[188:191], v[12:15]\n"
    "s_waitcnt vmcnt(8) lgkmcnt(0)\n"
    "s_barrier\n"
    "ds_read_b128 v[160:163], v210\n"
    "v_mfma_f32_16x16x32_bf16 v[16:19], v[132:135], v[176:179], v[16:19]\n"
    "s_mov_b32 m0, s87\n"
    "ds_read_b64_tr_b16 v[112:113], v208\n"
    "v_mfma_f32_16x16x32_bf16 v[20:23], v[132:135], v[180:183], v[20:23]\n"
    "buffer_load_dwordx4 v213, s[28:31], s48 offen lds\n"
    "v_mfma_f32_16x16x32_bf16 v[24:27], v[132:135], v[184:187], v[24:27]\n"
    "ds_read_b64_tr_b16 v[114:115], v208 offset:512\n"
    "v_mfma_f32_16x16x32_bf16 v[28:31], v[132:135], v[188:191], v[28:31]\n"
    "buffer_load_dwordx4 v213, s[28:31], s49 offen offset:1024 lds\n"
    "v_mfma_f32_16x16x32_bf16 v[32:35], v[136:139], v[176:179], v[32:35]\n"
    "ds_read_b128 v[164:167], v210 offset:8192\n"
    "v_mfma_f32_16x16x32_bf16 v[36:39], v[136:139], v[180:183], v[36:39]\n"
    "buffer_load_dwordx4 v213, s[28:31], s50 offen offset:2048 lds\n"
    "v_mfma_f32_16x16x32_bf16 v[40:43], v[136:139], v[184:187], v[40:43]\n"
    "ds_read_b128 v[168:171], v210 offset:16384\n"
    "v_mfma_f32_16x16x32_bf16 v[44:47], v[136:139], v[188:191], v[44:47]\n"
    "ds_read_b128 v[172:175], v215\n"
    "v_mfma_f32_16x16x32_bf16 v[48:51], v[140:143], v[176:179], v[48:51]\n"
    "ds_read_b64_tr_b16 v[116:117], v208 offset:1024\n"
    "v_mfma_f32_16x16x32_bf16 v[52:55], v[140:143], v[180:183], v[52:55]\n"
    "ds_read_b64_tr_b16 v[118:119], v208 offset:1536\n"
    "v_mfma_f32_16x16x32_bf16 v[56:59], v[140:143], v[184:187], v[56:59]\n"
    "ds_read_b64_tr_b16 v[120:121], v208 offset:2048\n"
    "v_mfma_f32_16x16x32_bf16 v[60:63], v[140:143], v[188:191], v[60:63]\n"
    "ds_read_b64_tr_b16 v[122:123], v208 offset:2560\n"
    "ds_read_b64_tr_b16 v[124:125], v208 offset:3072\n"
    "ds_read_b64_tr_b16 v[126:127], v208 offset:3584\n"
    "v_mfma_f32_16x16x32_bf16 v[0:3], v[144:147], v[192:195], v[0:3]\n"
    "v_xor_b32 v211, 64, v210\n"
    "v_add_u32 v223, 24576, v211\n"
    "v_cndmask_b32 v223, v224, v223, s[92:93]\n"
    "s_add_u32 s38, s87, 16384\n"
    "s_mov_b32 m0, s38\n"
    "ds_read_b128 v[176:179], v211\n"
    "v_mfma_f32_16x16x32_bf16 v[4:7], v[144:147], v[196:199], v[4:7]\n"
    "buffer_load_dwordx4 v213, s[28:31], s51 offen lds\n"
    "v_mfma_f32_16x16x32_bf16 v[8:11], v[144:147], v[200:203], v[8:11]\n"
    "ds_read_b64_tr_b16 v[128:129], v208 offset:16384\n"
    "v_mfma_f32_16x16x32_bf16 v[12:15], v[144:147], v[204:207], v[12:15]\n"
    "ds_read_b64_tr_b16 v[130:131], v208 offset:16896\n"
    "v_mfma_f32_16x16x32_bf16 v[16:19], v[148:151], v[192:195], v[16:19]\n"
    "buffer_load_dwordx4 v213, s[28:31], s52 offen offset:1024 lds\n"
    "v_mfma_f32_16x16x32_bf16 v[20:23], v[148:151], v[196:199], v[20:23]\n"
    "ds_read_b128 v[180:183], v211 offset:8192\n"
    "v_mfma_f32_16x16x32_bf16 v[24:27], v[148:151], v[200:203], v[24:27]\n"
    "ds_read_b128 v[184:187], v211 offset:16384\n"
    "v_mfma_f32_16x16x32_bf16 v[28:31], v[148:151], v[204:207], v[28:31]\n"
    "buffer_load_dwordx4 v213, s[28:31], s53 offen offset:2048 lds\n"
    "v_mfma_f32_16x16x32_bf16 v[32:35], v[152:155], v[192:195], v[32:35]\n"
    "ds_read_b128 v[188:191], v223\n"
    "v_mfma_f32_16x16x32_bf16 v[36:39], v[152:155], v[196:199], v[36:39]\n"
    "ds_read_b64_tr_b16 v[132:133], v208 offset:17408\n"
    "v_mfma_f32_16x16x32_bf16 v[40:43], v[152:155], v[200:203], v[40:43]\n"
    "ds_read_b64_tr_b16 v[134:135], v208 offset:17920\n"
    "v_mfma_f32_16x16x32_bf16 v[44:47], v[152:155], v[204:207], v[44:47]\n"
    "ds_read_b64_tr_b16 v[136:137], v208 offset:18432\n"
    "v_mfma_f32_16x16x32_bf16 v[48:51], v[156:159], v[192:195], v[48:51]\n"
    "ds_read_b64_tr_b16 v[138:139], v208 offset:18944\n"
    "v_mfma_f32_16x16x32_bf16 v[52:55], v[156:159], v[196:199], v[52:55]\n"
    "ds_read_b64_tr_b16 v[140:141], v208 offset:19456\n"
    "v_mfma_f32_16x16x32_bf16 v[56:59], v[156:159], v[200:203], v[56:59]\n"
    "ds_read_b64_tr_b16 v[142:143], v208 offset:19968\n"
    "v_mfma_f32_16x16x32_bf16 v[60:63], v[156:159], v[204:207], v[60:63]\n"
    "s_waitcnt lgkmcnt(15)\n"
    "v_mfma_f32_16x16x32_bf16 v[0:3], v[112:115], v[160:163], v[0:3]\n"
    "v_xor_b32 v210, s63, v212\n"
    "v_add_u32 v210, s71, v210\n"
    "v_mfma_f32_16x16x32_bf16 v[4:7], v[112:115], v[164:167], v[4:7]\n"
    "v_add_u32 v215, 24576, v210\n"
    "v_cndmask_b32 v215, v224, v215, s[92:93]\n"
    "s_add_u32 s28, s83, s73\n"
    "s_addc_u32 s29, s84, 0\n"
    "v_mfma_f32_16x16x32_bf16 v[8:11], v[112:115], v[168:171], v[8:11]\n"
    "v_mfma_f32_16x16x32_bf16 v[12:15], v[112:115], v[172:175], v[12:15]\n"
    "s_waitcnt vmcnt(6) lgkmcnt(0)\n"
    "s_barrier\n"
    "ds_read_b128 v[192:195], v210\n"
    "v_mfma_f32_16x16x32_bf16 v[16:19], v[116:119], v[160:163], v[16:19]\n"
    "s_mov_b32 m0, s85\n"
    "ds_read_b64_tr_b16 v[144:145], v208 offset:32768\n"
    "v_mfma_f32_16x16x32_bf16 v[20:23], v[116:119], v[164:167], v[20:23]\n"
    "buffer_load_dwordx4 v213, s[28:31], s48 offen lds\n"
    "v_mfma_f32_16x16x32_bf16 v[24:27], v[116:119], v[168:171], v[24:27]\n"
    "ds_read_b64_tr_b16 v[146:147], v208 offset:33280\n"
    "v_mfma_f32_16x16x32_bf16 v[28:31], v[116:119], v[172:175], v[28:31]\n"
    "buffer_load_dwordx4 v213, s[28:31], s49 offen offset:1024 lds\n"
    "v_mfma_f32_16x16x32_bf16 v[32:35], v[120:123], v[160:163], v[32:35]\n"
    "ds_read_b128 v[196:199], v210 offset:8192\n"
    "v_mfma_f32_16x16x32_bf16 v[36:39], v[120:123], v[164:167], v[36:39]\n"
    "buffer_load_dwordx4 v213, s[28:31], s50 offen offset:2048 lds\n"
    "v_mfma_f32_16x16x32_bf16 v[40:43], v[120:123], v[168:171], v[40:43]\n"
    "ds_read_b128 v[200:203], v210 offset:16384\n"
    "v_mfma_f32_16x16x32_bf16 v[44:47], v[120:123], v[172:175], v[44:47]\n"
    "ds_read_b128 v[204:207], v215\n"
    "v_mfma_f32_16x16x32_bf16 v[48:51], v[124:127], v[160:163], v[48:51]\n"
    "ds_read_b64_tr_b16 v[148:149], v208 offset:33792\n"
    "v_mfma_f32_16x16x32_bf16 v[52:55], v[124:127], v[164:167], v[52:55]\n"
    "ds_read_b64_tr_b16 v[150:151], v208 offset:34304\n"
    "v_mfma_f32_16x16x32_bf16 v[56:59], v[124:127], v[168:171], v[56:59]\n"
    "ds_read_b64_tr_b16 v[152:153], v208 offset:34816\n"
    "v_mfma_f32_16x16x32_bf16 v[60:63], v[124:127], v[172:175], v[60:63]\n"
    "ds_read_b64_tr_b16 v[154:155], v208 offset:35328\n"
    "ds_read_b64_tr_b16 v[156:157], v208 offset:35840\n"
    "ds_read_b64_tr_b16 v[158:159], v208 offset:36352\n"
    "v_mfma_f32_16x16x32_bf16 v[0:3], v[128:131], v[176:179], v[0:3]\n"
    "v_xor_b32 v211, 64, v210\n"
    "v_add_u32 v223, 24576, v211\n"
    "v_cndmask_b32 v223, v224, v223, s[92:93]\n"
    "s_add_u32 s38, s85, 16384\n"
    "s_mov_b32 m0, s38\n"
    "ds_read_b128 v[160:163], v211\n"
    "v_mfma_f32_16x16x32_bf16 v[4:7], v[128:131], v[180:183], v[4:7]\n"
    "buffer_load_dwordx4 v213, s[28:31], s51 offen lds\n"
    "v_mfma_f32_16x16x32_bf16 v[8:11], v[128:131], v[184:187], v[8:11]\n"
    "ds_read_b64_tr_b16 v[112:113], v208 offset:49152\n"
    "v_mfma_f32_16x16x32_bf16 v[12:15], v[128:131], v[188:191], v[12:15]\n"
    "ds_read_b64_tr_b16 v[114:115], v208 offset:49664\n"
    "v_mfma_f32_16x16x32_bf16 v[16:19], v[132:135], v[176:179], v[16:19]\n"
    "buffer_load_dwordx4 v213, s[28:31], s52 offen offset:1024 lds\n"
    "v_mfma_f32_16x16x32_bf16 v[20:23], v[132:135], v[180:183], v[20:23]\n"
    "ds_read_b128 v[164:167], v211 offset:8192\n"
    "v_mfma_f32_16x16x32_bf16 v[24:27], v[132:135], v[184:187], v[24:27]\n"
    "ds_read_b128 v[168:171], v211 offset:16384\n"
    "v_mfma_f32_16x16x32_bf16 v[28:31], v[132:135], v[188:191], v[28:31]\n"
    "buffer_load_dwordx4 v213, s[28:31], s53 offen offset:2048 lds\n"
    "v_mfma_f32_16x16x32_bf16 v[32:35], v[136:139], v[176:179], v[32:35]\n"
    "ds_read_b128 v[172:175], v223\n"
    "v_mfma_f32_16x16x32_bf16 v[36:39], v[136:139], v[180:183], v[36:39]\n"
    "ds_read_b64_tr_b16 v[116:117], v208 offset:50176\n"
    "v_mfma_f32_16x16x32_bf16 v[40:43], v[136:139], v[184:187], v[40:43]\n"
    "ds_read_b64_tr_b16 v[118:119], v208 offset:50688\n"
    "v_mfma_f32_16x16x32_bf16 v[44:47], v[136:139], v[188:191], v[44:47]\n"
    "ds_read_b64_tr_b16 v[120:121], v208 offset:51200\n"
    "v_mfma_f32_16x16x32_bf16 v[48:51], v[140:143], v[176:179], v[48:51]\n"
    "ds_read_b64_tr_b16 v[122:123], v208 offset:51712\n"
    "v_mfma_f32_16x16x32_bf16 v[52:55], v[140:143], v[180:183], v[52:55]\n"
    "ds_read_b64_tr_b16 v[124:125], v208 offset:52224\n"
    "v_mfma_f32_16x16x32_bf16 v[56:59], v[140:143], v[184:187], v[56:59]\n"
    "ds_read_b64_tr_b16 v[126:127], v208 offset:52736\n"
    "v_mfma_f32_16x16x32_bf16 v[60:63], v[140:143], v[188:191], v[60:63]\n"
    "s_waitcnt lgkmcnt(15)\n"
    "v_mfma_f32_16x16x32_bf16 v[0:3], v[144:147], v[192:195], v[0:3]\n"
    "v_xor_b32 v210, s56, v212\n"
    "v_add_u32 v210, s64, v210\n"
    "v_mfma_f32_16x16x32_bf16 v[4:7], v[144:147], v[196:199], v[4:7]\n"
    "v_add_u32 v215, 24576, v210\n"
    "v_cndmask_b32 v215, v224, v215, s[92:93]\n"
    "s_add_u32 s28, s83, s74\n"
    "s_addc_u32 s29, s84, 0\n"
    "v_mfma_f32_16x16x32_bf16 v[8:11], v[144:147], v[200:203], v[8:11]\n"
    "v_mfma_f32_16x16x32_bf16 v[12:15], v[144:147], v[204:207], v[12:15]\n"
    "s_waitcnt vmcnt(6) lgkmcnt(0)\n"
    "s_barrier\n"
    "ds_read_b128 v[176:179], v210\n"
    "v_mfma_f32_16x16x32_bf16 v[16:19], v[148:151], v[192:195], v[16:19]\n"
    "s_mov_b32 m0, s86\n"
    "ds_read_b64_tr_b16 v[128:129], v209\n"
    "v_mfma_f32_16x16x32_bf16 v[20:23], v[148:151], v[196:199], v[20:23]\n"
    "buffer_load_dwordx4 v213, s[28:31], s48 offen lds\n"
    "v_mfma_f32_16x16x32_bf16 v[24:27], v[148:151], v[200:203], v[24:27]\n"
    "ds_read_b64_tr_b16 v[130:131], v209 offset:512\n"
    "v_mfma_f32_16x16x32_bf16 v[28:31], v[148:151], v[204:207], v[28:31]\n"
    "buffer_load_dwordx4 v213, s[28:31], s49 offen offset:1024 lds\n"
    "v_mfma_f32_16x16x32_bf16 v[32:35], v[152:155], v[192:195], v[32:35]\n"
    "ds_read_b128 v[180:183], v210 offset:8192\n"
    "v_mfma_f32_16x16x32_bf16 v[36:39], v[152:155], v[196:199], v[36:39]\n"
    "buffer_load_dwordx4 v213, s[28:31], s50 offen offset:2048 lds\n"
    "v_mfma_f32_16x16x32_bf16 v[40:43], v[152:155], v[200:203], v[40:43]\n"
    "ds_read_b128 v[184:187], v210 offset:16384\n"
    "v_mfma_f32_16x16x32_bf16 v[44:47], v[152:155], v[204:207], v[44:47]\n"
    "ds_read_b128 v[188:191], v215\n"
    "v_mfma_f32_16x16x32_bf16 v[48:51], v[156:159], v[192:195], v[48:51]\n"
    "ds_read_b64_tr_b16 v[132:133], v209 offset:1024\n"
    "v_mfma_f32_16x16x32_bf16 v[52:55], v[156:159], v[196:199], v[52:55]\n"
    "ds_read_b64_tr_b16 v[134:135], v209 offset:1536\n"
    "v_mfma_f32_16x16x32_bf16 v[56:59], v[156:159], v[200:203], v[56:59]\n"
    "ds_read_b64_tr_b16 v[136:137], v209 offset:2048\n"
    "v_mfma_f32_16x16x32_bf16 v[60:63], v[156:159], v[204:207], v[60:63]\n"
    "ds_read_b64_tr_b16 v[138:139], v209 offset:2560\n"
    "v_mfma_f32_16x16x32_bf16 v[0:3], v[112:115], v[160:163], v[0:3]\n"
    "v_xor_b32 v211, 64, v210\n"
    "v_add_u32 v223, 24576, v211\n"
    "v_cndmask_b32 v223, v224, v223, s[92:93]\n"
    "s_add_u32 s38, s86, 16384\n"
    "s_mov_b32 m0, s38\n"
    "ds_read_b128 v[192:195], v211\n"
    "v_mfma_f32_16x16x32_bf16 v[4:7], v[112:115], v[164:167], v[4:7]\n"
    "buffer_load_dwordx4 v213, s[28:31], s51 offen lds\n"
    "v_mfma_f32_16x16x32_bf16 v[8:11], v[112:115], v[168:171], v[8:11]\n"
    "ds_read_b64_tr_b16 v[144:145], v209 offset:16384\n"
    "v_mfma_f32_16x16x32_bf16 v[12:15], v[112:115], v[172:175], v[12:15]\n"
    "ds_read_b64_tr_b16 v[146:147], v209 offset:16896\n"
    "v_mfma_f32_16x16x32_bf16 v[16:19], v[116:119], v[160:163], v[16:19]\n"
    "buffer_load_dwordx4 v213, s[28:31], s52 offen offset:1024 lds\n"
    "v_mfma_f32_16x16x32_bf16 v[20:23], v[116:119], v[164:167], v[20:23]\n"
    "ds_read_b128 v[196:199], v211 offset:8192\n"
    "v_mfma_f32_16x16x32_bf16 v[24:27], v[116:119], v[168:171], v[24:27]\n"
    "ds_read_b128 v[200:203], v211 offset:16384\n"
    "v_mfma_f32_16x16x32_bf16 v[28:31], v[116:119], v[172:175], v[28:31]\n"
    "buffer_load_dwordx4 v213, s[28:31], s53 offen offset:2048 lds\n"
    "v_mfma_f32_16x16x32_bf16 v[32:35], v[120:123], v[160:163], v[32:35]\n"
    "ds_read_b128 v[204:207], v223\n"
    "v_mfma_f32_16x16x32_bf16 v[36:39], v[120:123], v[164:167], v[36:39]\n"
    "ds_read_b64_tr_b16 v[148:149], v209 offset:17408\n"
    "v_mfma_f32_16x16x32_bf16 v[40:43], v[120:123], v[168:171], v[40:43]\n"
    "ds_read_b64_tr_b16 v[150:151], v209 offset:17920\n"
    "v_mfma_f32_16x16x32_bf16 v[44:47], v[120:123], v[172:175], v[44:47]\n"
    "ds_read_b64_tr_b16 v[152:153], v209 offset:18432\n"
    "v_mfma_f32_16x16x32_bf16 v[48:51], v[124:127], v[160:163], v[48:51]\n"
    "ds_read_b64_tr_b16 v[154:155], v209 offset:18944\n"
    "v_mfma_f32_16x16x32_bf16 v[52:55], v[124:127], v[164:167], v[52:55]\n"
    "v_mfma_f32_16x16x32_bf16 v[56:59], v[124:127], v[168:171], v[56:59]\n"
    "v_mfma_f32_16x16x32_bf16 v[60:63], v[124:127], v[172:175], v[60:63]\n"
    "s_waitcnt lgkmcnt(15)\n"
    "v_mfma_f32_16x16x32_bf16 v[64:67], v[128:131], v[176:179], 0\n"
    "v_xor_b32 v210, s57, v212\n"
    "v_add_u32 v210, s65, v210\n"
    "v_mfma_f32_16x16x32_bf16 v[68:71], v[128:131], v[180:183], 0\n"
    "v_add_u32 v215, 24576, v210\n"
    "v_cndmask_b32 v215, v224, v215, s[92:93]\n"
    "s_add_u32 s28, s83, s75\n"
    "s_addc_u32 s29, s84, 0\n"
    "v_mfma_f32_16x16x32_bf16 v[72:75], v[128:131], v[184:187], 0\n"
    "s_waitcnt lgkmcnt(14)\n"
    "v_mfma_f32_16x16x32_bf16 v[76:79], v[128:131], v[188:191], 0\n"
    "s_waitcnt vmcnt(6) lgkmcnt(0)\n"
    "s_barrier\n"
    "ds_read_b128 v[160:163], v210\n"
    "v_mfma_f32_16x16x32_bf16 v[80:83], v[132:135], v[176:179], 0\n"
    "s_mov_b32 m0, s87\n"
    "ds_read_b64_tr_b16 v[112:113], v208\n"
    "v_mfma_f32_16x16x32_bf16 v[84:87], v[132:135], v[180:183], 0\n"
    "buffer_load_dwordx4 v213, s[28:31], s48 offen lds\n"
    "v_mfma_f32_16x16x32_bf16 v[88:91], v[132:135], v[184:187], 0\n"
    "ds_read_b64_tr_b16 v[114:115], v208 offset:512\n"
    "v_mfma_f32_16x16x32_bf16 v[92:95], v[132:135], v[188:191], 0\n"
    "buffer_load_dwordx4 v213, s[28:31], s49 offen offset:1024 lds\n"
    "v_mfma_f32_16x16x32_bf16 v[96:99], v[136:139], v[176:179], 0\n"
    "ds_read_b128 v[164:167], v210 offset:8192\n"
    "v_mfma_f32_16x16x32_bf16 v[100:103], v[136:139], v[180:183], 0\n"
    "buffer_load_dwordx4 v213, s[28:31], s50 offen offset:2048 lds\n"
    "v_mfma_f32_16x16x32_bf16 v[104:107], v[136:139], v[184:187], 0\n"
    "ds_read_b128 v[168:171], v210 offset:16384\n"
    "v_mfma_f32_16x16x32_bf16 v[108:111], v[136:139], v[188:191], 0\n"
    "ds_read_b128 v[172:175], v215\n"
    "ds_read_b64_tr_b16 v[116:117], v208 offset:1024\n"
    "ds_read_b64_tr_b16 v[118:119], v208 offset:1536\n"
    "ds_read_b64_tr_b16 v[120:121], v208 offset:2048\n"
    "ds_read_b64_tr_b16 v[122:123], v208 offset:2560\n"
    "v_mfma_f32_16x16x32_bf16 v[64:67], v[144:147], v[192:195], v[64:67]\n"
    "v_xor_b32 v211, 64, v210\n"
    "v_add_u32 v223, 24576, v211\n"
    "v_cndmask_b32 v223, v224, v223, s[92:93]\n"
    "s_add_u32 s38, s87, 16384\n"
    "s_mov_b32 m0, s38\n"
    "ds_read_b128 v[176:179], v211\n"
    "v_mfma_f32_16x16x32_bf16 v[68:71], v[144:147], v[196:199], v[68:71]\n"
    "buffer_load_dwordx4 v213, s[28:31], s51 offen lds\n"
    "v_mfma_f32_16x16x32_bf16 v[72:75], v[144:147], v[200:203], v[72:75]\n"
    "ds_read_b64_tr_b16 v[128:129], v208 offset:16384\n"
    "v_mfma_f32_16x16x32_bf16 v[76:79], v[144:147], v[204:207], v[76:79]\n"
    "ds_read_b64_tr_b16 v[130:131], v208 offset:16896\n"
    "v_mfma_f32_16x16x32_bf16 v[80:83], v[148:151], v[192:195], v[80:83]\n"
    "buffer_load_dwordx4 v213, s[28:31], s52 offen offset:1024 lds\n"
    "v_mfma_f32_16x16x32_bf16 v[84:87], v[148:151], v[196:199], v[84:87]\n"
    "ds_read_b128 v[180:183], v211 offset:8192\n"
    "v_mfma_f32_16x16x32_bf16 v[88:91], v[148:151], v[200:203], v[88:91]\n"
    "ds_read_b128 v[184:187], v211 offset:16384\n"
    "v_mfma_f32_16x16x32_bf16 v[92:95], v[148:151], v[204:207], v[92:95]\n"
    "buffer_load_dwordx4 v213, s[28:31], s53 offen offset:2048 lds\n"
    "v_mfma_f32_16x16x32_bf16 v[96:99], v[152:155], v[192:195], v[96:99]\n"
    "ds_read_b128 v[188:191], v223\n"
    "v_mfma_f32_16x16x32_bf16 v[100:103], v[152:155], v[196:199], v[100:103]\n"
    "ds_read_b64_tr_b16 v[132:133], v208 offset:17408\n"
    "v_mfma_f32_16x16x32_bf16 v[104:107], v[152:155], v[200:203], v[104:107]\n"
    "ds_read_b64_tr_b16 v[134:135], v208 offset:17920\n"
    "v_mfma_f32_16x16x32_bf16 v[108:111], v[152:155], v[204:207], v[108:111]\n"
    "ds_read_b64_tr_b16 v[136:137], v208 offset:18432\n"
    "ds_read_b64_tr_b16 v[138:139], v208 offset:18944\n"
    "s_waitcnt lgkmcnt(15)\n"
    "v_mfma_f32_16x16x32_bf16 v[64:67], v[112:115], v[160:163], v[64:67]\n"
    "v_xor_b32 v210, s58, v212\n"
    "v_add_u32 v210, s66, v210\n"
    "v_mfma_f32_16x16x32_bf16 v[68:71], v[112:115], v[164:167], v[68:71]\n"
    "v_add_u32 v215, 24576, v210\n"
    "v_cndmask_b32 v215, v224, v215, s[92:93]\n"
    "s_add_u32 s28, s83, s76\n"
    "s_addc_u32 s29, s84, 0\n"
    "v_mfma_f32_16x16x32_bf16 v[72:75], v[112:115], v[168:171], v[72:75]\n"
    "s_waitcnt lgkmcnt(14)\n"
    "v_mfma_f32_16x16x32_bf16 v[76:79], v[112:115], v[172:175], v[76:79]\n"
    "s_waitcnt vmcnt(6) lgkmcnt(0)\n"
    "s_barrier\n"
    "ds_read_b128 v[192:195], v210\n"
    "v_mfma_f32_16x16x32_bf16 v[80:83], v[116:119], v[160:163], v[80:83]\n"
    "s_mov_b32 m0, s85\n"
    "ds_read_b64_tr_b16 v[144:145], v208 offset:32768\n"
    "v_mfma_f32_16x16x32_bf16 v[84:87], v[116:119], v[164:167], v[84:87]\n"
    "buffer_load_dwordx4 v213, s[28:31], s48 offen lds\n"
    "v_mfma_f32_16x16x32_bf16 v[88:91], v[116:119], v[168:171], v[88:91]\n"
    "ds_read_b64_tr_b16 v[146:147], v208 offset:33280\n"
    "v_mfma_f32_16x16x32_bf16 v[92:95], v[116:119], v[172:175], v[92:95]\n"
    "buffer_load_dwordx4 v213, s[28:31], s49 offen offset:1024 lds\n"
    "v_mfma_f32_16x16x32_bf16 v[96:99], v[120:123], v[160:163], v[96:99]\n"
    "ds_read_b128 v[196:199], v210 offset:8192\n"
    "v_mfma_f32_16x16x32_bf16 v[100:103], v[120:123], v[164:167], v[100:103]\n"
    "buffer_load_dwordx4 v213, s[28:31], s50 offen offset:2048 lds\n"
    "v_mfma_f32_16x16x32_bf16 v[104:107], v[120:123], v[168:171], v[104:107]\n"
    "ds_read_b128 v[200:203], v210 offset:16384\n"
    "v_mfma_f32_16x16x32_bf16 v[108:111], v[120:123], v[172:175], v[108:111]\n"
    "ds_read_b128 v[204:207], v215\n"
    "ds_read_b64_tr_b16 v[148:149], v208 offset:33792\n"
    "ds_read_b64_tr_b16 v[150:151], v208 offset:34304\n"
    "ds_read_b64_tr_b16 v[152:153], v208 offset:34816\n"
    "ds_read_b64_tr_b16 v[154:155], v208 offset:35328\n"
    "v_mfma_f32_16x16x32_bf16 v[64:67], v[128:131], v[176:179], v[64:67]\n"
    "v_xor_b32 v211, 64, v210\n"
    "v_add_u32 v223, 24576, v211\n"
    "v_cndmask_b32 v223, v224, v223, s[92:93]\n"
    "s_add_u32 s38, s85, 16384\n"
    "s_mov_b32 m0, s38\n"
    "ds_read_b128 v[160:163], v211\n"
    "v_mfma_f32_16x16x32_bf16 v[68:71], v[128:131], v[180:183], v[68:71]\n"
    "buffer_load_dwordx4 v213, s[28:31], s51 offen lds\n"
    "v_mfma_f32_16x16x32_bf16 v[72:75], v[128:131], v[184:187], v[72:75]\n"
    "ds_read_b64_tr_b16 v[112:113], v208 offset:49152\n"
    "v_mfma_f32_16x16x32_bf16 v[76:79], v[128:131], v[188:191], v[76:79]\n"
    "ds_read_b64_tr_b16 v[114:115], v208 offset:49664\n"
    "v_mfma_f32_16x16x32_bf16 v[80:83], v[132:135], v[176:179], v[80:83]\n"
    "buffer_load_dwordx4 v213, s[28:31], s52 offen offset:1024 lds\n"
    "v_mfma_f32_16x16x32_bf16 v[84:87], v[132:135], v[180:183], v[84:87]\n"
    "ds_read_b128 v[164:167], v211 offset:8192\n"
    "v_mfma_f32_16x16x32_bf16 v[88:91], v[132:135], v[184:187], v[88:91]\n"
    "ds_read_b128 v[168:171], v211 offset:16384\n"
    "v_mfma_f32_16x16x32_bf16 v[92:95], v[132:135], v[188:191], v[92:95]\n"
    "buffer_load_dwordx4 v213, s[28:31], s53 offen offset:2048 lds\n"
    "v_mfma_f32_16x16x32_bf16 v[96:99], v[136:139], v[176:179], v[96:99]\n"
    "ds_read_b128 v[172:175], v223\n"
    "v_mfma_f32_16x16x32_bf16 v[100:103], v[136:139], v[180:183], v[100:103]\n"
    "ds_read_b64_tr_b16 v[116:117], v208 offset:50176\n"
    "v_mfma_f32_16x16x32_bf16 v[104:107], v[136:139], v[184:187], v[104:107]\n"
    "ds_read_b64_tr_b16 v[118:119], v208 offset:50688\n"
    "v_mfma_f32_16x16x32_bf16 v[108:111], v[136:139], v[188:191], v[108:111]\n"
    "ds_read_b64_tr_b16 v[120:121], v208 offset:51200\n"
    "ds_read_b64_tr_b16 v[122:123], v208 offset:51712\n"
    "s_waitcnt lgkmcnt(15)\n"
    "v_mfma_f32_16x16x32_bf16 v[64:67], v[144:147], v[192:195], v[64:67]\n"
    "v_xor_b32 v210, s59, v212\n"
    "v_add_u32 v210, s67, v210\n"
    "v_mfma_f32_16x16x32_bf16 v[68:71], v[144:147], v[196:199], v[68:71]\n"
    "v_add_u32 v215, 24576, v210\n"
    "v_cndmask_b32 v215, v224, v215, s[92:93]\n"
    "s_add_u32 s28, s83, s77\n"
    "s_addc_u32 s29, s84, 0\n"
    "v_mfma_f32_16x16x32_bf16 v[72:75], v[144:147], v[200:203], v[72:75]\n"
    "s_waitcnt lgkmcnt(14)\n"
    "v_mfma_f32_16x16x32_bf16 v[76:79], v[144:147], v[204:207], v[76:79]\n"
    "s_waitcnt vmcnt(6) lgkmcnt(0)\n"
    "s_barrier\n"
    "ds_read_b128 v[176:179], v210\n"
    "v_mfma_f32_16x16x32_bf16 v[80:83], v[148:151], v[192:195], v[80:83]\n"
    "s_mov_b32 m0, s86\n"
    "ds_read_b64_tr_b16 v[128:129], v209\n"
    "v_mfma_f32_16x16x32_bf16 v[84:87], v[148:151], v[196:199], v[84:87]\n"
    "buffer_load_dwordx4 v213, s[28:31], s48 offen lds\n"
    "v_mfma_f32_16x16x32_bf16 v[88:91], v[148:151], v[200:203], v[88:91]\n"
    "ds_read_b64_tr_b16 v[130:131], v209 offset:512\n"
    "v_mfma_f32_16x16x32_bf16 v[92:95], v[148:151], v[204:207], v[92:95]\n"
    "buffer_load_dwordx4 v213, s[28:31], s49 offen offset:1024 lds\n"
    "v_mfma_f32_16x16x32_bf16 v[96:99], v[152:155], v[192:195], v[96:99]\n"
    "ds_read_b128 v[180:183], v210 offset:8192\n"
    "v_mfma_f32_16x16x32_bf16 v[100:103], v[152:155], v[196:199], v[100:103]\n"
    "buffer_load_dwordx4 v213, s[28:31], s50 offen offset:2048 lds\n"
    "v_mfma_f32_16x16x32_bf16 v[104:107], v[152:155], v[200:203], v[104:107]\n"
    "ds_read_b128 v[184:187], v210 offset:16384\n"
    "v_mfma_f32_16x16x32_bf16 v[108:111], v[152:155], v[204:207], v[108:111]\n"
    "ds_read_b128 v[188:191], v215\n"
    "ds_read_b64_tr_b16 v[132:133], v209 offset:1024\n"
    "ds_read_b64_tr_b16 v[134:135], v209 offset:1536\n"
    "ds_read_b64_tr_b16 v[136:137], v209 offset:2048\n"
    "ds_read_b64_tr_b16 v[138:139], v209 offset:2560\n"
    "v_mfma_f32_16x16x32_bf16 v[64:67], v[112:115], v[160:163], v[64:67]\n"
    "v_xor_b32 v211, 64, v210\n"
    "v_add_u32 v223, 24576, v211\n"
    "v_cndmask_b32 v223, v224, v223, s[92:93]\n"
    "s_add_u32 s38, s86, 16384\n"
    "s_mov_b32 m0, s38\n"
    "ds_read_b128 v[192:195], v211\n"
    "v_mfma_f32_16x16x32_bf16 v[68:71], v[112:115], v[164:167], v[68:71]\n"
    "buffer_load_dwordx4 v213, s[28:31], s51 offen lds\n"
    "v_mfma_f32_16x16x32_bf16 v[72:75], v[112:115], v[168:171], v[72:75]\n"
    "ds_read_b64_tr_b16 v[144:145], v209 offset:16384\n"
    "v_mfma_f32_16x16x32_bf16 v[76:79], v[112:115], v[172:175], v[76:79]\n"
    "ds_read_b64_tr_b16 v[146:147], v209 offset:16896\n"
    "v_mfma_f32_16x16x32_bf16 v[80:83], v[116:119], v[160:163], v[80:83]\n"
    "buffer_load_dwordx4 v213, s[28:31], s52 offen offset:1024 lds\n"
    "v_mfma_f32_16x16x32_bf16 v[84:87], v[116:119], v[164:167], v[84:87]\n"
    "ds_read_b128 v[196:199], v211 offset:8192\n"
    "v_mfma_f32_16x16x32_bf16 v[88:91], v[116:119], v[168:171], v[88:91]\n"
    "ds_read_b128 v[200:203], v211 offset:16384\n"
    "v_mfma_f32_16x16x32_bf16 v[92:95], v[116:119], v[172:175], v[92:95]\n"
    "buffer_load_dwordx4 v213, s[28:31], s53 offen offset:2048 lds\n"
    "v_mfma_f32_16x16x32_bf16 v[96:99], v[120:123], v[160:163], v[96:99]\n"
    "ds_read_b128 v[204:207], v223\n"
    "v_mfma_f32_16x16x32_bf16 v[100:103], v[120:123], v[164:167], v[100:103]\n"
    "ds_read_b64_tr_b16 v[148:149], v209 offset:17408\n"
    "v_mfma_f32_16x16x32_bf16 v[104:107], v[120:123], v[168:171], v[104:107]\n"
    "ds_read_b64_tr_b16 v[150:151], v209 offset:17920\n"
    "v_mfma_f32_16x16x32_bf16 v[108:111], v[120:123], v[172:175], v[108:111]\n"
    "ds_read_b64_tr_b16 v[152:153], v209 offset:18432\n"
    "ds_read_b64_tr_b16 v[154:155], v209 offset:18944\n"
    "s_waitcnt lgkmcnt(15)\n"
    "v_mfma_f32_16x16x32_bf16 v[64:67], v[128:131], v[176:179], v[64:67]\n"
    "v_xor_b32 v210, s60, v212\n"
    "v_add_u32 v210, s68, v210\n"
    "v_mfma_f32_16x16x32_bf16 v[68:71], v[128:131], v[180:183], v[68:71]\n"
    "v_add_u32 v215, 24576, v210\n"
    "v_cndmask_b32 v215, v224, v215, s[92:93]\n"
    "s_add_u32 s28, s83, s78\n"
    "s_addc_u32 s29, s84, 0\n"
    "v_mfma_f32_16x16x32_bf16 v[72:75], v[128:131], v[184:187], v[72:75]\n"
    "s_waitcnt lgkmcnt(14)\n"
    "v_mfma_f32_16x16x32_bf16 v[76:79], v[128:131], v[188:191], v[76:79]\n"
    "s_waitcnt vmcnt(6) lgkmcnt(0)\n"
    "s_barrier\n"
    "ds_read_b128 v[160:163], v210\n"
    "v_mfma_f32_16x16x32_bf16 v[80:83], v[132:135], v[176:179], v[80:83]\n"
    "s_mov_b32 m0, s87\n"
    "ds_read_b64_tr_b16 v[112:113], v208\n"
    "v_mfma_f32_16x16x32_bf16 v[84:87], v[132:135], v[180:183], v[84:87]\n"
    "buffer_load_dwordx4 v213, s[28:31], s48 offen lds\n"
    "v_mfma_f32_16x16x32_bf16 v[88:91], v[132:135], v[184:187], v[88:91]\n"
    "ds_read_b64_tr_b16 v[114:115], v208 offset:512\n"
    "v_mfma_f32_16x16x32_bf16 v[92:95], v[132:135], v[188:191], v[92:95]\n"
    "buffer_load_dwordx4 v213, s[28:31], s49 offen offset:1024 lds\n"
    "v_mfma_f32_16x16x32_bf16 v[96:99], v[136:139], v[176:179], v[96:99]\n"
    "ds_read_b128 v[164:167], v210 offset:8192\n"
    "v_mfma_f32_16x16x32_bf16 v[100:103], v[136:139], v[180:183], v[100:103]\n"
    "buffer_load_dwordx4 v213, s[28:31], s50 offen offset:2048 lds\n"
    "v_mfma_f32_16x16x32_bf16 v[104:107], v[136:139], v[184:187], v[104:107]\n"
    "ds_read_b128 v[168:171], v210 offset:16384\n"
    "v_mfma_f32_16x16x32_bf16 v[108:111], v[136:139], v[188:191], v[108:111]\n"
    "ds_read_b128 v[172:175], v215\n"
    "ds_read_b64_tr_b16 v[116:117], v208 offset:1024\n"
    "ds_read_b64_tr_b16 v[118:119], v208 offset:1536\n"
    "ds_read_b64_tr_b16 v[120:121], v208 offset:2048\n"
    "ds_read_b64_tr_b16 v[122:123], v208 offset:2560\n"
    "v_mfma_f32_16x16x32_bf16 v[64:67], v[144:147], v[192:195], v[64:67]\n"
    "v_xor_b32 v211, 64, v210\n"
    "v_add_u32 v223, 24576, v211\n"
    "v_cndmask_b32 v223, v224, v223, s[92:93]\n"
    "s_add_u32 s38, s87, 16384\n"
    "s_mov_b32 m0, s38\n"
    "ds_read_b128 v[176:179], v211\n"
    "v_mfma_f32_16x16x32_bf16 v[68:71], v[144:147], v[196:199], v[68:71]\n"
    "buffer_load_dwordx4 v213, s[28:31], s51 offen lds\n"
    "v_mfma_f32_16x16x32_bf16 v[72:75], v[144:147], v[200:203], v[72:75]\n"
    "ds_read_b64_tr_b16 v[128:129], v208 offset:16384\n"
    "v_mfma_f32_16x16x32_bf16 v[76:79], v[144:147], v[204:207], v[76:79]\n"
    "ds_read_b64_tr_b16 v[130:131], v208 offset:16896\n"
    "v_mfma_f32_16x16x32_bf16 v[80:83], v[148:151], v[192:195], v[80:83]\n"
    "buffer_load_dwordx4 v213, s[28:31], s52 offen offset:1024 lds\n"
    "v_mfma_f32_16x16x32_bf16 v[84:87], v[148:151], v[196:199], v[84:87]\n"
    "ds_read_b128 v[180:183], v211 offset:8192\n"
    "v_mfma_f32_16x16x32_bf16 v[88:91], v[148:151], v[200:203], v[88:91]\n"
    "ds_read_b128 v[184:187], v211 offset:16384\n"
    "v_mfma_f32_16x16x32_bf16 v[92:95], v[148:151], v[204:207], v[92:95]\n"
    "buffer_load_dwordx4 v213, s[28:31], s53 offen offset:2048 lds\n"
    "v_mfma_f32_16x16x32_bf16 v[96:99], v[152:155], v[192:195], v[96:99]\n"
    "ds_read_b128 v[188:191], v223\n"
    "v_mfma_f32_16x16x32_bf16 v[100:103], v[152:155], v[196:199], v[100:103]\n"
    "ds_read_b64_tr_b16 v[132:133], v208 offset:17408\n"
    "v_mfma_f32_16x16x32_bf16 v[104:107], v[152:155], v[200:203], v[104:107]\n"
    "ds_read_b64_tr_b16 v[134:135], v208 offset:17920\n"
    "v_mfma_f32_16x16x32_bf16 v[108:111], v[152:155], v[204:207], v[108:111]\n"
    "ds_read_b64_tr_b16 v[136:137], v208 offset:18432\n"
    "ds_read_b64_tr_b16 v[138:139], v208 offset:18944\n"
    "s_waitcnt lgkmcnt(15)\n"
    "v_mfma_f32_16x16x32_bf16 v[64:67], v[112:115], v[160:163], v[64:67]\n"
    "v_xor_b32 v210, s61, v212\n"
    "v_add_u32 v210, s69, v210\n"
    "v_mfma_f32_16x16x32_bf16 v[68:71], v[112:115], v[164:167], v[68:71]\n"
    "v_add_u32 v215, 24576, v210\n"
    "v_cndmask_b32 v215, v224, v215, s[92:93]\n"
    "s_add_u32 s28, s83, s79\n"
    "s_addc_u32 s29, s84, 0\n"
    "v_mfma_f32_16x16x32_bf16 v[72:75], v[112:115], v[168:171], v[72:75]\n"
    "s_waitcnt lgkmcnt(14)\n"
    "v_mfma_f32_16x16x32_bf16 v[76:79], v[112:115], v[172:175], v[76:79]\n"
    "s_waitcnt vmcnt(6) lgkmcnt(0)\n"
    "s_barrier\n"
    "ds_read_b128 v[192:195], v210\n"
    "v_mfma_f32_16x16x32_bf16 v[80:83], v[116:119], v[160:163], v[80:83]\n"
    "s_mov_b32 m0, s85\n"
    "ds_read_b64_tr_b16 v[144:145], v208 offset:32768\n"
    "v_mfma_f32_16x16x32_bf16 v[84:87], v[116:119], v[164:167], v[84:87]\n"
    "buffer_load_dwordx4 v213, s[28:31], s48 offen lds\n"
    "v_mfma_f32_16x16x32_bf16 v[88:91], v[116:119], v[168:171], v[88:91]\n"
    "ds_read_b64_tr_b16 v[146:147], v208 offset:33280\n"
    "v_mfma_f32_16x16x32_bf16 v[92:95], v[116:119], v[172:175], v[92:95]\n"
    "buffer_load_dwordx4 v213, s[28:31], s49 offen offset:1024 lds\n"
    "v_mfma_f32_16x16x32_bf16 v[96:99], v[120:123], v[160:163], v[96:99]\n"
    "ds_read_b128 v[196:199], v210 offset:8192\n"
    "v_mfma_f32_16x16x32_bf16 v[100:103], v[120:123], v[164:167], v[100:103]\n"
    "buffer_load_dwordx4 v213, s[28:31], s50 offen offset:2048 lds\n"
    "v_mfma_f32_16x16x32_bf16 v[104:107], v[120:123], v[168:171], v[104:107]\n"
    "ds_read_b128 v[200:203], v210 offset:16384\n"
    "v_mfma_f32_16x16x32_bf16 v[108:111], v[120:123], v[172:175], v[108:111]\n"
    "ds_read_b128 v[204:207], v215\n"
    "ds_read_b64_tr_b16 v[148:149], v208 offset:33792\n"
    "ds_read_b64_tr_b16 v[150:151], v208 offset:34304\n"
    "ds_read_b64_tr_b16 v[152:153], v208 offset:34816\n"
    "ds_read_b64_tr_b16 v[154:155], v208 offset:35328\n"
    "v_mfma_f32_16x16x32_bf16 v[64:67], v[128:131], v[176:179], v[64:67]\n"
    "v_xor_b32 v211, 64, v210\n"
    "v_add_u32 v223, 24576, v211\n"
    "v_cndmask_b32 v223, v224, v223, s[92:93]\n"
    "s_add_u32 s38, s85, 16384\n"
    "s_mov_b32 m0, s38\n"
    "ds_read_b128 v[160:163], v211\n"
    "v_mfma_f32_16x16x32_bf16 v[68:71], v[128:131], v[180:183], v[68:71]\n"
    "buffer_load_dwordx4 v213, s[28:31], s51 offen lds\n"
    "v_mfma_f32_16x16x32_bf16 v[72:75], v[128:131], v[184:187], v[72:75]\n"
    "ds_read_b64_tr_b16 v[112:113], v208 offset:49152\n"
    "v_mfma_f32_16x16x32_bf16 v[76:79], v[128:131], v[188:191], v[76:79]\n"
    "ds_read_b64_tr_b16 v[114:115], v208 offset:49664\n"
    "v_mfma_f32_16x16x32_bf16 v[80:83], v[132:135], v[176:179], v[80:83]\n"
    "buffer_load_dwordx4 v213, s[28:31], s52 offen offset:1024 lds\n"
    "v_mfma_f32_16x16x32_bf16 v[84:87], v[132:135], v[180:183], v[84:87]\n"
    "ds_read_b128 v[164:167], v211 offset:8192\n"
    "v_mfma_f32_16x16x32_bf16 v[88:91], v[132:135], v[184:187], v[88:91]\n"
    "ds_read_b128 v[168:171], v211 offset:16384\n"
    "v_mfma_f32_16x16x32_bf16 v[92:95], v[132:135], v[188:191], v[92:95]\n"
    "buffer_load_dwordx4 v213, s[28:31], s53 offen offset:2048 lds\n"
    "v_mfma_f32_16x16x32_bf16 v[96:99], v[136:139], v[176:179], v[96:99]\n"
    "ds_read_b128 v[172:175], v223\n"
    "v_mfma_f32_16x16x32_bf16 v[100:103], v[136:139], v[180:183], v[100:103]\n"
    "ds_read_b64_tr_b16 v[116:117], v208 offset:50176\n"
    "v_mfma_f32_16x16x32_bf16 v[104:107], v[136:139], v[184:187], v[104:107]\n"
    "ds_read_b64_tr_b16 v[118:119], v208 offset:50688\n"
    "v_mfma_f32_16x16x32_bf16 v[108:111], v[136:139], v[188:191], v[108:111]\n"
    "ds_read_b64_tr_b16 v[120:121], v208 offset:51200\n"
    "ds_read_b64_tr_b16 v[122:123], v208 offset:51712\n"
    "s_waitcnt lgkmcnt(15)\n"
    "v_mfma_f32_16x16x32_bf16 v[64:67], v[144:147], v[192:195], v[64:67]\n"
    "v_xor_b32 v210, s62, v212\n"
    "v_add_u32 v210, s70, v210\n"
    "v_mfma_f32_16x16x32_bf16 v[68:71], v[144:147], v[196:199], v[68:71]\n"
    "v_add_u32 v215, 24576, v210\n"
    "v_cndmask_b32 v215, v224, v215, s[92:93]\n"
    "v_mfma_f32_16x16x32_bf16 v[72:75], v[144:147], v[200:203], v[72:75]\n"
    "s_waitcnt lgkmcnt(14)\n"
    "v_mfma_f32_16x16x32_bf16 v[76:79], v[144:147], v[204:207], v[76:79]\n"
    "s_waitcnt vmcnt(6) lgkmcnt(0)\n"
    "s_barrier\n"
    "ds_read_b128 v[176:179], v210\n"
    "v_mfma_f32_16x16x32_bf16 v[80:83], v[148:151], v[192:195], v[80:83]\n"
    "ds_read_b64_tr_b16 v[128:129], v209\n"
    "v_mfma_f32_16x16x32_bf16 v[84:87], v[148:151], v[196:199], v[84:87]\n"
    "ds_read_b64_tr_b16 v[130:131], v209 offset:512\n"
    "v_mfma_f32_16x16x32_bf16 v[88:91], v[148:151], v[200:203], v[88:91]\n"
    "ds_read_b128 v[180:183], v210 offset:8192\n"
    "v_mfma_f32_16x16x32_bf16 v[92:95], v[148:151], v[204:207], v[92:95]\n"
    "ds_read_b128 v[184:187], v210 offset:16384\n"
    "v_mfma_f32_16x16x32_bf16 v[96:99], v[152:155], v[192:195], v[96:99]\n"
    "ds_read_b128 v[188:191], v215\n"
    "v_mfma_f32_16x16x32_bf16 v[100:103], v[152:155], v[196:199], v[100:103]\n"
    "ds_read_b64_tr_b16 v[132:133], v209 offset:1024\n"
    "v_mfma_f32_16x16x32_bf16 v[104:107], v[152:155], v[200:203], v[104:107]\n"
    "ds_read_b64_tr_b16 v[134:135], v209 offset:1536\n"
    "v_mfma_f32_16x16x32_bf16 v[108:111], v[152:155], v[204:207], v[108:111]\n"
    "ds_read_b64_tr_b16 v[136:137], v209 offset:2048\n"
    "ds_read_b64_tr_b16 v[138:139], v209 offset:2560\n"
    "v_mfma_f32_16x16x32_bf16 v[64:67], v[112:115], v[160:163], v[64:67]\n"
    "v_xor_b32 v211, 64, v210\n"
    "v_add_u32 v223, 24576, v211\n"
    "v_cndmask_b32 v223, v224, v223, s[92:93]\n"
    "ds_read_b128 v[192:195], v211\n"
    "v_mfma_f32_16x16x32_bf16 v[68:71], v[112:115], v[164:167], v[68:71]\n"
    "ds_read_b64_tr_b16 v[144:145], v209 offset:16384\n"
    "v_cvt_pk_bf16_f32 v0, v0, v1\n"
    "v_mfma_f32_16x16x32_bf16 v[72:75], v[112:115], v[168:171], v[72:75]\n"
    "ds_read_b64_tr_b16 v[146:147], v209 offset:16896\n"
    "v_cvt_pk_bf16_f32 v1, v2, v3\n"
    "v_mfma_f32_16x16x32_bf16 v[76:79], v[112:115], v[172:175], v[76:79]\n"
    "ds_read_b128 v[196:199], v211 offset:8192\n"
    "v_cvt_pk_bf16_f32 v2, v4, v5\n"
    "v_mfma_f32_16x16x32_bf16 v[80:83], v[116:119], v[160:163], v[80:83]\n"
    "ds_read_b128 v[200:203], v211 offset:16384\n"
    "v_cvt_pk_bf16_f32 v3, v6, v7\n"
    "v_mfma_f32_16x16x32_bf16 v[84:87], v[116:119], v[164:167], v[84:87]\n"
    "ds_read_b128 v[204:207], v223\n"
    "v_mfma_f32_16x16x32_bf16 v[88:91], v[116:119], v[168:171], v[88:91]\n"
    "ds_read_b64_tr_b16 v[148:149], v209 offset:17408\n"
    "v_mfma_f32_16x16x32_bf16 v[92:95], v[116:119], v[172:175], v[92:95]\n"
    "ds_read_b64_tr_b16 v[150:151], v209 offset:17920\n"
    "v_mfma_f32_16x16x32_bf16 v[96:99], v[120:123], v[160:163], v[96:99]\n"
    "ds_read_b64_tr_b16 v[152:153], v209 offset:18432\n"
    "v_mfma_f32_16x16x32_bf16 v[100:103], v[120:123], v[164:167], v[100:103]\n"
    "ds_read_b64_tr_b16 v[154:155], v209 offset:18944\n"
    "v_mfma_f32_16x16x32_bf16 v[104:107], v[120:123], v[168:171], v[104:107]\n"
    "s_mov_b32 s38, 0\n"
    "buffer_store_dwordx4 v[0:3], v214, s[32:35], s38 offen sc1\n"
    "v_mfma_f32_16x16x32_bf16 v[108:111], v[120:123], v[172:175], v[108:111]\n"
    "v_cvt_pk_bf16_f32 v8, v8, v9\n"
    "v_cvt_pk_bf16_f32 v9, v10, v11\n"
    "v_cvt_pk_bf16_f32 v10, v12, v13\n"
    "v_cvt_pk_bf16_f32 v11, v14, v15\n"
    "s_mov_b32 s38, 0x2000\n"
    "buffer_store_dwordx4 v[8:11], v214, s[32:35], s38 offen sc1\n"
    "s_waitcnt lgkmcnt(15)\n"
    "v_mfma_f32_16x16x32_bf16 v[64:67], v[128:131], v[176:179], v[64:67]\n"
    "v_xor_b32 v210, s63, v212\n"
    "v_add_u32 v210, s71, v210\n"
    "v_mfma_f32_16x16x32_bf16 v[68:71], v[128:131], v[180:183], v[68:71]\n"
    "v_add_u32 v215, 24576, v210\n"
    "v_cndmask_b32 v215, v224, v215, s[92:93]\n"
    "v_cvt_pk_bf16_f32 v16, v16, v17\n"
    "v_mfma_f32_16x16x32_bf16 v[72:75], v[128:131], v[184:187], v[72:75]\n"
    "v_cvt_pk_bf16_f32 v17, v18, v19\n"
    "v_cvt_pk_bf16_f32 v18, v20, v21\n"
    "s_waitcnt lgkmcnt(14)\n"
    "v_mfma_f32_16x16x32_bf16 v[76:79], v[128:131], v[188:191], v[76:79]\n"
    "s_waitcnt vmcnt(2) lgkmcnt(0)\n"
    "s_barrier\n"
    "ds_read_b128 v[160:163], v210\n"
    "v_cvt_pk_bf16_f32 v19, v22, v23\n"
    "v_mfma_f32_16x16x32_bf16 v[80:83], v[132:135], v[176:179], v[80:83]\n"
    "ds_read_b64_tr_b16 v[112:113], v208\n"
    "s_mov_b32 s38, 0x4000\n"
    "buffer_store_dwordx4 v[16:19], v214, s[32:35], s38 offen sc1\n"
    "v_mfma_f32_16x16x32_bf16 v[84:87], v[132:135], v[180:183], v[84:87]\n"
    "ds_read_b64_tr_b16 v[114:115], v208 offset:512\n"
    "v_cvt_pk_bf16_f32 v24, v24, v25\n"
    "v_mfma_f32_16x16x32_bf16 v[88:91], v[132:135], v[184:187], v[88:91]\n"
    "ds_read_b128 v[164:167], v210 offset:8192\n"
    "v_cvt_pk_bf16_f32 v25, v26, v27\n"
    "v_mfma_f32_16x16x32_bf16 v[92:95], v[132:135], v[188:191], v[92:95]\n"
    "ds_read_b128 v[168:171], v210 offset:16384\n"
    "v_cvt_pk_bf16_f32 v26, v28, v29\n"
    "v_mfma_f32_16x16x32_bf16 v[96:99], v[136:139], v[176:179], v[96:99]\n"
    "ds_read_b128 v[172:175], v215\n"
    "v_cvt_pk_bf16_f32 v27, v30, v31\n"
    "v_mfma_f32_16x16x32_bf16 v[100:103], v[136:139], v[180:183], v[100:103]\n"
    "ds_read_b64_tr_b16 v[116:117], v208 offset:1024\n"
    "s_mov_b32 s38, 0x6000\n"
    "buffer_store_dwordx4 v[24:27], v214, s[32:35], s38 offen sc1\n"
    "v_mfma_f32_16x16x32_bf16 v[104:107], v[136:139], v[184:187], v[104:107]\n"
    "ds_read_b64_tr_b16 v[118:119], v208 offset:1536\n"
    "v_mfma_f32_16x16x32_bf16 v[108:111], v[136:139], v[188:191], v[108:111]\n"
    "ds_read_b64_tr_b16 v[120:121], v208 offset:2048\n"
    "ds_read_b64_tr_b16 v[122:123], v208 offset:2560\n"
    "v_mfma_f32_16x16x32_bf16 v[64:67], v[144:147], v[192:195], v[64:67]\n"
    "v_xor_b32 v211, 64, v210\n"
    "v_add_u32 v223, 24576, v211\n"
    "v_cndmask_b32 v223, v224, v223, s[92:93]\n"
    "ds_read_b128 v[176:179], v211\n"
    "v_mfma_f32_16x16x32_bf16 v[68:71], v[144:147], v[196:199], v[68:71]\n"
    "ds_read_b64_tr_b16 v[128:129], v208 offset:16384\n"
    "v_cvt_pk_bf16_f32 v32, v32, v33\n"
    "v_mfma_f32_16x16x32_bf16 v[72:75], v[144:147], v[200:203], v[72:75]\n"
    "ds_read_b64_tr_b16 v[130:131], v208 offset:16896\n"
    "v_cvt_pk_bf16_f32 v33, v34, v35\n"
    "v_mfma_f32_16x16x32_bf16 v[76:79], v[144:147], v[204:207], v[76:79]\n"
    "ds_read_b128 v[180:183], v211 offset:8192\n"
    "v_cvt_pk_bf16_f32 v34, v36, v37\n"
    "v_mfma_f32_16x16x32_bf16 v[80:83], v[148:151], v[192:195], v[80:83]\n"
    "ds_read_b128 v[184:187], v211 offset:16384\n"
    "v_cvt_pk_bf16_f32 v35, v38, v39\n"
    "v_mfma_f32_16x16x32_bf16 v[84:87], v[148:151], v[196:199], v[84:87]\n"
    "ds_read_b128 v[188:191], v223\n"
    "v_mfma_f32_16x16x32_bf16 v[88:91], v[148:151], v[200:203], v[88:91]\n"
    "ds_read_b64_tr_b16 v[132:133], v208 offset:17408\n"
    "v_mfma_f32_16x16x32_bf16 v[92:95], v[148:151], v[204:207], v[92:95]\n"
    "ds_read_b64_tr_b16 v[134:135], v208 offset:17920\n"
    "v_mfma_f32_16x16x32_bf16 v[96:99], v[152:155], v[192:195], v[96:99]\n"
    "ds_read_b64_tr_b16 v[136:137], v208 offset:18432\n"
    "v_mfma_f32_16x16x32_bf16 v[100:103], v[152:155], v[196:199], v[100:103]\n"
    "ds_read_b64_tr_b16 v[138:139], v208 offset:18944\n"
    "v_mfma_f32_16x16x32_bf16 v[104:107], v[152:155], v[200:203], v[104:107]\n"
    "s_mov_b32 s38, 0x8000\n"
    "buffer_store_dwordx4 v[32:35], v214, s[32:35], s38 offen sc1\n"
    "v_mfma_f32_16x16x32_bf16 v[108:111], v[152:155], v[204:207], v[108:111]\n"
    "v_cvt_pk_bf16_f32 v40, v40, v41\n"
    "v_cvt_pk_bf16_f32 v41, v42, v43\n"
    "v_cvt_pk_bf16_f32 v42, v44, v45\n"
    "v_cvt_pk_bf16_f32 v43, v46, v47\n"
    "s_mov_b32 s38, 0xa000\n"
    "buffer_store_dwordx4 v[40:43], v214, s[32:35], s38 offen sc1\n"
    "s_waitcnt lgkmcnt(15)\n"
    "v_mfma_f32_16x16x32_bf16 v[64:67], v[112:115], v[160:163], v[64:67]\n"
    "v_mfma_f32_16x16x32_bf16 v[68:71], v[112:115], v[164:167], v[68:71]\n"
    "v_cvt_pk_bf16_f32 v48, v48, v49\n"
    "v_cvt_pk_bf16_f32 v49, v50, v51\n"
    "v_mfma_f32_16x16x32_bf16 v[72:75], v[112:115], v[168:171], v[72:75]\n"
    "v_cvt_pk_bf16_f32 v50, v52, v53\n"
    "v_cvt_pk_bf16_f32 v51, v54, v55\n"
    "s_waitcnt lgkmcnt(14)\n"
    "v_mfma_f32_16x16x32_bf16 v[76:79], v[112:115], v[172:175], v[76:79]\n"
    "s_mov_b32 s38, 0xc000\n"
    "buffer_store_dwordx4 v[48:51], v214, s[32:35], s38 offen sc1\n"
    "s_waitcnt lgkmcnt(12)\n"
    "v_mfma_f32_16x16x32_bf16 v[80:83], v[116:119], v[160:163], v[80:83]\n"
    "v_cvt_pk_bf16_f32 v56, v56, v57\n"
    "v_cvt_pk_bf16_f32 v57, v58, v59\n"
    "v_mfma_f32_16x16x32_bf16 v[84:87], v[116:119], v[164:167], v[84:87]\n"
    "v_cvt_pk_bf16_f32 v58, v60, v61\n"
    "v_cvt_pk_bf16_f32 v59, v62, v63\n"
    "v_mfma_f32_16x16x32_bf16 v[88:91], v[116:119], v[168:171], v[88:91]\n"
    "s_mov_b32 s38, 0xe000\n"
    "buffer_store_dwordx4 v[56:59], v214, s[32:35], s38 offen sc1\n"
    "v_mfma_f32_16x16x32_bf16 v[92:95], v[116:119], v[172:175], v[92:95]\n"
    "s_waitcnt lgkmcnt(10)\n"
    "v_mfma_f32_16x16x32_bf16 v[96:99], v[120:123], v[160:163], v[96:99]\n"
    "v_mfma_f32_16x16x32_bf16 v[100:103], v[120:123], v[164:167], v[100:103]\n"
    "v_mfma_f32_16x16x32_bf16 v[104:107], v[120:123], v[168:171], v[104:107]\n"
    "v_mfma_f32_16x16x32_bf16 v[108:111], v[120:123], v[172:175], v[108:111]\n"
    "s_waitcnt lgkmcnt(7)\n"
    "v_mfma_f32_16x16x32_bf16 v[64:67], v[128:131], v[176:179], v[64:67]\n"
    "s_waitcnt lgkmcnt(6)\n"
    "v_mfma_f32_16x16x32_bf16 v[68:71], v[128:131], v[180:183], v[68:71]\n"
    "s_waitcnt lgkmcnt(5)\n"
    "v_mfma_f32_16x16x32_bf16 v[72:75], v[128:131], v[184:187], v[72:75]\n"
    "s_waitcnt lgkmcnt(4)\n"
    "v_mfma_f32_16x16x32_bf16 v[76:79], v[128:131], v[188:191], v[76:79]\n"
    "s_waitcnt lgkmcnt(2)\n"
    "v_mfma_f32_16x16x32_bf16 v[80:83], v[132:135], v[176:179], v[80:83]\n"
    "v_mfma_f32_16x16x32_bf16 v[84:87], v[132:135], v[180:183], v[84:87]\n"
    "v_mfma_f32_16x16x32_bf16 v[88:91], v[132:135], v[184:187], v[88:91]\n"
    "v_mfma_f32_16x16x32_bf16 v[92:95], v[132:135], v[188:191], v[92:95]\n"
    "s_waitcnt lgkmcnt(0)\n"
    "v_mfma_f32_16x16x32_bf16 v[96:99], v[136:139], v[176:179], v[96:99]\n"
    "v_mfma_f32_16x16x32_bf16 v[100:103], v[136:139], v[180:183], v[100:103]\n"
    "v_mfma_f32_16x16x32_bf16 v[104:107], v[136:139], v[184:187], v[104:107]\n"
    "v_mfma_f32_16x16x32_bf16 v[108:111], v[136:139], v[188:191], v[108:111]\n"
    "v_cvt_pk_bf16_f32 v64, v64, v65\n"
    "v_cvt_pk_bf16_f32 v65, v66, v67\n"
    "v_cvt_pk_bf16_f32 v66, v68, v69\n"
    "v_cvt_pk_bf16_f32 v67, v70, v71\n"
    "s_mov_b32 s38, 0x10000\n"
    "buffer_store_dwordx4 v[64:67], v214, s[32:35], s38 offen sc1\n"
    "v_cvt_pk_bf16_f32 v72, v72, v73\n"
    "v_cvt_pk_bf16_f32 v73, v74, v75\n"
    "v_cvt_pk_bf16_f32 v74, v76, v77\n"
    "v_cvt_pk_bf16_f32 v75, v78, v79\n"
    "s_mov_b32 s38, 0x12000\n"
    "buffer_store_dwordx4 v[72:75], v214, s[32:35], s38 offen sc1\n"
    "v_cvt_pk_bf16_f32 v80, v80, v81\n"
    "v_cvt_pk_bf16_f32 v81, v82, v83\n"
    "v_cvt_pk_bf16_f32 v82, v84, v85\n"
    "v_cvt_pk_bf16_f32 v83, v86, v87\n"
    "s_mov_b32 s38, 0x14000\n"
    "buffer_store_dwordx4 v[80:83], v214, s[32:35], s38 offen sc1\n"
    "v_cvt_pk_bf16_f32 v88, v88, v89\n"
    "v_cvt_pk_bf16_f32 v89, v90, v91\n"
    "v_cvt_pk_bf16_f32 v90, v92, v93\n"
    "v_cvt_pk_bf16_f32 v91, v94, v95\n"
    "s_mov_b32 s38, 0x16000\n"
    "buffer_store_dwordx4 v[88:91], v214, s[32:35], s38 offen sc1\n"
    "v_cvt_pk_bf16_f32 v96, v96, v97\n"
    "v_cvt_pk_bf16_f32 v97, v98, v99\n"
    "v_cvt_pk_bf16_f32 v98, v100, v101\n"
    "v_cvt_pk_bf16_f32 v99, v102, v103\n"
    "s_mov_b32 s38, 0x18000\n"
    "buffer_store_dwordx4 v[96:99], v214, s[32:35], s38 offen sc1\n"
    "v_cvt_pk_bf16_f32 v104, v104, v105\n"
    "v_cvt_pk_bf16_f32 v105, v106, v107\n"
    "v_cvt_pk_bf16_f32 v106, v108, v109\n"
    "v_cvt_pk_bf16_f32 v107, v110, v111\n"
    "s_mov_b32 s38, 0x1a000\n"
    "buffer_store_dwordx4 v[104:107], v214, s[32:35], s38 offen sc1\n"
    "s_branch L_end_%=\n"
    "L_safe_%=:\n"
    "s_waitcnt vmcnt(0) lgkmcnt(0)\n"
    "s_barrier\n"
    "v_and_b32 v244, 15, v254\n"
    "v_lshrrev_b32 v245, 4, v254\n"
    "v_and_b32 v239, 1, v245\n"
    "v_lshlrev_b32 v239, 1, v239\n"
    "v_bfe_u32 v240, v254, 3, 1\n"
    "v_add_u32 v239, v239, v240\n"
    "v_lshlrev_b32 v246, 5, v239\n"
    "v_and_b32 v239, 3, v254\n"
    "v_lshlrev_b32 v239, 3, v239\n"
    "v_add_u32 v246, v246, v239\n"
    "v_bfe_u32 v247, v254, 2, 2\n"
    "v_lshrrev_b32 v239, 3, v254\n"
    "v_and_b32 v240, 7, v254\n"
    "v_and_b32 v241, 6, v239\n"
    "v_xor_b32 v241, v241, v240\n"
    "v_lshlrev_b32 v241, 4, v241\n"
    "v_lshlrev_b32 v239, 9, v239\n"
    "v_add_u32 v234, v239, v241\n"
    "v_and_b32 v240, 6, v244\n"
    "v_xor_b32 v240, v240, v245\n"
    "v_lshlrev_b32 v240, 4, v240\n"
    "v_lshrrev_b32 v239, 3, v244\n"
    "v_lshlrev_b32 v239, 12, v239\n"
    "v_and_b32 v241, 7, v244\n"
    "v_lshlrev_b32 v241, 7, v241\n"
    "v_add_u32 v239, v239, v241\n"
    "v_add_u32 v239, v239, v240\n"
    "s_lshl_b32 s38, s36, 14\n"
    "v_add_u32 v223, s38, v239\n"
    "v_xor_b32 v224, 64, v223\n"
    "v_lshrrev_b32 v239, 1, v245\n"
    "v_lshlrev_b32 v239, 10, v239\n"
    "v_and_b32 v240, 1, v245\n"
    "v_lshlrev_b32 v240, 2, v240\n"
    "v_add_u32 v240, v240, v247\n"
    "v_lshlrev_b32 v240, 7, v240\n"
    "v_add_u32 v239, v239, v240\n"
    "s_add_u32 s38, s38, 65536\n"
    "v_add_u32 v239, s38, v239\n"
    "v_xor_b32 v240, 0, v246\n"
    "v_add_u32 v225, v239, v240\n"
    "v_xor_b32 v240, 32, v246\n"
    "v_add_u32 v226, v239, v240\n"
    "v_xor_b32 v240, 64, v246\n"
    "v_add_u32 v227, v239, v240\n"
    "v_xor_b32 v240, 96, v246\n"
    "v_add_u32 v228, v239, v240\n"
    "s_lshl_b32 s90, s36, 14\n"
    "s_add_u32 s20, s8, s90\n"
    "s_addc_u32 s21, s9, 0\n"
    "s_sub_u32 s20, s20, 2688\n"
    "s_subb_u32 s21, s21, 0\n"
    "s_and_b32 s21, s21, 0xffff\n"
    "s_lshl_b32 s39, s36, 7\n"
    "s_add_u32 s24, s10, s39\n"
    "s_addc_u32 s25, s11, 0\n"
    "s_and_b32 s25, s25, 0xffff\n"
    "s_mov_b32 s40, 0xa80\n"
    "s_mov_b32 s41, 0x700\n"
    "s_mov_b32 s42, 0x380\n"
    "s_mov_b32 s43, 0\n"
    "s_mov_b32 s44, 0x1a80\n"
    "s_mov_b32 s45, 0x1700\n"
    "s_mov_b32 s46, 0x1380\n"
    "s_mov_b32 s47, 0x1000\n"
    "s_mov_b32 s48, 0x2a80\n"
    "s_mov_b32 s49, 0x2700\n"
    "s_mov_b32 s50, 0x2380\n"
    "s_mov_b32 s51, 0x2000\n"
    "s_mov_b32 s52, 0x3a80\n"
    "s_mov_b32 s53, 0x3700\n"
    "s_mov_b32 s54, 0x3380\n"
    "s_mov_b32 s55, 0x3000\n"
    "s_mov_b32 s56, 0\n"
    "s_mov_b32 s57, 0xc00\n"
    "s_mov_b32 s58, 0x1800\n"
    "s_mov_b32 s59, 0x2400\n"
    "s_mov_b32 s60, 0x4000\n"
    "s_mov_b32 s61, 0x4c00\n"
    "s_mov_b32 s62, 0x5800\n"
    "s_mov_b32 s63, 0x6400\n"
    "s_mov_b32 s64, 0x8000\n"
    "s_mov_b32 s65, 0x8c00\n"
    "s_mov_b32 s66, 0x9800\n"
    "s_mov_b32 s67, 0xa400\n"
    "s_mov_b32 s68, 0xc000\n"
    "s_mov_b32 s69, 0xcc00\n"
    "s_mov_b32 s70, 0xd800\n"
    "s_mov_b32 s71, 0xe400\n"
    "s_add_u32 s72, s90, 0\n"
    "s_sub_u32 s39, 4, s36\n"
    "s_and_b32 s39, s39, 3\n"
    "s_lshl_b32 s39, s39, 12\n"
    "s_add_u32 s39, s39, s90\n"
    "s_add_u32 s76, s39, 65536\n"
    "s_add_u32 s73, s90, 4096\n"
    "s_sub_u32 s39, 5, s36\n"
    "s_and_b32 s39, s39, 3\n"
    "s_lshl_b32 s39, s39, 12\n"
    "s_add_u32 s39, s39, s90\n"
    "s_add_u32 s77, s39, 65536\n"
    "s_add_u32 s74, s90, 8192\n"
    "s_sub_u32 s39, 6, s36\n"
    "s_and_b32 s39, s39, 3\n"
    "s_lshl_b32 s39, s39, 12\n"
    "s_add_u32 s39, s39, s90\n"
    "s_add_u32 s78, s39, 65536\n"
    "s_add_u32 s75, s90, 12288\n"
    "s_sub_u32 s39, 7, s36\n"
    "s_and_b32 s39, s39, 3\n"
    "s_lshl_b32 s39, s39, 12\n"
    "s_add_u32 s39, s39, s90\n"
    "s_add_u32 s79, s39, 65536\n"
    "v_mov_b32 v216, 0xff800000\n"
    "v_mov_b32 v217, 0xff800000\n"
    "v_mov_b32 v218, 0xff800000\n"
    "v_mov_b32 v219, 0xff800000\n"
    "v_mov_b32 v220, 0xff800000\n"
    "v_mov_b32 v221, 0xff800000\n"
    "v_mov_b32 v222, 0xff800000\n"
    "s_mov_b32 s37, 16\n"
    "L_max_%=:\n"
    "s_mov_b32 m0, s72\n"
    "s_nop 0\n"
    "buffer_load_dwordx4 v234, s[20:23], s40 offen nt lds\n"
    "buffer_load_dwordx4 v234, s[20:23], s41 offen offset:1024 nt lds\n"
    "buffer_load_dwordx4 v234, s[20:23], s42 offen offset:2048 nt lds\n"
    "buffer_load_dwordx4 v234, s[20:23], s43 offen offset:3072 nt lds\n"
    "s_mov_b32 m0, s73\n"
    "s_nop 0\n"
    "buffer_load_dwordx4 v234, s[20:23], s44 offen nt lds\n"
    "buffer_load_dwordx4 v234, s[20:23], s45 offen offset:1024 nt lds\n"
    "buffer_load_dwordx4 v234, s[20:23], s46 offen offset:2048 nt lds\n"
    "buffer_load_dwordx4 v234, s[20:23], s47 offen offset:3072 nt lds\n"
    "s_mov_b32 m0, s74\n"
    "s_nop 0\n"
    "buffer_load_dwordx4 v234, s[20:23], s48 offen nt lds\n"
    "buffer_load_dwordx4 v234, s[20:23], s49 offen offset:1024 nt lds\n"
    "buffer_load_dwordx4 v234, s[20:23], s50 offen offset:2048 nt lds\n"
    "buffer_load_dwordx4 v234, s[20:23], s51 offen offset:3072 nt lds\n"
    "s_mov_b32 m0, s75\n"
    "s_nop 0\n"
    "buffer_load_dwordx4 v234, s[20:23], s52 offen nt lds\n"
    "buffer_load_dwordx4 v234, s[20:23], s53 offen offset:1024 nt lds\n"
    "buffer_load_dwordx4 v234, s[20:23], s54 offen offset:2048 nt lds\n"
    "buffer_load_dwordx4 v234, s[20:23], s55 offen offset:3072 nt lds\n"
    "s_add_u32 s20, s20, 65536\n"
    "s_addc_u32 s21, s21, 0\n"
    "s_waitcnt vmcnt(0)\n"
    "ds_read_b128 v[112:115], v223\n"
    "ds_read_b128 v[116:119], v224\n"
    "ds_read_b128 v[120:123], v223 offset:1024\n"
    "ds_read_b128 v[124:127], v224 offset:1024\n"
    "ds_read_b128 v[128:131], v223 offset:2048\n"
    "ds_read_b128 v[132:135], v224 offset:2048\n"
    "ds_read_b128 v[136:139], v223 offset:3072\n"
    "ds_read_b128 v[140:143], v224 offset:3072\n"
    "ds_read_b128 v[144:147], v223 offset:8192\n"
    "ds_read_b128 v[148:151], v224 offset:8192\n"
    "ds_read_b128 v[152:155], v223 offset:9216\n"
    "ds_read_b128 v[156:159], v224 offset:9216\n"
    "ds_read_b128 v[160:163], v223 offset:10240\n"
    "ds_read_b128 v[164:167], v224 offset:10240\n"
    "ds_read_b128 v[168:171], v223 offset:11264\n"
    "ds_read_b128 v[172:175], v224 offset:11264\n"
    "s_waitcnt lgkmcnt(15)\n"
    "v_mfma_f32_16x16x32_bf16 v[176:179], v[112:115], a[0:3], 0\n"
    "s_waitcnt lgkmcnt(14)\n"
    "v_mfma_f32_16x16x32_bf16 v[176:179], v[116:119], a[4:7], v[176:179]\n"
    "s_waitcnt lgkmcnt(13)\n"
    "v_mfma_f32_16x16x32_bf16 v[176:179], v[120:123], a[8:11], v[176:179]\n"
    "s_waitcnt lgkmcnt(12)\n"
    "v_mfma_f32_16x16x32_bf16 v[176:179], v[124:127], a[12:15], v[176:179]\n"
    "s_waitcnt lgkmcnt(11)\n"
    "v_mfma_f32_16x16x32_bf16 v[176:179], v[128:131], a[16:19], v[176:179]\n"
    "s_waitcnt lgkmcnt(10)\n"
    "v_mfma_f32_16x16x32_bf16 v[176:179], v[132:135], a[20:23], v[176:179]\n"
    "s_waitcnt lgkmcnt(9)\n"
    "v_mfma_f32_16x16x32_bf16 v[176:179], v[136:139], a[24:27], v[176:179]\n"
    "s_waitcnt lgkmcnt(8)\n"
    "v_mfma_f32_16x16x32_bf16 v[176:179], v[140:143], a[28:31], v[176:179]\n"
    "s_waitcnt lgkmcnt(7)\n"
    "v_mfma_f32_16x16x32_bf16 v[180:183], v[144:147], a[0:3], 0\n"
    "s_waitcnt lgkmcnt(6)\n"
    "v_mfma_f32_16x16x32_bf16 v[180:183], v[148:151], a[4:7], v[180:183]\n"
    "s_waitcnt lgkmcnt(5)\n"
    "v_mfma_f32_16x16x32_bf16 v[180:183], v[152:155], a[8:11], v[180:183]\n"
    "s_waitcnt lgkmcnt(4)\n"
    "v_mfma_f32_16x16x32_bf16 v[180:183], v[156:159], a[12:15], v[180:183]\n"
    "s_waitcnt lgkmcnt(3)\n"
    "v_mfma_f32_16x16x32_bf16 v[180:183], v[160:163], a[16:19], v[180:183]\n"
    "s_waitcnt lgkmcnt(2)\n"
    "v_mfma_f32_16x16x32_bf16 v[180:183], v[164:167], a[20:23], v[180:183]\n"
    "s_waitcnt lgkmcnt(1)\n"
    "v_mfma_f32_16x16x32_bf16 v[180:183], v[168:171], a[24:27], v[180:183]\n"
    "s_waitcnt lgkmcnt(0)\n"
    "v_mfma_f32_16x16x32_bf16 v[180:183], v[172:175], a[28:31], v[180:183]\n"
    "v_max3_f32 v216, v216, v176, v177\n"
    "v_max3_f32 v216, v216, v178, v179\n"
    "v_mfma_f32_16x16x32_bf16 v[184:187], v[112:115], a[32:35], 0\n"
    "v_mfma_f32_16x16x32_bf16 v[184:187], v[116:119], a[36:39], v[184:187]\n"
    "v_mfma_f32_16x16x32_bf16 v[184:187], v[120:123], a[40:43], v[184:187]\n"
    "v_mfma_f32_16x16x32_bf16 v[184:187], v[124:127], a[44:47], v[184:187]\n"
    "v_mfma_f32_16x16x32_bf16 v[184:187], v[128:131], a[48:51], v[184:187]\n"
    "v_mfma_f32_16x16x32_bf16 v[184:187], v[132:135], a[52:55], v[184:187]\n"
    "v_mfma_f32_16x16x32_bf16 v[184:187], v[136:139], a[56:59], v[184:187]\n"
    "v_mfma_f32_16x16x32_bf16 v[184:187], v[140:143], a[60:63], v[184:187]\n"
    "v_max3_f32 v216, v216, v180, v181\n"
    "v_max3_f32 v216, v216, v182, v183\n"
    "v_mfma_f32_16x16x32_bf16 v[188:191], v[144:147], a[32:35], 0\n"
    "v_mfma_f32_16x16x32_bf16 v[188:191], v[148:151], a[36:39], v[188:191]\n"
    "v_mfma_f32_16x16x32_bf16 v[188:191], v[152:155], a[40:43], v[188:191]\n"
    "v_mfma_f32_16x16x32_bf16 v[188:191], v[156:159], a[44:47], v[188:191]\n"
    "v_mfma_f32_16x16x32_bf16 v[188:191], v[160:163], a[48:51], v[188:191]\n"
    "v_mfma_f32_16x16x32_bf16 v[188:191], v[164:167], a[52:55], v[188:191]\n"
    "v_mfma_f32_16x16x32_bf16 v[188:191], v[168:171], a[56:59], v[188:191]\n"
    "v_mfma_f32_16x16x32_bf16 v[188:191], v[172:175], a[60:63], v[188:191]\n"
    "v_max3_f32 v217, v217, v184, v185\n"
    "v_max3_f32 v217, v217, v186, v187\n"
    "v_mfma_f32_16x16x32_bf16 v[176:179], v[112:115], a[64:67], 0\n"
    "v_mfma_f32_16x16x32_bf16 v[176:179], v[116:119], a[68:71], v[176:179]\n"
    "v_mfma_f32_16x16x32_bf16 v[176:179], v[120:123], a[72:75], v[176:179]\n"
    "v_mfma_f32_16x16x32_bf16 v[176:179], v[124:127], a[76:79], v[176:179]\n"
    "v_mfma_f32_16x16x32_bf16 v[176:179], v[128:131], a[80:83], v[176:179]\n"
    "v_mfma_f32_16x16x32_bf16 v[176:179], v[132:135], a[84:87], v[176:179]\n"
    "v_mfma_f32_16x16x32_bf16 v[176:179], v[136:139], a[88:91], v[176:179]\n"
    "v_mfma_f32_16x16x32_bf16 v[176:179], v[140:143], a[92:95], v[176:179]\n"
    "v_max3_f32 v217, v217, v188, v189\n"
    "v_max3_f32 v217, v217, v190, v191\n"
    "v_mfma_f32_16x16x32_bf16 v[180:183], v[144:147], a[64:67], 0\n"
    "v_mfma_f32_16x16x32_bf16 v[180:183], v[148:151], a[68:71], v[180:183]\n"
    "v_mfma_f32_16x16x32_bf16 v[180:183], v[152:155], a[72:75], v[180:183]\n"
    "v_mfma_f32_16x16x32_bf16 v[180:183], v[156:159], a[76:79], v[180:183]\n"
    "v_mfma_f32_16x16x32_bf16 v[180:183], v[160:163], a[80:83], v[180:183]\n"
    "v_mfma_f32_16x16x32_bf16 v[180:183], v[164:167], a[84:87], v[180:183]\n"
    "v_mfma_f32_16x16x32_bf16 v[180:183], v[168:171], a[88:91], v[180:183]\n"
    "v_mfma_f32_16x16x32_bf16 v[180:183], v[172:175], a[92:95], v[180:183]\n"
    "v_max3_f32 v218, v218, v176, v177\n"
    "v_max3_f32 v218, v218, v178, v179\n"
    "v_mfma_f32_16x16x32_bf16 v[184:187], v[112:115], a[96:99], 0\n"
    "v_mfma_f32_16x16x32_bf16 v[184:187], v[116:119], a[100:103], v[184:187]\n"
    "v_mfma_f32_16x16x32_bf16 v[184:187], v[120:123], a[104:107], v[184:187]\n"
    "v_mfma_f32_16x16x32_bf16 v[184:187], v[124:127], a[108:111], v[184:187]\n"
    "v_mfma_f32_16x16x32_bf16 v[184:187], v[128:131], a[112:115], v[184:187]\n"
    "v_mfma_f32_16x16x32_bf16 v[184:187], v[132:135], a[116:119], v[184:187]\n"
    "v_mfma_f32_16x16x32_bf16 v[184:187], v[136:139], a[120:123], v[184:187]\n"
    "v_mfma_f32_16x16x32_bf16 v[184:187], v[140:143], a[124:127], v[184:187]\n"
    "v_max3_f32 v218, v218, v180, v181\n"
    "v_max3_f32 v218, v218, v182, v183\n"
    "v_mfma_f32_16x16x32_bf16 v[188:191], v[144:147], a[96:99], 0\n"
    "v_mfma_f32_16x16x32_bf16 v[188:191], v[148:151], a[100:103], v[188:191]\n"
    "v_mfma_f32_16x16x32_bf16 v[188:191], v[152:155], a[104:107], v[188:191]\n"
    "v_mfma_f32_16x16x32_bf16 v[188:191], v[156:159], a[108:111], v[188:191]\n"
    "v_mfma_f32_16x16x32_bf16 v[188:191], v[160:163], a[112:115], v[188:191]\n"
    "v_mfma_f32_16x16x32_bf16 v[188:191], v[164:167], a[116:119], v[188:191]\n"
    "v_mfma_f32_16x16x32_bf16 v[188:191], v[168:171], a[120:123], v[188:191]\n"
    "v_mfma_f32_16x16x32_bf16 v[188:191], v[172:175], a[124:127], v[188:191]\n"
    "v_max3_f32 v219, v219, v184, v185\n"
    "v_max3_f32 v219, v219, v186, v187\n"
    "v_mfma_f32_16x16x32_bf16 v[176:179], v[112:115], a[128:131], 0\n"
    "v_mfma_f32_16x16x32_bf16 v[176:179], v[116:119], a[132:135], v[176:179]\n"
    "v_mfma_f32_16x16x32_bf16 v[176:179], v[120:123], a[136:139], v[176:179]\n"
    "v_mfma_f32_16x16x32_bf16 v[176:179], v[124:127], a[140:143], v[176:179]\n"
    "v_mfma_f32_16x16x32_bf16 v[176:179], v[128:131], a[144:147], v[176:179]\n"
    "v_mfma_f32_16x16x32_bf16 v[176:179], v[132:135], a[148:151], v[176:179]\n"
    "v_mfma_f32_16x16x32_bf16 v[176:179], v[136:139], a[152:155], v[176:179]\n"
    "v_mfma_f32_16x16x32_bf16 v[176:179], v[140:143], a[156:159], v[176:179]\n"
    "v_max3_f32 v219, v219, v188, v189\n"
    "v_max3_f32 v219, v219, v190, v191\n"
    "v_mfma_f32_16x16x32_bf16 v[180:183], v[144:147], a[128:131], 0\n"
    "v_mfma_f32_16x16x32_bf16 v[180:183], v[148:151], a[132:135], v[180:183]\n"
    "v_mfma_f32_16x16x32_bf16 v[180:183], v[152:155], a[136:139], v[180:183]\n"
    "v_mfma_f32_16x16x32_bf16 v[180:183], v[156:159], a[140:143], v[180:183]\n"
    "v_mfma_f32_16x16x32_bf16 v[180:183], v[160:163], a[144:147], v[180:183]\n"
    "v_mfma_f32_16x16x32_bf16 v[180:183], v[164:167], a[148:151], v[180:183]\n"
    "v_mfma_f32_16x16x32_bf16 v[180:183], v[168:171], a[152:155], v[180:183]\n"
    "v_mfma_f32_16x16x32_bf16 v[180:183], v[172:175], a[156:159], v[180:183]\n"
    "v_max3_f32 v220, v220, v176, v177\n"
    "v_max3_f32 v220, v220, v178, v179\n"
    "v_mfma_f32_16x16x32_bf16 v[184:187], v[112:115], a[160:163], 0\n"
    "v_mfma_f32_16x16x32_bf16 v[184:187], v[116:119], a[164:167], v[184:187]\n"
    "v_mfma_f32_16x16x32_bf16 v[184:187], v[120:123], a[168:171], v[184:187]\n"
    "v_mfma_f32_16x16x32_bf16 v[184:187], v[124:127], a[172:175], v[184:187]\n"
    "v_mfma_f32_16x16x32_bf16 v[184:187], v[128:131], a[176:179], v[184:187]\n"
    "v_mfma_f32_16x16x32_bf16 v[184:187], v[132:135], a[180:183], v[184:187]\n"
    "v_mfma_f32_16x16x32_bf16 v[184:187], v[136:139], a[184:187], v[184:187]\n"
    "v_mfma_f32_16x16x32_bf16 v[184:187], v[140:143], a[188:191], v[184:187]\n"
    "v_max3_f32 v220, v220, v180, v181\n"
    "v_max3_f32 v220, v220, v182, v183\n"
    "v_mfma_f32_16x16x32_bf16 v[188:191], v[144:147], a[160:163], 0\n"
    "v_mfma_f32_16x16x32_bf16 v[188:191], v[148:151], a[164:167], v[188:191]\n"
    "v_mfma_f32_16x16x32_bf16 v[188:191], v[152:155], a[168:171], v[188:191]\n"
    "v_mfma_f32_16x16x32_bf16 v[188:191], v[156:159], a[172:175], v[188:191]\n"
    "v_mfma_f32_16x16x32_bf16 v[188:191], v[160:163], a[176:179], v[188:191]\n"
    "v_mfma_f32_16x16x32_bf16 v[188:191], v[164:167], a[180:183], v[188:191]\n"
    "v_mfma_f32_16x16x32_bf16 v[188:191], v[168:171], a[184:187], v[188:191]\n"
    "v_mfma_f32_16x16x32_bf16 v[188:191], v[172:175], a[188:191], v[188:191]\n"
    "v_max3_f32 v221, v221, v184, v185\n"
    "v_max3_f32 v221, v221, v186, v187\n"
    "v_mfma_f32_16x16x32_bf16 v[176:179], v[112:115], a[192:195], 0\n"
    "v_mfma_f32_16x16x32_bf16 v[176:179], v[116:119], a[196:199], v[176:179]\n"
    "v_mfma_f32_16x16x32_bf16 v[176:179], v[120:123], a[200:203], v[176:179]\n"
    "v_mfma_f32_16x16x32_bf16 v[176:179], v[124:127], a[204:207], v[176:179]\n"
    "v_mfma_f32_16x16x32_bf16 v[176:179], v[128:131], a[208:211], v[176:179]\n"
    "v_mfma_f32_16x16x32_bf16 v[176:179], v[132:135], a[212:215], v[176:179]\n"
    "v_mfma_f32_16x16x32_bf16 v[176:179], v[136:139], a[216:219], v[176:179]\n"
    "v_mfma_f32_16x16x32_bf16 v[176:179], v[140:143], a[220:223], v[176:179]\n"
    "v_max3_f32 v221, v221, v188, v189\n"
    "v_max3_f32 v221, v221, v190, v191\n"
    "v_mfma_f32_16x16x32_bf16 v[180:183], v[144:147], a[192:195], 0\n"
    "v_mfma_f32_16x16x32_bf16 v[180:183], v[148:151], a[196:199], v[180:183]\n"
    "v_mfma_f32_16x16x32_bf16 v[180:183], v[152:155], a[200:203], v[180:183]\n"
    "v_mfma_f32_16x16x32_bf16 v[180:183], v[156:159], a[204:207], v[180:183]\n"
    "v_mfma_f32_16x16x32_bf16 v[180:183], v[160:163], a[208:211], v[180:183]\n"
    "v_mfma_f32_16x16x32_bf16 v[180:183], v[164:167], a[212:215], v[180:183]\n"
    "v_mfma_f32_16x16x32_bf16 v[180:183], v[168:171], a[216:219], v[180:183]\n"
    "v_mfma_f32_16x16x32_bf16 v[180:183], v[172:175], a[220:223], v[180:183]\n"
    "v_max3_f32 v222, v222, v176, v177\n"
    "v_max3_f32 v222, v222, v178, v179\n"
    "s_nop 5\n"
    "v_max3_f32 v222, v222, v180, v181\n"
    "v_max3_f32 v222, v222, v182, v183\n"
    "s_sub_u32 s37, s37, 1\n"
    "s_cmp_lg_u32 s37, 0\n"
    "s_cbranch_scc1 L_max_%=\n"
    "v_mov_b32 v247, v216\n"
    "s_nop 1\n"
    "v_permlane32_swap_b32 v216, v247\n"
    "v_max_f32 v216, v216, v247\n"
    "v_mov_b32 v247, v216\n"
    "s_nop 1\n"
    "v_permlane16_swap_b32 v216, v247\n"
    "v_max_f32 v216, v216, v247\n"
    "v_mov_b32 v247, v217\n"
    "s_nop 1\n"
    "v_permlane32_swap_b32 v217, v247\n"
    "v_max_f32 v217, v217, v247\n"
    "v_mov_b32 v247, v217\n"
    "s_nop 1\n"
    "v_permlane16_swap_b32 v217, v247\n"
    "v_max_f32 v217, v217, v247\n"
    "v_mov_b32 v247, v218\n"
    "s_nop 1\n"
    "v_permlane32_swap_b32 v218, v247\n"
    "v_max_f32 v218, v218, v247\n"
    "v_mov_b32 v247, v218\n"
    "s_nop 1\n"
    "v_permlane16_swap_b32 v218, v247\n"
    "v_max_f32 v218, v218, v247\n"
    "v_mov_b32 v247, v219\n"
    "s_nop 1\n"
    "v_permlane32_swap_b32 v219, v247\n"
    "v_max_f32 v219, v219, v247\n"
    "v_mov_b32 v247, v219\n"
    "s_nop 1\n"
    "v_permlane16_swap_b32 v219, v247\n"
    "v_max_f32 v219, v219, v247\n"
    "v_mov_b32 v247, v220\n"
    "s_nop 1\n"
    "v_permlane32_swap_b32 v220, v247\n"
    "v_max_f32 v220, v220, v247\n"
    "v_mov_b32 v247, v220\n"
    "s_nop 1\n"
    "v_permlane16_swap_b32 v220, v247\n"
    "v_max_f32 v220, v220, v247\n"
    "v_mov_b32 v247, v221\n"
    "s_nop 1\n"
    "v_permlane32_swap_b32 v221, v247\n"
    "v_max_f32 v221, v221, v247\n"
    "v_mov_b32 v247, v221\n"
    "s_nop 1\n"
    "v_permlane16_swap_b32 v221, v247\n"
    "v_max_f32 v221, v221, v247\n"
    "v_mov_b32 v247, v222\n"
    "s_nop 1\n"
    "v_permlane32_swap_b32 v222, v247\n"
    "v_max_f32 v222, v222, v247\n"
    "v_mov_b32 v247, v222\n"
    "s_nop 1\n"
    "v_permlane16_swap_b32 v222, v247\n"
    "v_max_f32 v222, v222, v247\n"
    "v_and_b32 v248, 15, v254\n"
    "v_lshlrev_b32 v248, 4, v248\n"
    "v_add_u32 v248, 159744, v248\n"
    "s_lshl_b32 s38, s36, 2\n"
    "v_add_u32 v249, s38, v248\n"
    "ds_write_b32 v249, v216\n"
    "ds_write_b32 v249, v217 offset:256\n"
    "ds_write_b32 v249, v218 offset:512\n"
    "ds_write_b32 v249, v219 offset:768\n"
    "ds_write_b32 v249, v220 offset:1024\n"
    "ds_write_b32 v249, v221 offset:1280\n"
    "ds_write_b32 v249, v222 offset:1536\n"
    "s_waitcnt lgkmcnt(0)\n"
    "s_barrier\n"
    "ds_read_b128 v[176:179], v248\n"
    "ds_read_b128 v[180:183], v248 offset:256\n"
    "ds_read_b128 v[184:187], v248 offset:512\n"
    "ds_read_b128 v[188:191], v248 offset:768\n"
    "ds_read_b128 v[192:195], v248 offset:1024\n"
    "ds_read_b128 v[196:199], v248 offset:1280\n"
    "ds_read_b128 v[200:203], v248 offset:1536\n"
    "s_waitcnt lgkmcnt(6)\n"
    "v_max3_f32 v216, v176, v177, v178\n"
    "v_max_f32 v216, v216, v179\n"
    "s_waitcnt lgkmcnt(5)\n"
    "v_max3_f32 v217, v180, v181, v182\n"
    "v_max_f32 v217, v217, v183\n"
    "s_waitcnt lgkmcnt(4)\n"
    "v_max3_f32 v218, v184, v185, v186\n"
    "v_max_f32 v218, v218, v187\n"
    "s_waitcnt lgkmcnt(3)\n"
    "v_max3_f32 v219, v188, v189, v190\n"
    "v_max_f32 v219, v219, v191\n"
    "s_waitcnt lgkmcnt(2)\n"
    "v_max3_f32 v220, v192, v193, v194\n"
    "v_max_f32 v220, v220, v195\n"
    "s_waitcnt lgkmcnt(1)\n"
    "v_max3_f32 v221, v196, v197, v198\n"
    "v_max_f32 v221, v221, v199\n"
    "s_waitcnt lgkmcnt(0)\n"
    "v_max3_f32 v222, v200, v201, v202\n"
    "v_max_f32 v222, v222, v203\n"
    "s_waitcnt vmcnt(0) lgkmcnt(0)\n"
    "s_barrier\n"
    "s_lshl_b32 s90, s36, 14\n"
    "s_add_u32 s20, s8, s90\n"
    "s_addc_u32 s21, s9, 0\n"
    "s_sub_u32 s20, s20, 2688\n"
    "s_subb_u32 s21, s21, 0\n"
    "s_and_b32 s21, s21, 0xffff\n"
    "s_lshl_b32 s39, s36, 7\n"
    "s_add_u32 s24, s10, s39\n"
    "s_addc_u32 s25, s11, 0\n"
    "s_and_b32 s25, s25, 0xffff\n"
    "s_mov_b32 s40, 0xa80\n"
    "s_mov_b32 s41, 0x700\n"
    "s_mov_b32 s42, 0x380\n"
    "s_mov_b32 s43, 0\n"
    "s_mov_b32 s44, 0x1a80\n"
    "s_mov_b32 s45, 0x1700\n"
    "s_mov_b32 s46, 0x1380\n"
    "s_mov_b32 s47, 0x1000\n"
    "s_mov_b32 s48, 0x2a80\n"
    "s_mov_b32 s49, 0x2700\n"
    "s_mov_b32 s50, 0x2380\n"
    "s_mov_b32 s51, 0x2000\n"
    "s_mov_b32 s52, 0x3a80\n"
    "s_mov_b32 s53, 0x3700\n"
    "s_mov_b32 s54, 0x3380\n"
    "s_mov_b32 s55, 0x3000\n"
    "s_mov_b32 s56, 0\n"
    "s_mov_b32 s57, 0xc00\n"
    "s_mov_b32 s58, 0x1800\n"
    "s_mov_b32 s59, 0x2400\n"
    "s_mov_b32 s60, 0x4000\n"
    "s_mov_b32 s61, 0x4c00\n"
    "s_mov_b32 s62, 0x5800\n"
    "s_mov_b32 s63, 0x6400\n"
    "s_mov_b32 s64, 0x8000\n"
    "s_mov_b32 s65, 0x8c00\n"
    "s_mov_b32 s66, 0x9800\n"
    "s_mov_b32 s67, 0xa400\n"
    "s_mov_b32 s68, 0xc000\n"
    "s_mov_b32 s69, 0xcc00\n"
    "s_mov_b32 s70, 0xd800\n"
    "s_mov_b32 s71, 0xe400\n"
    "s_add_u32 s72, s90, 0\n"
    "s_sub_u32 s39, 4, s36\n"
    "s_and_b32 s39, s39, 3\n"
    "s_lshl_b32 s39, s39, 12\n"
    "s_add_u32 s39, s39, s90\n"
    "s_add_u32 s76, s39, 65536\n"
    "s_add_u32 s73, s90, 4096\n"
    "s_sub_u32 s39, 5, s36\n"
    "s_and_b32 s39, s39, 3\n"
    "s_lshl_b32 s39, s39, 12\n"
    "s_add_u32 s39, s39, s90\n"
    "s_add_u32 s77, s39, 65536\n"
    "s_add_u32 s74, s90, 8192\n"
    "s_sub_u32 s39, 6, s36\n"
    "s_and_b32 s39, s39, 3\n"
    "s_lshl_b32 s39, s39, 12\n"
    "s_add_u32 s39, s39, s90\n"
    "s_add_u32 s78, s39, 65536\n"
    "s_add_u32 s75, s90, 12288\n"
    "s_sub_u32 s39, 7, s36\n"
    "s_and_b32 s39, s39, 3\n"
    "s_lshl_b32 s39, s39, 12\n"
    "s_add_u32 s39, s39, s90\n"
    "s_add_u32 s79, s39, 65536\n"
    "s_mov_b32 m0, s72\n"
    "s_nop 0\n"
    "buffer_load_dwordx4 v234, s[20:23], s40 offen nt lds\n"
    "buffer_load_dwordx4 v234, s[20:23], s41 offen offset:1024 nt lds\n"
    "buffer_load_dwordx4 v234, s[20:23], s42 offen offset:2048 nt lds\n"
    "buffer_load_dwordx4 v234, s[20:23], s43 offen offset:3072 nt lds\n"
    "s_mov_b32 m0, s73\n"
    "s_nop 0\n"
    "buffer_load_dwordx4 v234, s[20:23], s44 offen nt lds\n"
    "buffer_load_dwordx4 v234, s[20:23], s45 offen offset:1024 nt lds\n"
    "buffer_load_dwordx4 v234, s[20:23], s46 offen offset:2048 nt lds\n"
    "buffer_load_dwordx4 v234, s[20:23], s47 offen offset:3072 nt lds\n"
    "s_mov_b32 m0, s74\n"
    "s_nop 0\n"
    "buffer_load_dwordx4 v234, s[20:23], s48 offen nt lds\n"
    "buffer_load_dwordx4 v234, s[20:23], s49 offen offset:1024 nt lds\n"
    "buffer_load_dwordx4 v234, s[20:23], s50 offen offset:2048 nt lds\n"
    "buffer_load_dwordx4 v234, s[20:23], s51 offen offset:3072 nt lds\n"
    "s_mov_b32 m0, s75\n"
    "s_nop 0\n"
    "buffer_load_dwordx4 v234, s[20:23], s52 offen nt lds\n"
    "buffer_load_dwordx4 v234, s[20:23], s53 offen offset:1024 nt lds\n"
    "buffer_load_dwordx4 v234, s[20:23], s54 offen offset:2048 nt lds\n"
    "buffer_load_dwordx4 v234, s[20:23], s55 offen offset:3072 nt lds\n"
    "s_mov_b32 m0, s76\n"
    "s_nop 0\n"
    "buffer_load_dwordx4 v234, s[24:27], s56 offen nt lds\n"
    "buffer_load_dwordx4 v234, s[24:27], s57 offen offset:1024 nt lds\n"
    "buffer_load_dwordx4 v234, s[24:27], s58 offen offset:2048 nt lds\n"
    "buffer_load_dwordx4 v234, s[24:27], s59 offen offset:3072 nt lds\n"
    "s_mov_b32 m0, s77\n"
    "s_nop 0\n"
    "buffer_load_dwordx4 v234, s[24:27], s60 offen nt lds\n"
    "buffer_load_dwordx4 v234, s[24:27], s61 offen offset:1024 nt lds\n"
    "buffer_load_dwordx4 v234, s[24:27], s62 offen offset:2048 nt lds\n"
    "buffer_load_dwordx4 v234, s[24:27], s63 offen offset:3072 nt lds\n"
    "s_mov_b32 m0, s78\n"
    "s_nop 0\n"
    "buffer_load_dwordx4 v234, s[24:27], s64 offen nt lds\n"
    "buffer_load_dwordx4 v234, s[24:27], s65 offen offset:1024 nt lds\n"
    "buffer_load_dwordx4 v234, s[24:27], s66 offen offset:2048 nt lds\n"
    "buffer_load_dwordx4 v234, s[24:27], s67 offen offset:3072 nt lds\n"
    "s_mov_b32 m0, s79\n"
    "s_nop 0\n"
    "buffer_load_dwordx4 v234, s[24:27], s68 offen nt lds\n"
    "buffer_load_dwordx4 v234, s[24:27], s69 offen offset:1024 nt lds\n"
    "buffer_load_dwordx4 v234, s[24:27], s70 offen offset:2048 nt lds\n"
    "buffer_load_dwordx4 v234, s[24:27], s71 offen offset:3072 nt lds\n"
    "v_mov_b32 v0, 0\n"
    "v_mov_b32 v1, 0\n"
    "v_mov_b32 v2, 0\n"
    "v_mov_b32 v3, 0\n"
    "v_mov_b32 v4, 0\n"
    "v_mov_b32 v5, 0\n"
    "v_mov_b32 v6, 0\n"
    "v_mov_b32 v7, 0\n"
    "v_mov_b32 v8, 0\n"
    "v_mov_b32 v9, 0\n"
    "v_mov_b32 v10, 0\n"
    "v_mov_b32 v11, 0\n"
    "v_mov_b32 v12, 0\n"
    "v_mov_b32 v13, 0\n"
    "v_mov_b32 v14, 0\n"
    "v_mov_b32 v15, 0\n"
    "v_accvgpr_write_b32 a224, 0\n"
    "v_accvgpr_write_b32 a225, 0\n"
    "v_accvgpr_write_b32 a226, 0\n"
    "v_accvgpr_write_b32 a227, 0\n"
    "v_mov_b32 v16, 0\n"
    "v_mov_b32 v17, 0\n"
    "v_mov_b32 v18, 0\n"
    "v_mov_b32 v19, 0\n"
    "v_mov_b32 v20, 0\n"
    "v_mov_b32 v21, 0\n"
    "v_mov_b32 v22, 0\n"
    "v_mov_b32 v23, 0\n"
    "v_mov_b32 v24, 0\n"
    "v_mov_b32 v25, 0\n"
    "v_mov_b32 v26, 0\n"
    "v_mov_b32 v27, 0\n"
    "v_mov_b32 v28, 0\n"
    "v_mov_b32 v29, 0\n"
    "v_mov_b32 v30, 0\n"
    "v_mov_b32 v31, 0\n"
    "v_accvgpr_write_b32 a228, 0\n"
    "v_accvgpr_write_b32 a229, 0\n"
    "v_accvgpr_write_b32 a230, 0\n"
    "v_accvgpr_write_b32 a231, 0\n"
    "v_mov_b32 v32, 0\n"
    "v_mov_b32 v33, 0\n"
    "v_mov_b32 v34, 0\n"
    "v_mov_b32 v35, 0\n"
    "v_mov_b32 v36, 0\n"
    "v_mov_b32 v37, 0\n"
    "v_mov_b32 v38, 0\n"
    "v_mov_b32 v39, 0\n"
    "v_mov_b32 v40, 0\n"
    "v_mov_b32 v41, 0\n"
    "v_mov_b32 v42, 0\n"
    "v_mov_b32 v43, 0\n"
    "v_mov_b32 v44, 0\n"
    "v_mov_b32 v45, 0\n"
    "v_mov_b32 v46, 0\n"
    "v_mov_b32 v47, 0\n"
    "v_accvgpr_write_b32 a232, 0\n"
    "v_accvgpr_write_b32 a233, 0\n"
    "v_accvgpr_write_b32 a234, 0\n"
    "v_accvgpr_write_b32 a235, 0\n"
    "v_mov_b32 v48, 0\n"
    "v_mov_b32 v49, 0\n"
    "v_mov_b32 v50, 0\n"
    "v_mov_b32 v51, 0\n"
    "v_mov_b32 v52, 0\n"
    "v_mov_b32 v53, 0\n"
    "v_mov_b32 v54, 0\n"
    "v_mov_b32 v55, 0\n"
    "v_mov_b32 v56, 0\n"
    "v_mov_b32 v57, 0\n"
    "v_mov_b32 v58, 0\n"
    "v_mov_b32 v59, 0\n"
    "v_mov_b32 v60, 0\n"
    "v_mov_b32 v61, 0\n"
    "v_mov_b32 v62, 0\n"
    "v_mov_b32 v63, 0\n"
    "v_accvgpr_write_b32 a236, 0\n"
    "v_accvgpr_write_b32 a237, 0\n"
    "v_accvgpr_write_b32 a238, 0\n"
    "v_accvgpr_write_b32 a239, 0\n"
    "v_mov_b32 v64, 0\n"
    "v_mov_b32 v65, 0\n"
    "v_mov_b32 v66, 0\n"
    "v_mov_b32 v67, 0\n"
    "v_mov_b32 v68, 0\n"
    "v_mov_b32 v69, 0\n"
    "v_mov_b32 v70, 0\n"
    "v_mov_b32 v71, 0\n"
    "v_mov_b32 v72, 0\n"
    "v_mov_b32 v73, 0\n"
    "v_mov_b32 v74, 0\n"
    "v_mov_b32 v75, 0\n"
    "v_mov_b32 v76, 0\n"
    "v_mov_b32 v77, 0\n"
    "v_mov_b32 v78, 0\n"
    "v_mov_b32 v79, 0\n"
    "v_accvgpr_write_b32 a240, 0\n"
    "v_accvgpr_write_b32 a241, 0\n"
    "v_accvgpr_write_b32 a242, 0\n"
    "v_accvgpr_write_b32 a243, 0\n"
    "v_mov_b32 v80, 0\n"
    "v_mov_b32 v81, 0\n"
    "v_mov_b32 v82, 0\n"
    "v_mov_b32 v83, 0\n"
    "v_mov_b32 v84, 0\n"
    "v_mov_b32 v85, 0\n"
    "v_mov_b32 v86, 0\n"
    "v_mov_b32 v87, 0\n"
    "v_mov_b32 v88, 0\n"
    "v_mov_b32 v89, 0\n"
    "v_mov_b32 v90, 0\n"
    "v_mov_b32 v91, 0\n"
    "v_mov_b32 v92, 0\n"
    "v_mov_b32 v93, 0\n"
    "v_mov_b32 v94, 0\n"
    "v_mov_b32 v95, 0\n"
    "v_accvgpr_write_b32 a244, 0\n"
    "v_accvgpr_write_b32 a245, 0\n"
    "v_accvgpr_write_b32 a246, 0\n"
    "v_accvgpr_write_b32 a247, 0\n"
    "v_mov_b32 v96, 0\n"
    "v_mov_b32 v97, 0\n"
    "v_mov_b32 v98, 0\n"
    "v_mov_b32 v99, 0\n"
    "v_mov_b32 v100, 0\n"
    "v_mov_b32 v101, 0\n"
    "v_mov_b32 v102, 0\n"
    "v_mov_b32 v103, 0\n"
    "v_mov_b32 v104, 0\n"
    "v_mov_b32 v105, 0\n"
    "v_mov_b32 v106, 0\n"
    "v_mov_b32 v107, 0\n"
    "v_mov_b32 v108, 0\n"
    "v_mov_b32 v109, 0\n"
    "v_mov_b32 v110, 0\n"
    "v_mov_b32 v111, 0\n"
    "v_accvgpr_write_b32 a248, 0\n"
    "v_accvgpr_write_b32 a249, 0\n"
    "v_accvgpr_write_b32 a250, 0\n"
    "v_accvgpr_write_b32 a251, 0\n"
    "s_waitcnt vmcnt(16)\n"
    "ds_read_b128 v[112:115], v223\n"
    "ds_read_b128 v[116:119], v224\n"
    "ds_read_b128 v[120:123], v223 offset:1024\n"
    "ds_read_b128 v[124:127], v224 offset:1024\n"
    "ds_read_b128 v[128:131], v223 offset:2048\n"
    "ds_read_b128 v[132:135], v224 offset:2048\n"
    "ds_read_b128 v[136:139], v223 offset:3072\n"
    "ds_read_b128 v[140:143], v224 offset:3072\n"
    "ds_read_b128 v[144:147], v223 offset:8192\n"
    "ds_read_b128 v[148:151], v224 offset:8192\n"
    "ds_read_b128 v[152:155], v223 offset:9216\n"
    "ds_read_b128 v[156:159], v224 offset:9216\n"
    "ds_read_b128 v[160:163], v223 offset:10240\n"
    "ds_read_b128 v[164:167], v224 offset:10240\n"
    "ds_read_b128 v[168:171], v223 offset:11264\n"
    "ds_read_b128 v[172:175], v224 offset:11264\n"
    "s_add_u32 s20, s20, 65536\n"
    "s_addc_u32 s21, s21, 0\n"
    "s_add_u32 s24, s24, 65536\n"
    "s_addc_u32 s25, s25, 0\n"
    "s_mov_b32 s37, 15\n"
    "L_sub_%=:\n"
    "s_waitcnt lgkmcnt(12)\n"
    "v_mfma_f32_16x16x32_bf16 v[176:179], v[112:115], a[0:3], 0\n"
    "v_mfma_f32_16x16x32_bf16 v[176:179], v[116:119], a[4:7], v[176:179]\n"
    "v_mfma_f32_16x16x32_bf16 v[176:179], v[120:123], a[8:11], v[176:179]\n"
    "v_mfma_f32_16x16x32_bf16 v[176:179], v[124:127], a[12:15], v[176:179]\n"
    "s_waitcnt lgkmcnt(8)\n"
    "v_mfma_f32_16x16x32_bf16 v[176:179], v[128:131], a[16:19], v[176:179]\n"
    "v_mfma_f32_16x16x32_bf16 v[176:179], v[132:135], a[20:23], v[176:179]\n"
    "v_mfma_f32_16x16x32_bf16 v[176:179], v[136:139], a[24:27], v[176:179]\n"
    "v_mfma_f32_16x16x32_bf16 v[176:179], v[140:143], a[28:31], v[176:179]\n"
    "s_waitcnt lgkmcnt(4)\n"
    "v_mfma_f32_16x16x32_bf16 v[180:183], v[144:147], a[0:3], 0\n"
    "s_mov_b32 m0, s72\n"
    "v_mfma_f32_16x16x32_bf16 v[180:183], v[148:151], a[4:7], v[180:183]\n"
    "buffer_load_dwordx4 v234, s[20:23], s40 offen nt lds\n"
    "v_mfma_f32_16x16x32_bf16 v[180:183], v[152:155], a[8:11], v[180:183]\n"
    "v_mfma_f32_16x16x32_bf16 v[180:183], v[156:159], a[12:15], v[180:183]\n"
    "s_nop 1\n"
    "v_sub_f32 v176, v176, v216\n"
    "v_exp_f32 v176, v176\n"
    "s_waitcnt lgkmcnt(0)\n"
    "v_mfma_f32_16x16x32_bf16 v[180:183], v[160:163], a[16:19], v[180:183]\n"
    "v_sub_f32 v177, v177, v216\n"
    "v_exp_f32 v177, v177\n"
    "v_mfma_f32_16x16x32_bf16 v[180:183], v[164:167], a[20:23], v[180:183]\n"
    "v_sub_f32 v178, v178, v216\n"
    "v_exp_f32 v178, v178\n"
    "v_mfma_f32_16x16x32_bf16 v[180:183], v[168:171], a[24:27], v[180:183]\n"
    "v_sub_f32 v179, v179, v216\n"
    "v_exp_f32 v179, v179\n"
    "v_mfma_f32_16x16x32_bf16 v[180:183], v[172:175], a[28:31], v[180:183]\n"
    "v_cvt_pk_bf16_f32 v192, v176, v177\n"
    "v_cvt_pk_bf16_f32 v193, v178, v179\n"
    "v_mfma_f32_16x16x32_bf16 v[184:187], v[112:115], a[32:35], 0\n"
    "ds_write_b64 v229, v[192:193]\n"
    "v_mfma_f32_16x16x32_bf16 v[184:187], v[116:119], a[36:39], v[184:187]\n"
    "buffer_load_dwordx4 v234, s[20:23], s41 offen offset:1024 nt lds\n"
    "v_mfma_f32_16x16x32_bf16 v[184:187], v[120:123], a[40:43], v[184:187]\n"
    "v_mfma_f32_16x16x32_bf16 v[184:187], v[124:127], a[44:47], v[184:187]\n"
    "v_sub_f32 v180, v180, v216\n"
    "v_exp_f32 v180, v180\n"
    "v_mfma_f32_16x16x32_bf16 v[184:187], v[128:131], a[48:51], v[184:187]\n"
    "v_sub_f32 v181, v181, v216\n"
    "v_exp_f32 v181, v181\n"
    "v_mfma_f32_16x16x32_bf16 v[184:187], v[132:135], a[52:55], v[184:187]\n"
    "v_sub_f32 v182, v182, v216\n"
    "v_exp_f32 v182, v182\n"
    "v_mfma_f32_16x16x32_bf16 v[184:187], v[136:139], a[56:59], v[184:187]\n"
    "v_sub_f32 v183, v183, v216\n"
    "v_exp_f32 v183, v183\n"
    "v_mfma_f32_16x16x32_bf16 v[184:187], v[140:143], a[60:63], v[184:187]\n"
    "v_cvt_pk_bf16_f32 v194, v180, v181\n"
    "v_cvt_pk_bf16_f32 v195, v182, v183\n"
    "v_mfma_f32_16x16x32_bf16 v[188:191], v[144:147], a[32:35], 0\n"
    "ds_write_b64 v229, v[194:195] offset:8\n"
    "v_mfma_f32_16x16x32_bf16 v[188:191], v[148:151], a[36:39], v[188:191]\n"
    "buffer_load_dwordx4 v234, s[20:23], s42 offen offset:2048 nt lds\n"
    "v_mfma_f32_16x16x32_bf16 v[188:191], v[152:155], a[40:43], v[188:191]\n"
    "buffer_load_dwordx4 v234, s[20:23], s43 offen offset:3072 nt lds\n"
    "v_mfma_f32_16x16x32_bf16 v[188:191], v[156:159], a[44:47], v[188:191]\n"
    "v_sub_f32 v184, v184, v217\n"
    "v_exp_f32 v184, v184\n"
    "v_mfma_f32_16x16x32_bf16 v[188:191], v[160:163], a[48:51], v[188:191]\n"
    "v_sub_f32 v185, v185, v217\n"
    "v_exp_f32 v185, v185\n"
    "v_mfma_f32_16x16x32_bf16 v[188:191], v[164:167], a[52:55], v[188:191]\n"
    "v_sub_f32 v186, v186, v217\n"
    "v_exp_f32 v186, v186\n"
    "v_mfma_f32_16x16x32_bf16 v[188:191], v[168:171], a[56:59], v[188:191]\n"
    "v_sub_f32 v187, v187, v217\n"
    "v_exp_f32 v187, v187\n"
    "v_mfma_f32_16x16x32_bf16 v[188:191], v[172:175], a[60:63], v[188:191]\n"
    "v_cvt_pk_bf16_f32 v196, v184, v185\n"
    "v_cvt_pk_bf16_f32 v197, v186, v187\n"
    "v_mfma_f32_16x16x32_bf16 v[176:179], v[112:115], a[64:67], 0\n"
    "ds_write_b64 v229, v[196:197] offset:4096\n"
    "s_mov_b32 m0, s73\n"
    "v_mfma_f32_16x16x32_bf16 v[176:179], v[116:119], a[68:71], v[176:179]\n"
    "buffer_load_dwordx4 v234, s[20:23], s44 offen nt lds\n"
    "v_mfma_f32_16x16x32_bf16 v[176:179], v[120:123], a[72:75], v[176:179]\n"
    "v_mfma_f32_16x16x32_bf16 v[176:179], v[124:127], a[76:79], v[176:179]\n"
    "v_sub_f32 v188, v188, v217\n"
    "v_exp_f32 v188, v188\n"
    "v_mfma_f32_16x16x32_bf16 v[176:179], v[128:131], a[80:83], v[176:179]\n"
    "v_sub_f32 v189, v189, v217\n"
    "v_exp_f32 v189, v189\n"
    "v_mfma_f32_16x16x32_bf16 v[176:179], v[132:135], a[84:87], v[176:179]\n"
    "v_sub_f32 v190, v190, v217\n"
    "v_exp_f32 v190, v190\n"
    "v_mfma_f32_16x16x32_bf16 v[176:179], v[136:139], a[88:91], v[176:179]\n"
    "v_sub_f32 v191, v191, v217\n"
    "v_exp_f32 v191, v191\n"
    "v_mfma_f32_16x16x32_bf16 v[176:179], v[140:143], a[92:95], v[176:179]\n"
    "v_cvt_pk_bf16_f32 v198, v188, v189\n"
    "v_cvt_pk_bf16_f32 v199, v190, v191\n"
    "v_mfma_f32_16x16x32_bf16 v[180:183], v[144:147], a[64:67], 0\n"
    "ds_write_b64 v229, v[198:199] offset:4104\n"
    "v_mfma_f32_16x16x32_bf16 v[180:183], v[148:151], a[68:71], v[180:183]\n"
    "buffer_load_dwordx4 v234, s[20:23], s45 offen offset:1024 nt lds\n"
    "v_mfma_f32_16x16x32_bf16 v[180:183], v[152:155], a[72:75], v[180:183]\n"
    "buffer_load_dwordx4 v234, s[20:23], s46 offen offset:2048 nt lds\n"
    "v_mfma_f32_16x16x32_bf16 v[180:183], v[156:159], a[76:79], v[180:183]\n"
    "v_sub_f32 v176, v176, v218\n"
    "v_exp_f32 v176, v176\n"
    "v_mfma_f32_16x16x32_bf16 v[180:183], v[160:163], a[80:83], v[180:183]\n"
    "v_sub_f32 v177, v177, v218\n"
    "v_exp_f32 v177, v177\n"
    "v_mfma_f32_16x16x32_bf16 v[180:183], v[164:167], a[84:87], v[180:183]\n"
    "v_sub_f32 v178, v178, v218\n"
    "v_exp_f32 v178, v178\n"
    "v_mfma_f32_16x16x32_bf16 v[180:183], v[168:171], a[88:91], v[180:183]\n"
    "v_sub_f32 v179, v179, v218\n"
    "v_exp_f32 v179, v179\n"
    "v_mfma_f32_16x16x32_bf16 v[180:183], v[172:175], a[92:95], v[180:183]\n"
    "v_cvt_pk_bf16_f32 v192, v176, v177\n"
    "v_cvt_pk_bf16_f32 v193, v178, v179\n"
    "v_mfma_f32_16x16x32_bf16 v[184:187], v[112:115], a[96:99], 0\n"
    "ds_write_b64 v229, v[192:193] offset:8192\n"
    "v_mfma_f32_16x16x32_bf16 v[184:187], v[116:119], a[100:103], v[184:187]\n"
    "buffer_load_dwordx4 v234, s[20:23], s47 offen offset:3072 nt lds\n"
    "v_mfma_f32_16x16x32_bf16 v[184:187], v[120:123], a[104:107], v[184:187]\n"
    "v_mfma_f32_16x16x32_bf16 v[184:187], v[124:127], a[108:111], v[184:187]\n"
    "v_sub_f32 v180, v180, v218\n"
    "v_exp_f32 v180, v180\n"
    "v_mfma_f32_16x16x32_bf16 v[184:187], v[128:131], a[112:115], v[184:187]\n"
    "v_sub_f32 v181, v181, v218\n"
    "v_exp_f32 v181, v181\n"
    "v_mfma_f32_16x16x32_bf16 v[184:187], v[132:135], a[116:119], v[184:187]\n"
    "v_sub_f32 v182, v182, v218\n"
    "v_exp_f32 v182, v182\n"
    "v_mfma_f32_16x16x32_bf16 v[184:187], v[136:139], a[120:123], v[184:187]\n"
    "v_sub_f32 v183, v183, v218\n"
    "v_exp_f32 v183, v183\n"
    "v_mfma_f32_16x16x32_bf16 v[184:187], v[140:143], a[124:127], v[184:187]\n"
    "v_cvt_pk_bf16_f32 v194, v180, v181\n"
    "v_cvt_pk_bf16_f32 v195, v182, v183\n"
    "v_mfma_f32_16x16x32_bf16 v[188:191], v[144:147], a[96:99], 0\n"
    "ds_write_b64 v229, v[194:195] offset:8200\n"
    "s_mov_b32 m0, s74\n"
    "v_mfma_f32_16x16x32_bf16 v[188:191], v[148:151], a[100:103], v[188:191]\n"
    "buffer_load_dwordx4 v234, s[20:23], s48 offen nt lds\n"
    "v_mfma_f32_16x16x32_bf16 v[188:191], v[152:155], a[104:107], v[188:191]\n"
    "buffer_load_dwordx4 v234, s[20:23], s49 offen offset:1024 nt lds\n"
    "v_mfma_f32_16x16x32_bf16 v[188:191], v[156:159], a[108:111], v[188:191]\n"
    "v_sub_f32 v184, v184, v219\n"
    "v_exp_f32 v184, v184\n"
    "v_mfma_f32_16x16x32_bf16 v[188:191], v[160:163], a[112:115], v[188:191]\n"
    "v_sub_f32 v185, v185, v219\n"
    "v_exp_f32 v185, v185\n"
    "v_mfma_f32_16x16x32_bf16 v[188:191], v[164:167], a[116:119], v[188:191]\n"
    "v_sub_f32 v186, v186, v219\n"
    "v_exp_f32 v186, v186\n"
    "v_mfma_f32_16x16x32_bf16 v[188:191], v[168:171], a[120:123], v[188:191]\n"
    "v_sub_f32 v187, v187, v219\n"
    "v_exp_f32 v187, v187\n"
    "v_mfma_f32_16x16x32_bf16 v[188:191], v[172:175], a[124:127], v[188:191]\n"
    "v_cvt_pk_bf16_f32 v196, v184, v185\n"
    "v_cvt_pk_bf16_f32 v197, v186, v187\n"
    "v_mfma_f32_16x16x32_bf16 v[176:179], v[112:115], a[128:131], 0\n"
    "ds_write_b64 v229, v[196:197] offset:12288\n"
    "v_mfma_f32_16x16x32_bf16 v[176:179], v[116:119], a[132:135], v[176:179]\n"
    "buffer_load_dwordx4 v234, s[20:23], s50 offen offset:2048 nt lds\n"
    "v_mfma_f32_16x16x32_bf16 v[176:179], v[120:123], a[136:139], v[176:179]\n"
    "v_mfma_f32_16x16x32_bf16 v[176:179], v[124:127], a[140:143], v[176:179]\n"
    "v_sub_f32 v188, v188, v219\n"
    "v_exp_f32 v188, v188\n"
    "v_mfma_f32_16x16x32_bf16 v[176:179], v[128:131], a[144:147], v[176:179]\n"
    "v_sub_f32 v189, v189, v219\n"
    "v_exp_f32 v189, v189\n"
    "v_mfma_f32_16x16x32_bf16 v[176:179], v[132:135], a[148:151], v[176:179]\n"
    "v_sub_f32 v190, v190, v219\n"
    "v_exp_f32 v190, v190\n"
    "v_mfma_f32_16x16x32_bf16 v[176:179], v[136:139], a[152:155], v[176:179]\n"
    "v_sub_f32 v191, v191, v219\n"
    "v_exp_f32 v191, v191\n"
    "v_mfma_f32_16x16x32_bf16 v[176:179], v[140:143], a[156:159], v[176:179]\n"
    "v_cvt_pk_bf16_f32 v198, v188, v189\n"
    "v_cvt_pk_bf16_f32 v199, v190, v191\n"
    "v_mfma_f32_16x16x32_bf16 v[180:183], v[144:147], a[128:131], 0\n"
    "ds_write_b64 v229, v[198:199] offset:12296\n"
    "v_mfma_f32_16x16x32_bf16 v[180:183], v[148:151], a[132:135], v[180:183]\n"
    "buffer_load_dwordx4 v234, s[20:23], s51 offen offset:3072 nt lds\n"
    "v_mfma_f32_16x16x32_bf16 v[180:183], v[152:155], a[136:139], v[180:183]\n"
    "v_mfma_f32_16x16x32_bf16 v[180:183], v[156:159], a[140:143], v[180:183]\n"
    "v_sub_f32 v176, v176, v220\n"
    "v_exp_f32 v176, v176\n"
    "v_mfma_f32_16x16x32_bf16 v[180:183], v[160:163], a[144:147], v[180:183]\n"
    "v_sub_f32 v177, v177, v220\n"
    "v_exp_f32 v177, v177\n"
    "v_mfma_f32_16x16x32_bf16 v[180:183], v[164:167], a[148:151], v[180:183]\n"
    "v_sub_f32 v178, v178, v220\n"
    "v_exp_f32 v178, v178\n"
    "v_mfma_f32_16x16x32_bf16 v[180:183], v[168:171], a[152:155], v[180:183]\n"
    "v_sub_f32 v179, v179, v220\n"
    "v_exp_f32 v179, v179\n"
    "v_mfma_f32_16x16x32_bf16 v[180:183], v[172:175], a[156:159], v[180:183]\n"
    "v_cvt_pk_bf16_f32 v192, v176, v177\n"
    "v_cvt_pk_bf16_f32 v193, v178, v179\n"
    "v_mfma_f32_16x16x32_bf16 v[184:187], v[112:115], a[160:163], 0\n"
    "ds_write_b64 v229, v[192:193] offset:16384\n"
    "s_mov_b32 m0, s75\n"
    "v_mfma_f32_16x16x32_bf16 v[184:187], v[116:119], a[164:167], v[184:187]\n"
    "buffer_load_dwordx4 v234, s[20:23], s52 offen nt lds\n"
    "v_mfma_f32_16x16x32_bf16 v[184:187], v[120:123], a[168:171], v[184:187]\n"
    "buffer_load_dwordx4 v234, s[20:23], s53 offen offset:1024 nt lds\n"
    "v_mfma_f32_16x16x32_bf16 v[184:187], v[124:127], a[172:175], v[184:187]\n"
    "v_sub_f32 v180, v180, v220\n"
    "v_exp_f32 v180, v180\n"
    "v_mfma_f32_16x16x32_bf16 v[184:187], v[128:131], a[176:179], v[184:187]\n"
    "v_sub_f32 v181, v181, v220\n"
    "v_exp_f32 v181, v181\n"
    "v_mfma_f32_16x16x32_bf16 v[184:187], v[132:135], a[180:183], v[184:187]\n"
    "v_sub_f32 v182, v182, v220\n"
    "v_exp_f32 v182, v182\n"
    "v_mfma_f32_16x16x32_bf16 v[184:187], v[136:139], a[184:187], v[184:187]\n"
    "v_sub_f32 v183, v183, v220\n"
    "v_exp_f32 v183, v183\n"
    "v_mfma_f32_16x16x32_bf16 v[184:187], v[140:143], a[188:191], v[184:187]\n"
    "v_cvt_pk_bf16_f32 v194, v180, v181\n"
    "v_cvt_pk_bf16_f32 v195, v182, v183\n"
    "v_mfma_f32_16x16x32_bf16 v[188:191], v[144:147], a[160:163], 0\n"
    "ds_write_b64 v229, v[194:195] offset:16392\n"
    "v_mfma_f32_16x16x32_bf16 v[188:191], v[148:151], a[164:167], v[188:191]\n"
    "buffer_load_dwordx4 v234, s[20:23], s54 offen offset:2048 nt lds\n"
    "v_mfma_f32_16x16x32_bf16 v[188:191], v[152:155], a[168:171], v[188:191]\n"
    "v_mfma_f32_16x16x32_bf16 v[188:191], v[156:159], a[172:175], v[188:191]\n"
    "v_sub_f32 v184, v184, v221\n"
    "v_exp_f32 v184, v184\n"
    "v_mfma_f32_16x16x32_bf16 v[188:191], v[160:163], a[176:179], v[188:191]\n"
    "v_sub_f32 v185, v185, v221\n"
    "v_exp_f32 v185, v185\n"
    "v_mfma_f32_16x16x32_bf16 v[188:191], v[164:167], a[180:183], v[188:191]\n"
    "v_sub_f32 v186, v186, v221\n"
    "v_exp_f32 v186, v186\n"
    "v_mfma_f32_16x16x32_bf16 v[188:191], v[168:171], a[184:187], v[188:191]\n"
    "v_sub_f32 v187, v187, v221\n"
    "v_exp_f32 v187, v187\n"
    "v_mfma_f32_16x16x32_bf16 v[188:191], v[172:175], a[188:191], v[188:191]\n"
    "v_cvt_pk_bf16_f32 v196, v184, v185\n"
    "v_cvt_pk_bf16_f32 v197, v186, v187\n"
    "v_mfma_f32_16x16x32_bf16 v[176:179], v[112:115], a[192:195], 0\n"
    "ds_write_b64 v229, v[196:197] offset:20480\n"
    "v_mfma_f32_16x16x32_bf16 v[176:179], v[116:119], a[196:199], v[176:179]\n"
    "buffer_load_dwordx4 v234, s[20:23], s55 offen offset:3072 nt lds\n"
    "v_mfma_f32_16x16x32_bf16 v[176:179], v[120:123], a[200:203], v[176:179]\n"
    "s_waitcnt vmcnt(16)\n"
    "ds_read_b64_tr_b16 v[112:113], v225\n"
    "v_mfma_f32_16x16x32_bf16 v[176:179], v[124:127], a[204:207], v[176:179]\n"
    "ds_read_b64_tr_b16 v[114:115], v225 offset:2048\n"
    "v_mfma_f32_16x16x32_bf16 v[176:179], v[128:131], a[208:211], v[176:179]\n"
    "ds_read_b64_tr_b16 v[116:117], v225 offset:4096\n"
    "v_mfma_f32_16x16x32_bf16 v[176:179], v[132:135], a[212:215], v[176:179]\n"
    "ds_read_b64_tr_b16 v[118:119], v225 offset:6144\n"
    "v_mfma_f32_16x16x32_bf16 v[176:179], v[136:139], a[216:219], v[176:179]\n"
    "ds_read_b64_tr_b16 v[120:121], v225 offset:8192\n"
    "v_mfma_f32_16x16x32_bf16 v[176:179], v[140:143], a[220:223], v[176:179]\n"
    "s_waitcnt lgkmcnt(6)\n"
    "s_barrier\n"
    "ds_read_b64_tr_b16 v[122:123], v225 offset:10240\n"
    "v_mfma_f32_16x16x32_bf16 v[180:183], v[144:147], a[192:195], 0\n"
    "ds_read_b128 v[200:203], v230\n"
    "ds_read_b128 v[212:215], v233\n"
    "v_mfma_f32_16x16x32_bf16 v[180:183], v[148:151], a[196:199], v[180:183]\n"
    "ds_read_b128 v[204:207], v231\n"
    "v_mfma_f32_16x16x32_bf16 v[180:183], v[152:155], a[200:203], v[180:183]\n"
    "ds_read_b128 v[208:211], v232\n"
    "v_mfma_f32_16x16x32_bf16 v[180:183], v[156:159], a[204:207], v[180:183]\n"
    "ds_read_b64_tr_b16 v[124:125], v225 offset:12288\n"
    "v_mfma_f32_16x16x32_bf16 v[180:183], v[160:163], a[208:211], v[180:183]\n"
    "ds_read_b64_tr_b16 v[126:127], v225 offset:14336\n"
    "v_mfma_f32_16x16x32_bf16 v[180:183], v[164:167], a[212:215], v[180:183]\n"
    "ds_read_b64_tr_b16 v[128:129], v226\n"
    "ds_read_b64_tr_b16 v[130:131], v226 offset:2048\n"
    "v_mfma_f32_16x16x32_bf16 v[180:183], v[168:171], a[216:219], v[180:183]\n"
    "ds_read_b64_tr_b16 v[132:133], v226 offset:4096\n"
    "ds_read_b64_tr_b16 v[134:135], v226 offset:6144\n"
    "v_mfma_f32_16x16x32_bf16 v[180:183], v[172:175], a[220:223], v[180:183]\n"
    "s_add_u32 s20, s20, 65536\n"
    "s_addc_u32 s21, s21, 0\n"
    "ds_read_b64_tr_b16 v[136:137], v226 offset:8192\n"
    "ds_read_b64_tr_b16 v[138:139], v226 offset:10240\n"
    "s_waitcnt lgkmcnt(11)\n"
    "v_mfma_f32_16x16x32_bf16 a[224:227], a[252:255], v[200:203], a[224:227]\n"
    "ds_read_b64_tr_b16 v[140:141], v226 offset:12288\n"
    "ds_read_b64_tr_b16 v[142:143], v226 offset:14336\n"
    "s_waitcnt lgkmcnt(8)\n"
    "v_mfma_f32_16x16x32_bf16 v[0:3], v[112:115], v[200:203], v[0:3]\n"
    "ds_read_b64_tr_b16 v[144:145], v227\n"
    "ds_read_b64_tr_b16 v[146:147], v227 offset:2048\n"
    "v_mfma_f32_16x16x32_bf16 v[0:3], v[116:119], v[204:207], v[0:3]\n"
    "ds_read_b64_tr_b16 v[148:149], v227 offset:4096\n"
    "ds_read_b64_tr_b16 v[150:151], v227 offset:6144\n"
    "v_mfma_f32_16x16x32_bf16 v[0:3], v[120:123], v[208:211], v[0:3]\n"
    "ds_read_b64_tr_b16 v[152:153], v227 offset:8192\n"
    "ds_read_b64_tr_b16 v[154:155], v227 offset:10240\n"
    "v_mfma_f32_16x16x32_bf16 v[0:3], v[124:127], v[212:215], v[0:3]\n"
    "ds_read_b64_tr_b16 v[156:157], v227 offset:12288\n"
    "ds_read_b64_tr_b16 v[158:159], v227 offset:14336\n"
    "s_waitcnt lgkmcnt(8)\n"
    "v_mfma_f32_16x16x32_bf16 v[4:7], v[128:131], v[200:203], v[4:7]\n"
    "ds_read_b64_tr_b16 v[160:161], v228\n"
    "ds_read_b64_tr_b16 v[162:163], v228 offset:2048\n"
    "v_mfma_f32_16x16x32_bf16 v[4:7], v[132:135], v[204:207], v[4:7]\n"
    "ds_read_b64_tr_b16 v[164:165], v228 offset:4096\n"
    "ds_read_b64_tr_b16 v[166:167], v228 offset:6144\n"
    "v_mfma_f32_16x16x32_bf16 v[4:7], v[136:139], v[208:211], v[4:7]\n"
    "ds_read_b64_tr_b16 v[168:169], v228 offset:8192\n"
    "ds_read_b64_tr_b16 v[170:171], v228 offset:10240\n"
    "v_mfma_f32_16x16x32_bf16 v[4:7], v[140:143], v[212:215], v[4:7]\n"
    "ds_read_b64_tr_b16 v[172:173], v228 offset:12288\n"
    "ds_read_b64_tr_b16 v[174:175], v228 offset:14336\n"
    "s_waitcnt lgkmcnt(8)\n"
    "v_mfma_f32_16x16x32_bf16 v[8:11], v[144:147], v[200:203], v[8:11]\n"
    "ds_read_b128 v[236:239], v230 offset:4096\n"
    "v_mfma_f32_16x16x32_bf16 v[8:11], v[148:151], v[204:207], v[8:11]\n"
    "ds_read_b128 v[240:243], v231 offset:4096\n"
    "v_mfma_f32_16x16x32_bf16 v[8:11], v[152:155], v[208:211], v[8:11]\n"
    "ds_read_b128 v[244:247], v232 offset:4096\n"
    "ds_read_b128 v[248:251], v233 offset:4096\n"
    "v_mfma_f32_16x16x32_bf16 v[8:11], v[156:159], v[212:215], v[8:11]\n"
    "s_waitcnt lgkmcnt(4)\n"
    "v_mfma_f32_16x16x32_bf16 v[12:15], v[160:163], v[200:203], v[12:15]\n"
    "s_mov_b32 m0, s76\n"
    "v_mfma_f32_16x16x32_bf16 v[12:15], v[164:167], v[204:207], v[12:15]\n"
    "buffer_load_dwordx4 v234, s[24:27], s56 offen nt lds\n"
    "v_mfma_f32_16x16x32_bf16 v[12:15], v[168:171], v[208:211], v[12:15]\n"
    "v_mfma_f32_16x16x32_bf16 v[12:15], v[172:175], v[212:215], v[12:15]\n"
    "s_waitcnt lgkmcnt(3)\n"
    "v_mfma_f32_16x16x32_bf16 a[228:231], a[252:255], v[236:239], a[228:231]\n"
    "v_sub_f32 v188, v188, v221\n"
    "v_exp_f32 v188, v188\n"
    "s_waitcnt lgkmcnt(0)\n"
    "v_mfma_f32_16x16x32_bf16 v[16:19], v[112:115], v[236:239], v[16:19]\n"
    "ds_read_b128 v[200:203], v230 offset:8192\n"
    "v_mfma_f32_16x16x32_bf16 v[16:19], v[116:119], v[240:243], v[16:19]\n"
    "ds_read_b128 v[204:207], v231 offset:8192\n"
    "v_mfma_f32_16x16x32_bf16 v[16:19], v[120:123], v[244:247], v[16:19]\n"
    "ds_read_b128 v[208:211], v232 offset:8192\n"
    "v_mfma_f32_16x16x32_bf16 v[16:19], v[124:127], v[248:251], v[16:19]\n"
    "ds_read_b128 v[212:215], v233 offset:8192\n"
    "v_mfma_f32_16x16x32_bf16 v[20:23], v[128:131], v[236:239], v[20:23]\n"
    "buffer_load_dwordx4 v234, s[24:27], s57 offen offset:1024 nt lds\n"
    "v_mfma_f32_16x16x32_bf16 v[20:23], v[132:135], v[240:243], v[20:23]\n"
    "v_sub_f32 v189, v189, v221\n"
    "v_exp_f32 v189, v189\n"
    "v_mfma_f32_16x16x32_bf16 v[20:23], v[136:139], v[244:247], v[20:23]\n"
    "v_sub_f32 v190, v190, v221\n"
    "v_exp_f32 v190, v190\n"
    "v_mfma_f32_16x16x32_bf16 v[20:23], v[140:143], v[248:251], v[20:23]\n"
    "v_sub_f32 v191, v191, v221\n"
    "v_exp_f32 v191, v191\n"
    "v_mfma_f32_16x16x32_bf16 v[24:27], v[144:147], v[236:239], v[24:27]\n"
    "buffer_load_dwordx4 v234, s[24:27], s58 offen offset:2048 nt lds\n"
    "v_mfma_f32_16x16x32_bf16 v[24:27], v[148:151], v[240:243], v[24:27]\n"
    "v_cvt_pk_bf16_f32 v198, v188, v189\n"
    "v_cvt_pk_bf16_f32 v199, v190, v191\n"
    "v_mfma_f32_16x16x32_bf16 v[24:27], v[152:155], v[244:247], v[24:27]\n"
    "ds_write_b64 v229, v[198:199] offset:20488\n"
    "v_mfma_f32_16x16x32_bf16 v[24:27], v[156:159], v[248:251], v[24:27]\n"
    "v_sub_f32 v176, v176, v222\n"
    "v_exp_f32 v176, v176\n"
    "v_mfma_f32_16x16x32_bf16 v[28:31], v[160:163], v[236:239], v[28:31]\n"
    "v_sub_f32 v177, v177, v222\n"
    "v_exp_f32 v177, v177\n"
    "v_mfma_f32_16x16x32_bf16 v[28:31], v[164:167], v[240:243], v[28:31]\n"
    "v_sub_f32 v178, v178, v222\n"
    "v_exp_f32 v178, v178\n"
    "v_mfma_f32_16x16x32_bf16 v[28:31], v[168:171], v[244:247], v[28:31]\n"
    "buffer_load_dwordx4 v234, s[24:27], s59 offen offset:3072 nt lds\n"
    "v_mfma_f32_16x16x32_bf16 v[28:31], v[172:175], v[248:251], v[28:31]\n"
    "v_sub_f32 v179, v179, v222\n"
    "v_exp_f32 v179, v179\n"
    "s_waitcnt lgkmcnt(4)\n"
    "v_mfma_f32_16x16x32_bf16 a[232:235], a[252:255], v[200:203], a[232:235]\n"
    "v_cvt_pk_bf16_f32 v192, v176, v177\n"
    "v_cvt_pk_bf16_f32 v193, v178, v179\n"
    "s_waitcnt lgkmcnt(1)\n"
    "v_mfma_f32_16x16x32_bf16 v[32:35], v[112:115], v[200:203], v[32:35]\n"
    "ds_read_b128 v[236:239], v230 offset:12288\n"
    "v_mfma_f32_16x16x32_bf16 v[32:35], v[116:119], v[204:207], v[32:35]\n"
    "ds_read_b128 v[240:243], v231 offset:12288\n"
    "v_mfma_f32_16x16x32_bf16 v[32:35], v[120:123], v[208:211], v[32:35]\n"
    "ds_read_b128 v[244:247], v232 offset:12288\n"
    "v_mfma_f32_16x16x32_bf16 v[32:35], v[124:127], v[212:215], v[32:35]\n"
    "ds_read_b128 v[248:251], v233 offset:12288\n"
    "s_mov_b32 m0, s77\n"
    "v_mfma_f32_16x16x32_bf16 v[36:39], v[128:131], v[200:203], v[36:39]\n"
    "buffer_load_dwordx4 v234, s[24:27], s60 offen nt lds\n"
    "v_mfma_f32_16x16x32_bf16 v[36:39], v[132:135], v[204:207], v[36:39]\n"
    "ds_write_b64 v229, v[192:193] offset:24576\n"
    "v_mfma_f32_16x16x32_bf16 v[36:39], v[136:139], v[208:211], v[36:39]\n"
    "v_sub_f32 v180, v180, v222\n"
    "v_exp_f32 v180, v180\n"
    "v_mfma_f32_16x16x32_bf16 v[36:39], v[140:143], v[212:215], v[36:39]\n"
    "v_sub_f32 v181, v181, v222\n"
    "v_exp_f32 v181, v181\n"
    "v_mfma_f32_16x16x32_bf16 v[40:43], v[144:147], v[200:203], v[40:43]\n"
    "v_sub_f32 v182, v182, v222\n"
    "v_exp_f32 v182, v182\n"
    "v_mfma_f32_16x16x32_bf16 v[40:43], v[148:151], v[204:207], v[40:43]\n"
    "buffer_load_dwordx4 v234, s[24:27], s61 offen offset:1024 nt lds\n"
    "v_mfma_f32_16x16x32_bf16 v[40:43], v[152:155], v[208:211], v[40:43]\n"
    "v_sub_f32 v183, v183, v222\n"
    "v_exp_f32 v183, v183\n"
    "v_mfma_f32_16x16x32_bf16 v[40:43], v[156:159], v[212:215], v[40:43]\n"
    "v_cvt_pk_bf16_f32 v194, v180, v181\n"
    "v_cvt_pk_bf16_f32 v195, v182, v183\n"
    "v_mfma_f32_16x16x32_bf16 v[44:47], v[160:163], v[200:203], v[44:47]\n"
    "ds_write_b64 v229, v[194:195] offset:24584\n"
    "v_mfma_f32_16x16x32_bf16 v[44:47], v[164:167], v[204:207], v[44:47]\n"
    "v_mfma_f32_16x16x32_bf16 v[44:47], v[168:171], v[208:211], v[44:47]\n"
    "v_mfma_f32_16x16x32_bf16 v[44:47], v[172:175], v[212:215], v[44:47]\n"
    "buffer_load_dwordx4 v234, s[24:27], s62 offen offset:2048 nt lds\n"
    "s_waitcnt lgkmcnt(5)\n"
    "v_mfma_f32_16x16x32_bf16 a[236:239], a[252:255], v[236:239], a[236:239]\n"
    "s_waitcnt lgkmcnt(2)\n"
    "v_mfma_f32_16x16x32_bf16 v[48:51], v[112:115], v[236:239], v[48:51]\n"
    "ds_read_b128 v[200:203], v230 offset:16384\n"
    "v_mfma_f32_16x16x32_bf16 v[48:51], v[116:119], v[240:243], v[48:51]\n"
    "ds_read_b128 v[204:207], v231 offset:16384\n"
    "v_mfma_f32_16x16x32_bf16 v[48:51], v[120:123], v[244:247], v[48:51]\n"
    "ds_read_b128 v[208:211], v232 offset:16384\n"
    "v_mfma_f32_16x16x32_bf16 v[48:51], v[124:127], v[248:251], v[48:51]\n"
    "ds_read_b128 v[212:215], v233 offset:16384\n"
    "v_mfma_f32_16x16x32_bf16 v[52:55], v[128:131], v[236:239], v[52:55]\n"
    "buffer_load_dwordx4 v234, s[24:27], s63 offen offset:3072 nt lds\n"
    "v_mfma_f32_16x16x32_bf16 v[52:55], v[132:135], v[240:243], v[52:55]\n"
    "v_mfma_f32_16x16x32_bf16 v[52:55], v[136:139], v[244:247], v[52:55]\n"
    "v_mfma_f32_16x16x32_bf16 v[52:55], v[140:143], v[248:251], v[52:55]\n"
    "v_mfma_f32_16x16x32_bf16 v[56:59], v[144:147], v[236:239], v[56:59]\n"
    "v_mfma_f32_16x16x32_bf16 v[56:59], v[148:151], v[240:243], v[56:59]\n"
    "s_mov_b32 m0, s78\n"
    "v_mfma_f32_16x16x32_bf16 v[56:59], v[152:155], v[244:247], v[56:59]\n"
    "buffer_load_dwordx4 v234, s[24:27], s64 offen nt lds\n"
    "v_mfma_f32_16x16x32_bf16 v[56:59], v[156:159], v[248:251], v[56:59]\n"
    "v_mfma_f32_16x16x32_bf16 v[60:63], v[160:163], v[236:239], v[60:63]\n"
    "v_mfma_f32_16x16x32_bf16 v[60:63], v[164:167], v[240:243], v[60:63]\n"
    "v_mfma_f32_16x16x32_bf16 v[60:63], v[168:171], v[244:247], v[60:63]\n"
    "v_mfma_f32_16x16x32_bf16 v[60:63], v[172:175], v[248:251], v[60:63]\n"
    "s_waitcnt lgkmcnt(3)\n"
    "v_mfma_f32_16x16x32_bf16 a[240:243], a[252:255], v[200:203], a[240:243]\n"
    "s_waitcnt lgkmcnt(0)\n"
    "s_barrier\n"
    "buffer_load_dwordx4 v234, s[24:27], s65 offen offset:1024 nt lds\n"
    "v_mfma_f32_16x16x32_bf16 v[64:67], v[112:115], v[200:203], v[64:67]\n"
    "ds_read_b128 v[236:239], v230 offset:20480\n"
    "v_mfma_f32_16x16x32_bf16 v[64:67], v[116:119], v[204:207], v[64:67]\n"
    "ds_read_b128 v[240:243], v231 offset:20480\n"
    "v_mfma_f32_16x16x32_bf16 v[64:67], v[120:123], v[208:211], v[64:67]\n"
    "ds_read_b128 v[244:247], v232 offset:20480\n"
    "v_mfma_f32_16x16x32_bf16 v[64:67], v[124:127], v[212:215], v[64:67]\n"
    "ds_read_b128 v[248:251], v233 offset:20480\n"
    "v_mfma_f32_16x16x32_bf16 v[68:71], v[128:131], v[200:203], v[68:71]\n"
    "v_mfma_f32_16x16x32_bf16 v[68:71], v[132:135], v[204:207], v[68:71]\n"
    "buffer_load_dwordx4 v234, s[24:27], s66 offen offset:2048 nt lds\n"
    "v_mfma_f32_16x16x32_bf16 v[68:71], v[136:139], v[208:211], v[68:71]\n"
    "v_mfma_f32_16x16x32_bf16 v[68:71], v[140:143], v[212:215], v[68:71]\n"
    "v_mfma_f32_16x16x32_bf16 v[72:75], v[144:147], v[200:203], v[72:75]\n"
    "v_mfma_f32_16x16x32_bf16 v[72:75], v[148:151], v[204:207], v[72:75]\n"
    "v_mfma_f32_16x16x32_bf16 v[72:75], v[152:155], v[208:211], v[72:75]\n"
    "v_mfma_f32_16x16x32_bf16 v[72:75], v[156:159], v[212:215], v[72:75]\n"
    "buffer_load_dwordx4 v234, s[24:27], s67 offen offset:3072 nt lds\n"
    "v_mfma_f32_16x16x32_bf16 v[76:79], v[160:163], v[200:203], v[76:79]\n"
    "v_mfma_f32_16x16x32_bf16 v[76:79], v[164:167], v[204:207], v[76:79]\n"
    "v_mfma_f32_16x16x32_bf16 v[76:79], v[168:171], v[208:211], v[76:79]\n"
    "v_mfma_f32_16x16x32_bf16 v[76:79], v[172:175], v[212:215], v[76:79]\n"
    "s_waitcnt lgkmcnt(3)\n"
    "v_mfma_f32_16x16x32_bf16 a[244:247], a[252:255], v[236:239], a[244:247]\n"
    "s_waitcnt lgkmcnt(0)\n"
    "v_mfma_f32_16x16x32_bf16 v[80:83], v[112:115], v[236:239], v[80:83]\n"
    "ds_read_b128 v[200:203], v230 offset:24576\n"
    "v_mfma_f32_16x16x32_bf16 v[80:83], v[116:119], v[240:243], v[80:83]\n"
    "ds_read_b128 v[204:207], v231 offset:24576\n"
    "v_mfma_f32_16x16x32_bf16 v[80:83], v[120:123], v[244:247], v[80:83]\n"
    "ds_read_b128 v[208:211], v232 offset:24576\n"
    "v_mfma_f32_16x16x32_bf16 v[80:83], v[124:127], v[248:251], v[80:83]\n"
    "ds_read_b128 v[212:215], v233 offset:24576\n"
    "s_mov_b32 m0, s79\n"
    "v_mfma_f32_16x16x32_bf16 v[84:87], v[128:131], v[236:239], v[84:87]\n"
    "buffer_load_dwordx4 v234, s[24:27], s68 offen nt lds\n"
    "v_mfma_f32_16x16x32_bf16 v[84:87], v[132:135], v[240:243], v[84:87]\n"
    "v_mfma_f32_16x16x32_bf16 v[84:87], v[136:139], v[244:247], v[84:87]\n"
    "buffer_load_dwordx4 v234, s[24:27], s69 offen offset:1024 nt lds\n"
    "v_mfma_f32_16x16x32_bf16 v[84:87], v[140:143], v[248:251], v[84:87]\n"
    "v_mfma_f32_16x16x32_bf16 v[88:91], v[144:147], v[236:239], v[88:91]\n"
    "v_mfma_f32_16x16x32_bf16 v[88:91], v[148:151], v[240:243], v[88:91]\n"
    "v_mfma_f32_16x16x32_bf16 v[88:91], v[152:155], v[244:247], v[88:91]\n"
    "v_mfma_f32_16x16x32_bf16 v[88:91], v[156:159], v[248:251], v[88:91]\n"
    "v_mfma_f32_16x16x32_bf16 v[92:95], v[160:163], v[236:239], v[92:95]\n"
    "buffer_load_dwordx4 v234, s[24:27], s70 offen offset:2048 nt lds\n"
    "v_mfma_f32_16x16x32_bf16 v[92:95], v[164:167], v[240:243], v[92:95]\n"
    "v_mfma_f32_16x16x32_bf16 v[92:95], v[168:171], v[244:247], v[92:95]\n"
    "v_mfma_f32_16x16x32_bf16 v[92:95], v[172:175], v[248:251], v[92:95]\n"
    "s_waitcnt lgkmcnt(3)\n"
    "v_mfma_f32_16x16x32_bf16 a[248:251], a[252:255], v[200:203], a[248:251]\n"
    "s_waitcnt lgkmcnt(0)\n"
    "v_mfma_f32_16x16x32_bf16 v[96:99], v[112:115], v[200:203], v[96:99]\n"
    "s_waitcnt vmcnt(15)\n"
    "ds_read_b128 v[112:115], v223\n"
    "v_mfma_f32_16x16x32_bf16 v[96:99], v[116:119], v[204:207], v[96:99]\n"
    "buffer_load_dwordx4 v234, s[24:27], s71 offen offset:3072 nt lds\n"
    "ds_read_b128 v[116:119], v224\n"
    "v_mfma_f32_16x16x32_bf16 v[96:99], v[120:123], v[208:211], v[96:99]\n"
    "ds_read_b128 v[120:123], v223 offset:1024\n"
    "v_mfma_f32_16x16x32_bf16 v[96:99], v[124:127], v[212:215], v[96:99]\n"
    "ds_read_b128 v[124:127], v224 offset:1024\n"
    "v_mfma_f32_16x16x32_bf16 v[100:103], v[128:131], v[200:203], v[100:103]\n"
    "ds_read_b128 v[128:131], v223 offset:2048\n"
    "v_mfma_f32_16x16x32_bf16 v[100:103], v[132:135], v[204:207], v[100:103]\n"
    "ds_read_b128 v[132:135], v224 offset:2048\n"
    "v_mfma_f32_16x16x32_bf16 v[100:103], v[136:139], v[208:211], v[100:103]\n"
    "ds_read_b128 v[136:139], v223 offset:3072\n"
    "v_mfma_f32_16x16x32_bf16 v[100:103], v[140:143], v[212:215], v[100:103]\n"
    "ds_read_b128 v[140:143], v224 offset:3072\n"
    "v_mfma_f32_16x16x32_bf16 v[104:107], v[144:147], v[200:203], v[104:107]\n"
    "ds_read_b128 v[144:147], v223 offset:8192\n"
    "v_mfma_f32_16x16x32_bf16 v[104:107], v[148:151], v[204:207], v[104:107]\n"
    "ds_read_b128 v[148:151], v224 offset:8192\n"
    "v_mfma_f32_16x16x32_bf16 v[104:107], v[152:155], v[208:211], v[104:107]\n"
    "ds_read_b128 v[152:155], v223 offset:9216\n"
    "v_mfma_f32_16x16x32_bf16 v[104:107], v[156:159], v[212:215], v[104:107]\n"
    "ds_read_b128 v[156:159], v224 offset:9216\n"
    "v_mfma_f32_16x16x32_bf16 v[108:111], v[160:163], v[200:203], v[108:111]\n"
    "ds_read_b128 v[160:163], v223 offset:10240\n"
    "v_mfma_f32_16x16x32_bf16 v[108:111], v[164:167], v[204:207], v[108:111]\n"
    "ds_read_b128 v[164:167], v224 offset:10240\n"
    "v_mfma_f32_16x16x32_bf16 v[108:111], v[168:171], v[208:211], v[108:111]\n"
    "ds_read_b128 v[168:171], v223 offset:11264\n"
    "v_mfma_f32_16x16x32_bf16 v[108:111], v[172:175], v[212:215], v[108:111]\n"
    "s_add_u32 s24, s24, 65536\n"
    "s_addc_u32 s25, s25, 0\n"
    "ds_read_b128 v[172:175], v224 offset:11264\n"
    "s_sub_u32 s37, s37, 1\n"
    "s_cmp_lg_u32 s37, 0\n"
    "s_cbranch_scc1 L_sub_%=\n"
    "s_mul_i32 s80, s36, 7168\n"
    "s_add_u32 s83, s12, s80\n"
    "s_addc_u32 s84, s13, 0\n"
    "s_sub_u32 s83, s83, 2688\n"
    "s_subb_u32 s84, s84, 0\n"
    "s_and_b32 s84, s84, 0xffff\n"
    "v_lshrrev_b32 v247, 3, v254\n"
    "v_and_b32 v248, 7, v254\n"
    "v_and_b32 v249, 2, v247\n"
    "v_xor_b32 v248, v248, v249\n"
    "s_and_b32 s80, s36, 1\n"
    "s_lshl_b32 s80, s80, 2\n"
    "v_xor_b32 v248, s80, v248\n"
    "v_lshlrev_b32 v248, 4, v248\n"
    "v_mul_u32_u24 v247, 896, v247\n"
    "v_add_u32 v235, v247, v248\n"
    "s_mov_b32 s30, 0xea80\n"
    "s_mov_b32 s31, 0x20000\n"
    "s_mov_b32 s40, 0xa80\n"
    "s_mov_b32 s41, 0x700\n"
    "s_mov_b32 s42, 0x380\n"
    "s_mov_b32 s43, 0\n"
    "s_mov_b32 s44, 0x7a80\n"
    "s_mov_b32 s45, 0x7700\n"
    "s_mov_b32 s46, 0x7380\n"
    "s_mov_b32 s47, 0x7000\n"
    "s_mov_b32 s48, 0xc80\n"
    "s_mov_b32 s49, 0x900\n"
    "s_mov_b32 s50, 0x580\n"
    "s_mov_b32 s51, 0x7c80\n"
    "s_mov_b32 s52, 0x7900\n"
    "s_mov_b32 s53, 0x7580\n"
    "s_lshl_b32 s80, s36, 12\n"
    "s_add_u32 s85, s80, 0\n"
    "s_add_u32 s86, s80, 32768\n"
    "s_add_u32 s87, s80, 65536\n"
    "s_add_u32 s80, s17, 0\n"
    "s_and_b32 s80, s80, 7\n"
    "s_lshl_b32 s81, s99, 2\n"
    "s_xor_b32 s81, s81, s80\n"
    "s_mul_i32 s72, s81, 57344\n"
    "s_lshr_b32 s81, s80, 2\n"
    "s_mul_i32 s64, s81, 28672\n"
    "s_and_b32 s80, s80, 3\n"
    "s_xor_b32 s80, s80, s81\n"
    "s_lshl_b32 s56, s80, 7\n"
    "s_add_u32 s80, s17, 1\n"
    "s_and_b32 s80, s80, 7\n"
    "s_lshl_b32 s81, s99, 2\n"
    "s_xor_b32 s81, s81, s80\n"
    "s_mul_i32 s73, s81, 57344\n"
    "s_lshr_b32 s81, s80, 2\n"
    "s_mul_i32 s65, s81, 28672\n"
    "s_and_b32 s80, s80, 3\n"
    "s_xor_b32 s80, s80, s81\n"
    "s_lshl_b32 s57, s80, 7\n"
    "s_add_u32 s80, s17, 2\n"
    "s_and_b32 s80, s80, 7\n"
    "s_lshl_b32 s81, s99, 2\n"
    "s_xor_b32 s81, s81, s80\n"
    "s_mul_i32 s74, s81, 57344\n"
    "s_lshr_b32 s81, s80, 2\n"
    "s_mul_i32 s66, s81, 28672\n"
    "s_and_b32 s80, s80, 3\n"
    "s_xor_b32 s80, s80, s81\n"
    "s_lshl_b32 s58, s80, 7\n"
    "s_add_u32 s80, s17, 3\n"
    "s_and_b32 s80, s80, 7\n"
    "s_lshl_b32 s81, s99, 2\n"
    "s_xor_b32 s81, s81, s80\n"
    "s_mul_i32 s75, s81, 57344\n"
    "s_lshr_b32 s81, s80, 2\n"
    "s_mul_i32 s67, s81, 28672\n"
    "s_and_b32 s80, s80, 3\n"
    "s_xor_b32 s80, s80, s81\n"
    "s_lshl_b32 s59, s80, 7\n"
    "s_add_u32 s80, s17, 4\n"
    "s_and_b32 s80, s80, 7\n"
    "s_lshl_b32 s81, s99, 2\n"
    "s_xor_b32 s81, s81, s80\n"
    "s_mul_i32 s76, s81, 57344\n"
    "s_lshr_b32 s81, s80, 2\n"
    "s_mul_i32 s68, s81, 28672\n"
    "s_and_b32 s80, s80, 3\n"
    "s_xor_b32 s80, s80, s81\n"
    "s_lshl_b32 s60, s80, 7\n"
    "s_add_u32 s80, s17, 5\n"
    "s_and_b32 s80, s80, 7\n"
    "s_lshl_b32 s81, s99, 2\n"
    "s_xor_b32 s81, s81, s80\n"
    "s_mul_i32 s77, s81, 57344\n"
    "s_lshr_b32 s81, s80, 2\n"
    "s_mul_i32 s69, s81, 28672\n"
    "s_and_b32 s80, s80, 3\n"
    "s_xor_b32 s80, s80, s81\n"
    "s_lshl_b32 s61, s80, 7\n"
    "s_add_u32 s80, s17, 6\n"
    "s_and_b32 s80, s80, 7\n"
    "s_lshl_b32 s81, s99, 2\n"
    "s_xor_b32 s81, s81, s80\n"
    "s_mul_i32 s78, s81, 57344\n"
    "s_lshr_b32 s81, s80, 2\n"
    "s_mul_i32 s70, s81, 28672\n"
    "s_and_b32 s80, s80, 3\n"
    "s_xor_b32 s80, s80, s81\n"
    "s_lshl_b32 s62, s80, 7\n"
    "s_add_u32 s80, s17, 7\n"
    "s_and_b32 s80, s80, 7\n"
    "s_lshl_b32 s81, s99, 2\n"
    "s_xor_b32 s81, s81, s80\n"
    "s_mul_i32 s79, s81, 57344\n"
    "s_lshr_b32 s81, s80, 2\n"
    "s_mul_i32 s71, s81, 28672\n"
    "s_and_b32 s80, s80, 3\n"
    "s_xor_b32 s80, s80, s81\n"
    "s_lshl_b32 s63, s80, 7\n"
    "v_and_b32 v247, 15, v254\n"
    "v_lshrrev_b32 v248, 4, v254\n"
    "v_lshlrev_b32 v249, 4, v247\n"
    "v_add_u32 v224, 159744, v249\n"
    "s_lshl_b32 s80, s36, 2\n"
    "v_add_u32 v223, s80, v224\n"
    "v_lshrrev_b32 v249, 1, v248\n"
    "s_lshl_b32 s80, s36, 3\n"
    "v_add_u32 v249, s80, v249\n"
    "v_xor_b32 v249, v249, v247\n"
    "v_lshlrev_b32 v249, 4, v249\n"
    "v_and_b32 v248, 1, v248\n"
    "v_lshlrev_b32 v248, 3, v248\n"
    "v_add_u32 v249, v249, v248\n"
    "v_lshlrev_b32 v247, 9, v247\n"
    "v_add_u32 v249, v249, v247\n"
    "v_add_u32 v234, 98304, v249\n"
    "s_waitcnt lgkmcnt(12)\n"
    "v_mfma_f32_16x16x32_bf16 v[176:179], v[112:115], a[0:3], 0\n"
    "v_mfma_f32_16x16x32_bf16 v[176:179], v[116:119], a[4:7], v[176:179]\n"
    "v_mfma_f32_16x16x32_bf16 v[176:179], v[120:123], a[8:11], v[176:179]\n"
    "v_mfma_f32_16x16x32_bf16 v[176:179], v[124:127], a[12:15], v[176:179]\n"
    "s_waitcnt lgkmcnt(8)\n"
    "v_mfma_f32_16x16x32_bf16 v[176:179], v[128:131], a[16:19], v[176:179]\n"
    "v_mfma_f32_16x16x32_bf16 v[176:179], v[132:135], a[20:23], v[176:179]\n"
    "v_mfma_f32_16x16x32_bf16 v[176:179], v[136:139], a[24:27], v[176:179]\n"
    "v_mfma_f32_16x16x32_bf16 v[176:179], v[140:143], a[28:31], v[176:179]\n"
    "s_waitcnt lgkmcnt(4)\n"
    "v_mfma_f32_16x16x32_bf16 v[180:183], v[144:147], a[0:3], 0\n"
    "v_mfma_f32_16x16x32_bf16 v[180:183], v[148:151], a[4:7], v[180:183]\n"
    "v_mfma_f32_16x16x32_bf16 v[180:183], v[152:155], a[8:11], v[180:183]\n"
    "v_mfma_f32_16x16x32_bf16 v[180:183], v[156:159], a[12:15], v[180:183]\n"
    "s_nop 3\n"
    "v_sub_f32 v176, v176, v216\n"
    "v_exp_f32 v176, v176\n"
    "s_waitcnt lgkmcnt(0)\n"
    "v_mfma_f32_16x16x32_bf16 v[180:183], v[160:163], a[16:19], v[180:183]\n"
    "v_sub_f32 v177, v177, v216\n"
    "v_exp_f32 v177, v177\n"
    "v_mfma_f32_16x16x32_bf16 v[180:183], v[164:167], a[20:23], v[180:183]\n"
    "v_sub_f32 v178, v178, v216\n"
    "v_exp_f32 v178, v178\n"
    "v_mfma_f32_16x16x32_bf16 v[180:183], v[168:171], a[24:27], v[180:183]\n"
    "v_sub_f32 v179, v179, v216\n"
    "v_exp_f32 v179, v179\n"
    "v_mfma_f32_16x16x32_bf16 v[180:183], v[172:175], a[28:31], v[180:183]\n"
    "v_cvt_pk_bf16_f32 v192, v176, v177\n"
    "v_cvt_pk_bf16_f32 v193, v178, v179\n"
    "v_mfma_f32_16x16x32_bf16 v[184:187], v[112:115], a[32:35], 0\n"
    "ds_write_b64 v229, v[192:193]\n"
    "v_mfma_f32_16x16x32_bf16 v[184:187], v[116:119], a[36:39], v[184:187]\n"
    "v_mfma_f32_16x16x32_bf16 v[184:187], v[120:123], a[40:43], v[184:187]\n"
    "v_mfma_f32_16x16x32_bf16 v[184:187], v[124:127], a[44:47], v[184:187]\n"
    "s_barrier\n"
    "v_sub_f32 v180, v180, v216\n"
    "v_exp_f32 v180, v180\n"
    "v_mfma_f32_16x16x32_bf16 v[184:187], v[128:131], a[48:51], v[184:187]\n"
    "v_sub_f32 v181, v181, v216\n"
    "v_exp_f32 v181, v181\n"
    "v_mfma_f32_16x16x32_bf16 v[184:187], v[132:135], a[52:55], v[184:187]\n"
    "v_sub_f32 v182, v182, v216\n"
    "v_exp_f32 v182, v182\n"
    "s_add_u32 s28, s83, s72\n"
    "s_addc_u32 s29, s84, 0\n"
    "v_mfma_f32_16x16x32_bf16 v[184:187], v[136:139], a[56:59], v[184:187]\n"
    "v_sub_f32 v183, v183, v216\n"
    "v_exp_f32 v183, v183\n"
    "v_mfma_f32_16x16x32_bf16 v[184:187], v[140:143], a[60:63], v[184:187]\n"
    "v_cvt_pk_bf16_f32 v194, v180, v181\n"
    "v_cvt_pk_bf16_f32 v195, v182, v183\n"
    "v_mfma_f32_16x16x32_bf16 v[188:191], v[144:147], a[32:35], 0\n"
    "ds_write_b64 v229, v[194:195] offset:8\n"
    "v_mfma_f32_16x16x32_bf16 v[188:191], v[148:151], a[36:39], v[188:191]\n"
    "v_mfma_f32_16x16x32_bf16 a[224:227], a[252:255], v[192:195], a[224:227]\n"
    "v_mfma_f32_16x16x32_bf16 v[188:191], v[152:155], a[40:43], v[188:191]\n"
    "v_mfma_f32_16x16x32_bf16 v[188:191], v[156:159], a[44:47], v[188:191]\n"
    "v_sub_f32 v184, v184, v217\n"
    "v_exp_f32 v184, v184\n"
    "v_mfma_f32_16x16x32_bf16 v[188:191], v[160:163], a[48:51], v[188:191]\n"
    "v_sub_f32 v185, v185, v217\n"
    "v_exp_f32 v185, v185\n"
    "v_mfma_f32_16x16x32_bf16 v[188:191], v[164:167], a[52:55], v[188:191]\n"
    "v_sub_f32 v186, v186, v217\n"
    "v_exp_f32 v186, v186\n"
    "v_mfma_f32_16x16x32_bf16 v[188:191], v[168:171], a[56:59], v[188:191]\n"
    "v_sub_f32 v187, v187, v217\n"
    "v_exp_f32 v187, v187\n"
    "v_mfma_f32_16x16x32_bf16 v[188:191], v[172:175], a[60:63], v[188:191]\n"
    "v_cvt_pk_bf16_f32 v196, v184, v185\n"
    "v_cvt_pk_bf16_f32 v197, v186, v187\n"
    "v_mfma_f32_16x16x32_bf16 v[176:179], v[112:115], a[64:67], 0\n"
    "ds_write_b64 v229, v[196:197] offset:4096\n"
    "s_mov_b32 m0, s85\n"
    "v_mfma_f32_16x16x32_bf16 v[176:179], v[116:119], a[68:71], v[176:179]\n"
    "buffer_load_dwordx4 v235, s[28:31], s40 offen lds\n"
    "v_mfma_f32_16x16x32_bf16 v[176:179], v[120:123], a[72:75], v[176:179]\n"
    "v_mfma_f32_16x16x32_bf16 v[176:179], v[124:127], a[76:79], v[176:179]\n"
    "v_sub_f32 v188, v188, v217\n"
    "v_exp_f32 v188, v188\n"
    "v_mfma_f32_16x16x32_bf16 v[176:179], v[128:131], a[80:83], v[176:179]\n"
    "v_sub_f32 v189, v189, v217\n"
    "v_exp_f32 v189, v189\n"
    "v_mfma_f32_16x16x32_bf16 v[176:179], v[132:135], a[84:87], v[176:179]\n"
    "v_sub_f32 v190, v190, v217\n"
    "v_exp_f32 v190, v190\n"
    "v_mfma_f32_16x16x32_bf16 v[176:179], v[136:139], a[88:91], v[176:179]\n"
    "v_sub_f32 v191, v191, v217\n"
    "v_exp_f32 v191, v191\n"
    "v_mfma_f32_16x16x32_bf16 v[176:179], v[140:143], a[92:95], v[176:179]\n"
    "v_cvt_pk_bf16_f32 v198, v188, v189\n"
    "v_cvt_pk_bf16_f32 v199, v190, v191\n"
    "v_mfma_f32_16x16x32_bf16 v[180:183], v[144:147], a[64:67], 0\n"
    "ds_write_b64 v229, v[198:199] offset:4104\n"
    "v_mfma_f32_16x16x32_bf16 v[180:183], v[148:151], a[68:71], v[180:183]\n"
    "v_mfma_f32_16x16x32_bf16 a[228:231], a[252:255], v[196:199], a[228:231]\n"
    "v_mfma_f32_16x16x32_bf16 v[180:183], v[152:155], a[72:75], v[180:183]\n"
    "buffer_load_dwordx4 v235, s[28:31], s41 offen offset:1024 lds\n"
    "v_mfma_f32_16x16x32_bf16 v[180:183], v[156:159], a[76:79], v[180:183]\n"
    "v_sub_f32 v176, v176, v218\n"
    "v_exp_f32 v176, v176\n"
    "v_mfma_f32_16x16x32_bf16 v[180:183], v[160:163], a[80:83], v[180:183]\n"
    "v_sub_f32 v177, v177, v218\n"
    "v_exp_f32 v177, v177\n"
    "v_mfma_f32_16x16x32_bf16 v[180:183], v[164:167], a[84:87], v[180:183]\n"
    "v_sub_f32 v178, v178, v218\n"
    "v_exp_f32 v178, v178\n"
    "v_mfma_f32_16x16x32_bf16 v[180:183], v[168:171], a[88:91], v[180:183]\n"
    "v_sub_f32 v179, v179, v218\n"
    "v_exp_f32 v179, v179\n"
    "v_mfma_f32_16x16x32_bf16 v[180:183], v[172:175], a[92:95], v[180:183]\n"
    "v_cvt_pk_bf16_f32 v192, v176, v177\n"
    "v_cvt_pk_bf16_f32 v193, v178, v179\n"
    "v_mfma_f32_16x16x32_bf16 v[184:187], v[112:115], a[96:99], 0\n"
    "ds_write_b64 v229, v[192:193] offset:8192\n"
    "v_mfma_f32_16x16x32_bf16 v[184:187], v[116:119], a[100:103], v[184:187]\n"
    "buffer_load_dwordx4 v235, s[28:31], s42 offen offset:2048 lds\n"
    "v_mfma_f32_16x16x32_bf16 v[184:187], v[120:123], a[104:107], v[184:187]\n"
    "v_mfma_f32_16x16x32_bf16 v[184:187], v[124:127], a[108:111], v[184:187]\n"
    "v_sub_f32 v180, v180, v218\n"
    "v_exp_f32 v180, v180\n"
    "v_mfma_f32_16x16x32_bf16 v[184:187], v[128:131], a[112:115], v[184:187]\n"
    "v_sub_f32 v181, v181, v218\n"
    "v_exp_f32 v181, v181\n"
    "v_mfma_f32_16x16x32_bf16 v[184:187], v[132:135], a[116:119], v[184:187]\n"
    "v_sub_f32 v182, v182, v218\n"
    "v_exp_f32 v182, v182\n"
    "v_mfma_f32_16x16x32_bf16 v[184:187], v[136:139], a[120:123], v[184:187]\n"
    "v_sub_f32 v183, v183, v218\n"
    "v_exp_f32 v183, v183\n"
    "v_mfma_f32_16x16x32_bf16 v[184:187], v[140:143], a[124:127], v[184:187]\n"
    "v_cvt_pk_bf16_f32 v194, v180, v181\n"
    "v_cvt_pk_bf16_f32 v195, v182, v183\n"
    "v_mfma_f32_16x16x32_bf16 v[188:191], v[144:147], a[96:99], 0\n"
    "ds_write_b64 v229, v[194:195] offset:8200\n"
    "v_mfma_f32_16x16x32_bf16 v[188:191], v[148:151], a[100:103], v[188:191]\n"
    "v_mfma_f32_16x16x32_bf16 a[232:235], a[252:255], v[192:195], a[232:235]\n"
    "v_mfma_f32_16x16x32_bf16 v[188:191], v[152:155], a[104:107], v[188:191]\n"
    "buffer_load_dwordx4 v235, s[28:31], s43 offen offset:3072 lds\n"
    "v_mfma_f32_16x16x32_bf16 v[188:191], v[156:159], a[108:111], v[188:191]\n"
    "v_sub_f32 v184, v184, v219\n"
    "v_exp_f32 v184, v184\n"
    "v_mfma_f32_16x16x32_bf16 v[188:191], v[160:163], a[112:115], v[188:191]\n"
    "v_sub_f32 v185, v185, v219\n"
    "v_exp_f32 v185, v185\n"
    "v_mfma_f32_16x16x32_bf16 v[188:191], v[164:167], a[116:119], v[188:191]\n"
    "v_sub_f32 v186, v186, v219\n"
    "v_exp_f32 v186, v186\n"
    "v_mfma_f32_16x16x32_bf16 v[188:191], v[168:171], a[120:123], v[188:191]\n"
    "v_sub_f32 v187, v187, v219\n"
    "v_exp_f32 v187, v187\n"
    "v_mfma_f32_16x16x32_bf16 v[188:191], v[172:175], a[124:127], v[188:191]\n"
    "v_cvt_pk_bf16_f32 v196, v184, v185\n"
    "v_cvt_pk_bf16_f32 v197, v186, v187\n"
    "v_mfma_f32_16x16x32_bf16 v[176:179], v[112:115], a[128:131], 0\n"
    "ds_write_b64 v229, v[196:197] offset:12288\n"
    "s_add_u32 s38, s85, 16384\n"
    "s_mov_b32 m0, s38\n"
    "v_mfma_f32_16x16x32_bf16 v[176:179], v[116:119], a[132:135], v[176:179]\n"
    "buffer_load_dwordx4 v235, s[28:31], s44 offen lds\n"
    "v_mfma_f32_16x16x32_bf16 v[176:179], v[120:123], a[136:139], v[176:179]\n"
    "v_mfma_f32_16x16x32_bf16 v[176:179], v[124:127], a[140:143], v[176:179]\n"
    "v_sub_f32 v188, v188, v219\n"
    "v_exp_f32 v188, v188\n"
    "v_mfma_f32_16x16x32_bf16 v[176:179], v[128:131], a[144:147], v[176:179]\n"
    "v_sub_f32 v189, v189, v219\n"
    "v_exp_f32 v189, v189\n"
    "v_mfma_f32_16x16x32_bf16 v[176:179], v[132:135], a[148:151], v[176:179]\n"
    "v_sub_f32 v190, v190, v219\n"
    "v_exp_f32 v190, v190\n"
    "v_mfma_f32_16x16x32_bf16 v[176:179], v[136:139], a[152:155], v[176:179]\n"
    "v_sub_f32 v191, v191, v219\n"
    "v_exp_f32 v191, v191\n"
    "v_mfma_f32_16x16x32_bf16 v[176:179], v[140:143], a[156:159], v[176:179]\n"
    "v_cvt_pk_bf16_f32 v198, v188, v189\n"
    "v_cvt_pk_bf16_f32 v199, v190, v191\n"
    "v_mfma_f32_16x16x32_bf16 v[180:183], v[144:147], a[128:131], 0\n"
    "ds_write_b64 v229, v[198:199] offset:12296\n"
    "v_mfma_f32_16x16x32_bf16 v[180:183], v[148:151], a[132:135], v[180:183]\n"
    "v_mfma_f32_16x16x32_bf16 a[236:239], a[252:255], v[196:199], a[236:239]\n"
    "v_mfma_f32_16x16x32_bf16 v[180:183], v[152:155], a[136:139], v[180:183]\n"
    "buffer_load_dwordx4 v235, s[28:31], s45 offen offset:1024 lds\n"
    "v_mfma_f32_16x16x32_bf16 v[180:183], v[156:159], a[140:143], v[180:183]\n"
    "v_sub_f32 v176, v176, v220\n"
    "v_exp_f32 v176, v176\n"
    "v_mfma_f32_16x16x32_bf16 v[180:183], v[160:163], a[144:147], v[180:183]\n"
    "v_sub_f32 v177, v177, v220\n"
    "v_exp_f32 v177, v177\n"
    "v_mfma_f32_16x16x32_bf16 v[180:183], v[164:167], a[148:151], v[180:183]\n"
    "v_sub_f32 v178, v178, v220\n"
    "v_exp_f32 v178, v178\n"
    "v_mfma_f32_16x16x32_bf16 v[180:183], v[168:171], a[152:155], v[180:183]\n"
    "v_sub_f32 v179, v179, v220\n"
    "v_exp_f32 v179, v179\n"
    "v_mfma_f32_16x16x32_bf16 v[180:183], v[172:175], a[156:159], v[180:183]\n"
    "v_cvt_pk_bf16_f32 v192, v176, v177\n"
    "v_cvt_pk_bf16_f32 v193, v178, v179\n"
    "v_mfma_f32_16x16x32_bf16 v[184:187], v[112:115], a[160:163], 0\n"
    "ds_write_b64 v229, v[192:193] offset:16384\n"
    "v_mfma_f32_16x16x32_bf16 v[184:187], v[116:119], a[164:167], v[184:187]\n"
    "buffer_load_dwordx4 v235, s[28:31], s46 offen offset:2048 lds\n"
    "v_mfma_f32_16x16x32_bf16 v[184:187], v[120:123], a[168:171], v[184:187]\n"
    "v_mfma_f32_16x16x32_bf16 v[184:187], v[124:127], a[172:175], v[184:187]\n"
    "v_sub_f32 v180, v180, v220\n"
    "v_exp_f32 v180, v180\n"
    "v_mfma_f32_16x16x32_bf16 v[184:187], v[128:131], a[176:179], v[184:187]\n"
    "v_sub_f32 v181, v181, v220\n"
    "v_exp_f32 v181, v181\n"
    "v_mfma_f32_16x16x32_bf16 v[184:187], v[132:135], a[180:183], v[184:187]\n"
    "v_sub_f32 v182, v182, v220\n"
    "v_exp_f32 v182, v182\n"
    "v_mfma_f32_16x16x32_bf16 v[184:187], v[136:139], a[184:187], v[184:187]\n"
    "v_sub_f32 v183, v183, v220\n"
    "v_exp_f32 v183, v183\n"
    "v_mfma_f32_16x16x32_bf16 v[184:187], v[140:143], a[188:191], v[184:187]\n"
    "v_cvt_pk_bf16_f32 v194, v180, v181\n"
    "v_cvt_pk_bf16_f32 v195, v182, v183\n"
    "v_mfma_f32_16x16x32_bf16 v[188:191], v[144:147], a[160:163], 0\n"
    "ds_write_b64 v229, v[194:195] offset:16392\n"
    "v_mfma_f32_16x16x32_bf16 v[188:191], v[148:151], a[164:167], v[188:191]\n"
    "v_mfma_f32_16x16x32_bf16 a[240:243], a[252:255], v[192:195], a[240:243]\n"
    "v_mfma_f32_16x16x32_bf16 v[188:191], v[152:155], a[168:171], v[188:191]\n"
    "buffer_load_dwordx4 v235, s[28:31], s47 offen offset:3072 lds\n"
    "s_add_u32 s28, s83, s73\n"
    "s_addc_u32 s29, s84, 0\n"
    "v_mfma_f32_16x16x32_bf16 v[188:191], v[156:159], a[172:175], v[188:191]\n"
    "v_sub_f32 v184, v184, v221\n"
    "v_exp_f32 v184, v184\n"
    "v_mfma_f32_16x16x32_bf16 v[188:191], v[160:163], a[176:179], v[188:191]\n"
    "v_sub_f32 v185, v185, v221\n"
    "v_exp_f32 v185, v185\n"
    "v_mfma_f32_16x16x32_bf16 v[188:191], v[164:167], a[180:183], v[188:191]\n"
    "v_sub_f32 v186, v186, v221\n"
    "v_exp_f32 v186, v186\n"
    "v_mfma_f32_16x16x32_bf16 v[188:191], v[168:171], a[184:187], v[188:191]\n"
    "v_sub_f32 v187, v187, v221\n"
    "v_exp_f32 v187, v187\n"
    "v_mfma_f32_16x16x32_bf16 v[188:191], v[172:175], a[188:191], v[188:191]\n"
    "v_cvt_pk_bf16_f32 v196, v184, v185\n"
    "v_cvt_pk_bf16_f32 v197, v186, v187\n"
    "v_mfma_f32_16x16x32_bf16 v[176:179], v[112:115], a[192:195], 0\n"
    "ds_write_b64 v229, v[196:197] offset:20480\n"
    "v_mfma_f32_16x16x32_bf16 v[176:179], v[116:119], a[196:199], v[176:179]\n"
    "s_waitcnt vmcnt(8)\n"
    "ds_read_b64_tr_b16 v[112:113], v225\n"
    "v_mfma_f32_16x16x32_bf16 v[176:179], v[120:123], a[200:203], v[176:179]\n"
    "ds_read_b64_tr_b16 v[114:115], v225 offset:2048\n"
    "v_mfma_f32_16x16x32_bf16 v[176:179], v[124:127], a[204:207], v[176:179]\n"
    "ds_read_b64_tr_b16 v[116:117], v225 offset:4096\n"
    "v_mfma_f32_16x16x32_bf16 v[176:179], v[128:131], a[208:211], v[176:179]\n"
    "ds_read_b64_tr_b16 v[118:119], v225 offset:6144\n"
    "v_mfma_f32_16x16x32_bf16 v[176:179], v[132:135], a[212:215], v[176:179]\n"
    "ds_read_b64_tr_b16 v[120:121], v225 offset:8192\n"
    "v_mfma_f32_16x16x32_bf16 v[176:179], v[136:139], a[216:219], v[176:179]\n"
    "ds_read_b64_tr_b16 v[122:123], v225 offset:10240\n"
    "v_mfma_f32_16x16x32_bf16 v[176:179], v[140:143], a[220:223], v[176:179]\n"
    "s_waitcnt lgkmcnt(7)\n"
    "s_barrier\n"
    "ds_read_b64_tr_b16 v[124:125], v225 offset:12288\n"
    "v_mfma_f32_16x16x32_bf16 v[180:183], v[144:147], a[192:195], 0\n"
    "ds_read_b128 v[200:203], v230\n"
    "ds_read_b128 v[212:215], v233\n"
    "v_mfma_f32_16x16x32_bf16 v[180:183], v[148:151], a[196:199], v[180:183]\n"
    "ds_read_b128 v[204:207], v231\n"
    "v_mfma_f32_16x16x32_bf16 v[180:183], v[152:155], a[200:203], v[180:183]\n"
    "ds_read_b128 v[208:211], v232\n"
    "v_mfma_f32_16x16x32_bf16 v[180:183], v[156:159], a[204:207], v[180:183]\n"
    "ds_read_b64_tr_b16 v[126:127], v225 offset:14336\n"
    "v_mfma_f32_16x16x32_bf16 v[180:183], v[160:163], a[208:211], v[180:183]\n"
    "ds_read_b64_tr_b16 v[128:129], v226\n"
    "ds_read_b64_tr_b16 v[130:131], v226 offset:2048\n"
    "v_mfma_f32_16x16x32_bf16 v[180:183], v[164:167], a[212:215], v[180:183]\n"
    "ds_read_b64_tr_b16 v[132:133], v226 offset:4096\n"
    "ds_read_b64_tr_b16 v[134:135], v226 offset:6144\n"
    "v_mfma_f32_16x16x32_bf16 v[180:183], v[168:171], a[216:219], v[180:183]\n"
    "ds_read_b64_tr_b16 v[136:137], v226 offset:8192\n"
    "ds_read_b64_tr_b16 v[138:139], v226 offset:10240\n"
    "v_mfma_f32_16x16x32_bf16 v[180:183], v[172:175], a[220:223], v[180:183]\n"
    "ds_read_b64_tr_b16 v[140:141], v226 offset:12288\n"
    "ds_read_b64_tr_b16 v[142:143], v226 offset:14336\n"
    "s_waitcnt lgkmcnt(8)\n"
    "v_mfma_f32_16x16x32_bf16 v[0:3], v[112:115], v[200:203], v[0:3]\n"
    "ds_read_b64_tr_b16 v[144:145], v227\n"
    "ds_read_b64_tr_b16 v[146:147], v227 offset:2048\n"
    "v_mfma_f32_16x16x32_bf16 v[0:3], v[116:119], v[204:207], v[0:3]\n"
    "ds_read_b64_tr_b16 v[148:149], v227 offset:4096\n"
    "ds_read_b64_tr_b16 v[150:151], v227 offset:6144\n"
    "v_mfma_f32_16x16x32_bf16 v[0:3], v[120:123], v[208:211], v[0:3]\n"
    "ds_read_b64_tr_b16 v[152:153], v227 offset:8192\n"
    "ds_read_b64_tr_b16 v[154:155], v227 offset:10240\n"
    "v_mfma_f32_16x16x32_bf16 v[0:3], v[124:127], v[212:215], v[0:3]\n"
    "ds_read_b64_tr_b16 v[156:157], v227 offset:12288\n"
    "ds_read_b64_tr_b16 v[158:159], v227 offset:14336\n"
    "s_waitcnt lgkmcnt(8)\n"
    "v_mfma_f32_16x16x32_bf16 v[4:7], v[128:131], v[200:203], v[4:7]\n"
    "ds_read_b64_tr_b16 v[160:161], v228\n"
    "ds_read_b64_tr_b16 v[162:163], v228 offset:2048\n"
    "v_mfma_f32_16x16x32_bf16 v[4:7], v[132:135], v[204:207], v[4:7]\n"
    "ds_read_b64_tr_b16 v[164:165], v228 offset:4096\n"
    "ds_read_b64_tr_b16 v[166:167], v228 offset:6144\n"
    "v_mfma_f32_16x16x32_bf16 v[4:7], v[136:139], v[208:211], v[4:7]\n"
    "ds_read_b64_tr_b16 v[168:169], v228 offset:8192\n"
    "ds_read_b64_tr_b16 v[170:171], v228 offset:10240\n"
    "v_mfma_f32_16x16x32_bf16 v[4:7], v[140:143], v[212:215], v[4:7]\n"
    "ds_read_b64_tr_b16 v[172:173], v228 offset:12288\n"
    "ds_read_b64_tr_b16 v[174:175], v228 offset:14336\n"
    "s_waitcnt lgkmcnt(8)\n"
    "v_mfma_f32_16x16x32_bf16 v[8:11], v[144:147], v[200:203], v[8:11]\n"
    "ds_read_b128 v[236:239], v230 offset:4096\n"
    "v_mfma_f32_16x16x32_bf16 v[8:11], v[148:151], v[204:207], v[8:11]\n"
    "ds_read_b128 v[240:243], v231 offset:4096\n"
    "v_mfma_f32_16x16x32_bf16 v[8:11], v[152:155], v[208:211], v[8:11]\n"
    "ds_read_b128 v[244:247], v232 offset:4096\n"
    "ds_read_b128 v[248:251], v233 offset:4096\n"
    "v_mfma_f32_16x16x32_bf16 v[8:11], v[156:159], v[212:215], v[8:11]\n"
    "v_sub_f32 v188, v188, v221\n"
    "v_exp_f32 v188, v188\n"
    "s_waitcnt lgkmcnt(4)\n"
    "v_mfma_f32_16x16x32_bf16 v[12:15], v[160:163], v[200:203], v[12:15]\n"
    "v_sub_f32 v189, v189, v221\n"
    "v_exp_f32 v189, v189\n"
    "v_mfma_f32_16x16x32_bf16 v[12:15], v[164:167], v[204:207], v[12:15]\n"
    "v_sub_f32 v190, v190, v221\n"
    "v_exp_f32 v190, v190\n"
    "v_mfma_f32_16x16x32_bf16 v[12:15], v[168:171], v[208:211], v[12:15]\n"
    "v_sub_f32 v191, v191, v221\n"
    "v_exp_f32 v191, v191\n"
    "v_mfma_f32_16x16x32_bf16 v[12:15], v[172:175], v[212:215], v[12:15]\n"
    "v_cvt_pk_bf16_f32 v198, v188, v189\n"
    "v_cvt_pk_bf16_f32 v199, v190, v191\n"
    "s_waitcnt lgkmcnt(0)\n"
    "v_mfma_f32_16x16x32_bf16 v[16:19], v[112:115], v[236:239], v[16:19]\n"
    "ds_read_b128 v[200:203], v230 offset:8192\n"
    "v_mfma_f32_16x16x32_bf16 v[16:19], v[116:119], v[240:243], v[16:19]\n"
    "ds_read_b128 v[204:207], v231 offset:8192\n"
    "v_mfma_f32_16x16x32_bf16 v[16:19], v[120:123], v[244:247], v[16:19]\n"
    "ds_read_b128 v[208:211], v232 offset:8192\n"
    "v_mfma_f32_16x16x32_bf16 v[16:19], v[124:127], v[248:251], v[16:19]\n"
    "ds_read_b128 v[212:215], v233 offset:8192\n"
    "v_mfma_f32_16x16x32_bf16 v[20:23], v[128:131], v[236:239], v[20:23]\n"
    "ds_write_b64 v229, v[198:199] offset:20488\n"
    "v_mfma_f32_16x16x32_bf16 v[20:23], v[132:135], v[240:243], v[20:23]\n"
    "v_mfma_f32_16x16x32_bf16 a[244:247], a[252:255], v[196:199], a[244:247]\n"
    "v_mfma_f32_16x16x32_bf16 v[20:23], v[136:139], v[244:247], v[20:23]\n"
    "v_sub_f32 v176, v176, v222\n"
    "v_exp_f32 v176, v176\n"
    "v_mfma_f32_16x16x32_bf16 v[20:23], v[140:143], v[248:251], v[20:23]\n"
    "v_sub_f32 v177, v177, v222\n"
    "v_exp_f32 v177, v177\n"
    "v_mfma_f32_16x16x32_bf16 v[24:27], v[144:147], v[236:239], v[24:27]\n"
    "v_sub_f32 v178, v178, v222\n"
    "v_exp_f32 v178, v178\n"
    "v_mfma_f32_16x16x32_bf16 v[24:27], v[148:151], v[240:243], v[24:27]\n"
    "v_sub_f32 v179, v179, v222\n"
    "v_exp_f32 v179, v179\n"
    "v_mfma_f32_16x16x32_bf16 v[24:27], v[152:155], v[244:247], v[24:27]\n"
    "v_cvt_pk_bf16_f32 v192, v176, v177\n"
    "v_cvt_pk_bf16_f32 v193, v178, v179\n"
    "v_mfma_f32_16x16x32_bf16 v[24:27], v[156:159], v[248:251], v[24:27]\n"
    "ds_write_b64 v229, v[192:193] offset:24576\n"
    "v_mfma_f32_16x16x32_bf16 v[28:31], v[160:163], v[236:239], v[28:31]\n"
    "v_sub_f32 v180, v180, v222\n"
    "v_exp_f32 v180, v180\n"
    "v_mfma_f32_16x16x32_bf16 v[28:31], v[164:167], v[240:243], v[28:31]\n"
    "v_sub_f32 v181, v181, v222\n"
    "v_exp_f32 v181, v181\n"
    "v_mfma_f32_16x16x32_bf16 v[28:31], v[168:171], v[244:247], v[28:31]\n"
    "v_sub_f32 v182, v182, v222\n"
    "v_exp_f32 v182, v182\n"
    "v_mfma_f32_16x16x32_bf16 v[28:31], v[172:175], v[248:251], v[28:31]\n"
    "v_sub_f32 v183, v183, v222\n"
    "v_exp_f32 v183, v183\n"
    "s_waitcnt lgkmcnt(2)\n"
    "v_mfma_f32_16x16x32_bf16 v[32:35], v[112:115], v[200:203], v[32:35]\n"
    "ds_read_b128 v[236:239], v230 offset:12288\n"
    "v_cvt_pk_bf16_f32 v194, v180, v181\n"
    "v_mfma_f32_16x16x32_bf16 v[32:35], v[116:119], v[204:207], v[32:35]\n"
    "ds_read_b128 v[240:243], v231 offset:12288\n"
    "v_cvt_pk_bf16_f32 v195, v182, v183\n"
    "v_mfma_f32_16x16x32_bf16 v[32:35], v[120:123], v[208:211], v[32:35]\n"
    "ds_read_b128 v[244:247], v232 offset:12288\n"
    "v_xor_b32 v225, 0, v234\n"
    "v_mfma_f32_16x16x32_bf16 v[32:35], v[124:127], v[212:215], v[32:35]\n"
    "ds_read_b128 v[248:251], v233 offset:12288\n"
    "v_xor_b32 v226, 32, v234\n"
    "v_mfma_f32_16x16x32_bf16 v[36:39], v[128:131], v[200:203], v[36:39]\n"
    "ds_write_b64 v229, v[194:195] offset:24584\n"
    "v_mfma_f32_16x16x32_bf16 v[36:39], v[132:135], v[204:207], v[36:39]\n"
    "v_mfma_f32_16x16x32_bf16 a[248:251], a[252:255], v[192:195], a[248:251]\n"
    "s_mov_b32 m0, s86\n"
    "v_mfma_f32_16x16x32_bf16 v[36:39], v[136:139], v[208:211], v[36:39]\n"
    "buffer_load_dwordx4 v235, s[28:31], s40 offen lds\n"
    "v_mfma_f32_16x16x32_bf16 v[36:39], v[140:143], v[212:215], v[36:39]\n"
    "v_xor_b32 v227, 64, v234\n"
    "v_xor_b32 v228, 96, v234\n"
    "v_mfma_f32_16x16x32_bf16 v[40:43], v[144:147], v[200:203], v[40:43]\n"
    "v_mfma_f32_16x16x32_bf16 v[40:43], v[148:151], v[204:207], v[40:43]\n"
    "v_accvgpr_read_b32 v216, a224\n"
    "ds_write_b32 v223, v216\n"
    "v_mfma_f32_16x16x32_bf16 v[40:43], v[152:155], v[208:211], v[40:43]\n"
    "v_accvgpr_read_b32 v217, a228\n"
    "ds_write_b32 v223, v217 offset:256\n"
    "v_mfma_f32_16x16x32_bf16 v[40:43], v[156:159], v[212:215], v[40:43]\n"
    "v_accvgpr_read_b32 v218, a232\n"
    "ds_write_b32 v223, v218 offset:512\n"
    "v_mfma_f32_16x16x32_bf16 v[44:47], v[160:163], v[200:203], v[44:47]\n"
    "buffer_load_dwordx4 v235, s[28:31], s41 offen offset:1024 lds\n"
    "v_mfma_f32_16x16x32_bf16 v[44:47], v[164:167], v[204:207], v[44:47]\n"
    "v_accvgpr_read_b32 v219, a236\n"
    "ds_write_b32 v223, v219 offset:768\n"
    "v_mfma_f32_16x16x32_bf16 v[44:47], v[168:171], v[208:211], v[44:47]\n"
    "v_accvgpr_read_b32 v220, a240\n"
    "ds_write_b32 v223, v220 offset:1024\n"
    "v_mfma_f32_16x16x32_bf16 v[44:47], v[172:175], v[212:215], v[44:47]\n"
    "v_accvgpr_read_b32 v221, a244\n"
    "ds_write_b32 v223, v221 offset:1280\n"
    "s_waitcnt lgkmcnt(7)\n"
    "v_mfma_f32_16x16x32_bf16 v[48:51], v[112:115], v[236:239], v[48:51]\n"
    "ds_read_b128 v[200:203], v230 offset:16384\n"
    "v_accvgpr_read_b32 v222, a248\n"
    "v_mfma_f32_16x16x32_bf16 v[48:51], v[116:119], v[240:243], v[48:51]\n"
    "ds_read_b128 v[204:207], v231 offset:16384\n"
    "v_mfma_f32_16x16x32_bf16 v[48:51], v[120:123], v[244:247], v[48:51]\n"
    "ds_read_b128 v[208:211], v232 offset:16384\n"
    "v_mfma_f32_16x16x32_bf16 v[48:51], v[124:127], v[248:251], v[48:51]\n"
    "ds_read_b128 v[212:215], v233 offset:16384\n"
    "v_mfma_f32_16x16x32_bf16 v[52:55], v[128:131], v[236:239], v[52:55]\n"
    "buffer_load_dwordx4 v235, s[28:31], s42 offen offset:2048 lds\n"
    "v_mfma_f32_16x16x32_bf16 v[52:55], v[132:135], v[240:243], v[52:55]\n"
    "ds_write_b32 v223, v222 offset:1536\n"
    "v_mfma_f32_16x16x32_bf16 v[52:55], v[136:139], v[244:247], v[52:55]\n"
    "v_mfma_f32_16x16x32_bf16 v[52:55], v[140:143], v[248:251], v[52:55]\n"
    "v_mfma_f32_16x16x32_bf16 v[56:59], v[144:147], v[236:239], v[56:59]\n"
    "v_mfma_f32_16x16x32_bf16 v[56:59], v[148:151], v[240:243], v[56:59]\n"
    "s_waitcnt lgkmcnt(0)\n"
    "s_barrier\n"
    "v_mfma_f32_16x16x32_bf16 v[56:59], v[152:155], v[244:247], v[56:59]\n"
    "buffer_load_dwordx4 v235, s[28:31], s43 offen offset:3072 lds\n"
    "v_mfma_f32_16x16x32_bf16 v[56:59], v[156:159], v[248:251], v[56:59]\n"
    "ds_read_b128 v[176:179], v224\n"
    "v_mfma_f32_16x16x32_bf16 v[60:63], v[160:163], v[236:239], v[60:63]\n"
    "ds_read_b128 v[180:183], v224 offset:256\n"
    "v_mfma_f32_16x16x32_bf16 v[60:63], v[164:167], v[240:243], v[60:63]\n"
    "ds_read_b128 v[184:187], v224 offset:512\n"
    "v_mfma_f32_16x16x32_bf16 v[60:63], v[168:171], v[244:247], v[60:63]\n"
    "ds_read_b128 v[188:191], v224 offset:768\n"
    "v_mfma_f32_16x16x32_bf16 v[60:63], v[172:175], v[248:251], v[60:63]\n"
    "s_add_u32 s38, s86, 16384\n"
    "s_mov_b32 m0, s38\n"
    "ds_read_b128 v[192:195], v224 offset:1024\n"
    "v_mfma_f32_16x16x32_bf16 v[64:67], v[112:115], v[200:203], v[64:67]\n"
    "s_barrier\n"
    "buffer_load_dwordx4 v235, s[28:31], s44 offen lds\n"
    "v_mfma_f32_16x16x32_bf16 v[64:67], v[116:119], v[204:207], v[64:67]\n"
    "ds_read_b128 v[236:239], v230 offset:20480\n"
    "s_waitcnt lgkmcnt(5)\n"
    "v_add_f32 v176, v176, v177\n"
    "v_mfma_f32_16x16x32_bf16 v[64:67], v[120:123], v[208:211], v[64:67]\n"
    "ds_read_b128 v[240:243], v231 offset:20480\n"
    "v_add_f32 v178, v178, v179\n"
    "v_mfma_f32_16x16x32_bf16 v[64:67], v[124:127], v[212:215], v[64:67]\n"
    "ds_read_b128 v[244:247], v232 offset:20480\n"
    "v_add_f32 v216, v176, v178\n"
    "v_mfma_f32_16x16x32_bf16 v[68:71], v[128:131], v[200:203], v[68:71]\n"
    "ds_read_b128 v[248:251], v233 offset:20480\n"
    "s_waitcnt lgkmcnt(7)\n"
    "v_add_f32 v180, v180, v181\n"
    "v_mfma_f32_16x16x32_bf16 v[68:71], v[132:135], v[204:207], v[68:71]\n"
    "v_rcp_f32 v216, v216\n"
    "v_mfma_f32_16x16x32_bf16 v[68:71], v[136:139], v[208:211], v[68:71]\n"
    "buffer_load_dwordx4 v235, s[28:31], s45 offen offset:1024 lds\n"
    "v_mfma_f32_16x16x32_bf16 v[68:71], v[140:143], v[212:215], v[68:71]\n"
    "v_add_f32 v182, v182, v183\n"
    "v_add_f32 v217, v180, v182\n"
    "v_mfma_f32_16x16x32_bf16 v[72:75], v[144:147], v[200:203], v[72:75]\n"
    "v_rcp_f32 v217, v217\n"
    "v_mfma_f32_16x16x32_bf16 v[72:75], v[148:151], v[204:207], v[72:75]\n"
    "s_waitcnt lgkmcnt(6)\n"
    "v_add_f32 v184, v184, v185\n"
    "v_add_f32 v186, v186, v187\n"
    "v_mfma_f32_16x16x32_bf16 v[72:75], v[152:155], v[208:211], v[72:75]\n"
    "v_add_f32 v218, v184, v186\n"
    "s_waitcnt lgkmcnt(5)\n"
    "v_add_f32 v188, v188, v189\n"
    "v_mfma_f32_16x16x32_bf16 v[72:75], v[156:159], v[212:215], v[72:75]\n"
    "v_rcp_f32 v218, v218\n"
    "v_mfma_f32_16x16x32_bf16 v[76:79], v[160:163], v[200:203], v[76:79]\n"
    "buffer_load_dwordx4 v235, s[28:31], s46 offen offset:2048 lds\n"
    "v_mfma_f32_16x16x32_bf16 v[76:79], v[164:167], v[204:207], v[76:79]\n"
    "v_add_f32 v190, v190, v191\n"
    "v_add_f32 v219, v188, v190\n"
    "v_mfma_f32_16x16x32_bf16 v[76:79], v[168:171], v[208:211], v[76:79]\n"
    "v_rcp_f32 v219, v219\n"
    "v_mfma_f32_16x16x32_bf16 v[76:79], v[172:175], v[212:215], v[76:79]\n"
    "s_waitcnt lgkmcnt(4)\n"
    "v_add_f32 v192, v192, v193\n"
    "v_add_f32 v194, v194, v195\n"
    "s_waitcnt lgkmcnt(0)\n"
    "v_mfma_f32_16x16x32_bf16 v[80:83], v[112:115], v[236:239], v[80:83]\n"
    "ds_read_b128 v[200:203], v230 offset:24576\n"
    "v_add_f32 v220, v192, v194\n"
    "v_mfma_f32_16x16x32_bf16 v[80:83], v[116:119], v[240:243], v[80:83]\n"
    "ds_read_b128 v[204:207], v231 offset:24576\n"
    "v_mfma_f32_16x16x32_bf16 v[80:83], v[120:123], v[244:247], v[80:83]\n"
    "ds_read_b128 v[208:211], v232 offset:24576\n"
    "v_mfma_f32_16x16x32_bf16 v[80:83], v[124:127], v[248:251], v[80:83]\n"
    "ds_read_b128 v[212:215], v233 offset:24576\n"
    "v_mfma_f32_16x16x32_bf16 v[84:87], v[128:131], v[236:239], v[84:87]\n"
    "buffer_load_dwordx4 v235, s[28:31], s47 offen offset:3072 lds\n"
    "s_add_u32 s28, s83, s74\n"
    "s_addc_u32 s29, s84, 0\n"
    "v_mfma_f32_16x16x32_bf16 v[84:87], v[132:135], v[240:243], v[84:87]\n"
    "v_rcp_f32 v220, v220\n"
    "v_mfma_f32_16x16x32_bf16 v[84:87], v[136:139], v[244:247], v[84:87]\n"
    "ds_read_b128 v[196:199], v224 offset:1280\n"
    "v_mfma_f32_16x16x32_bf16 v[84:87], v[140:143], v[248:251], v[84:87]\n"
    "s_mov_b32 m0, s87\n"
    "ds_read_b128 v[176:179], v224 offset:1536\n"
    "v_mfma_f32_16x16x32_bf16 v[88:91], v[144:147], v[236:239], v[88:91]\n"
    "buffer_load_dwordx4 v235, s[28:31], s40 offen lds\n"
    "v_mfma_f32_16x16x32_bf16 v[88:91], v[148:151], v[240:243], v[88:91]\n"
    "v_mfma_f32_16x16x32_bf16 v[88:91], v[152:155], v[244:247], v[88:91]\n"
    "v_mfma_f32_16x16x32_bf16 v[88:91], v[156:159], v[248:251], v[88:91]\n"
    "s_waitcnt lgkmcnt(1)\n"
    "v_add_f32 v196, v196, v197\n"
    "v_add_f32 v198, v198, v199\n"
    "v_mfma_f32_16x16x32_bf16 v[92:95], v[160:163], v[236:239], v[92:95]\n"
    "buffer_load_dwordx4 v235, s[28:31], s41 offen offset:1024 lds\n"
    "v_mfma_f32_16x16x32_bf16 v[92:95], v[164:167], v[240:243], v[92:95]\n"
    "v_add_f32 v221, v196, v198\n"
    "s_waitcnt lgkmcnt(0)\n"
    "v_add_f32 v176, v176, v177\n"
    "v_mfma_f32_16x16x32_bf16 v[92:95], v[168:171], v[244:247], v[92:95]\n"
    "v_rcp_f32 v221, v221\n"
    "v_mfma_f32_16x16x32_bf16 v[92:95], v[172:175], v[248:251], v[92:95]\n"
    "v_add_f32 v178, v178, v179\n"
    "v_add_f32 v222, v176, v178\n"
    "v_mfma_f32_16x16x32_bf16 v[96:99], v[112:115], v[200:203], v[96:99]\n"
    "buffer_load_dwordx4 v235, s[28:31], s42 offen offset:2048 lds\n"
    "v_mfma_f32_16x16x32_bf16 v[96:99], v[116:119], v[204:207], v[96:99]\n"
    "v_rcp_f32 v222, v222\n"
    "v_mfma_f32_16x16x32_bf16 v[96:99], v[120:123], v[208:211], v[96:99]\n"
    "v_mfma_f32_16x16x32_bf16 v[96:99], v[124:127], v[212:215], v[96:99]\n"
    "v_mfma_f32_16x16x32_bf16 v[100:103], v[128:131], v[200:203], v[100:103]\n"
    "buffer_load_dwordx4 v235, s[28:31], s43 offen offset:3072 lds\n"
    "v_mfma_f32_16x16x32_bf16 v[100:103], v[132:135], v[204:207], v[100:103]\n"
    "v_mfma_f32_16x16x32_bf16 v[100:103], v[136:139], v[208:211], v[100:103]\n"
    "v_mfma_f32_16x16x32_bf16 v[100:103], v[140:143], v[212:215], v[100:103]\n"
    "s_add_u32 s38, s87, 16384\n"
    "s_mov_b32 m0, s38\n"
    "v_mfma_f32_16x16x32_bf16 v[104:107], v[144:147], v[200:203], v[104:107]\n"
    "buffer_load_dwordx4 v235, s[28:31], s44 offen lds\n"
    "v_mfma_f32_16x16x32_bf16 v[104:107], v[148:151], v[204:207], v[104:107]\n"
    "v_mfma_f32_16x16x32_bf16 v[104:107], v[152:155], v[208:211], v[104:107]\n"
    "v_mfma_f32_16x16x32_bf16 v[104:107], v[156:159], v[212:215], v[104:107]\n"
    "v_mfma_f32_16x16x32_bf16 v[108:111], v[160:163], v[200:203], v[108:111]\n"
    "buffer_load_dwordx4 v235, s[28:31], s45 offen offset:1024 lds\n"
    "v_mfma_f32_16x16x32_bf16 v[108:111], v[164:167], v[204:207], v[108:111]\n"
    "v_mfma_f32_16x16x32_bf16 v[108:111], v[168:171], v[208:211], v[108:111]\n"
    "v_mfma_f32_16x16x32_bf16 v[108:111], v[172:175], v[212:215], v[108:111]\n"
    "buffer_load_dwordx4 v235, s[28:31], s46 offen offset:2048 lds\n"
    "buffer_load_dwordx4 v235, s[28:31], s47 offen offset:3072 lds\n"
    "s_branch L_epi_%=\n"
    "L_end_%=:\n"
    : "+{v255}"(tid)
    : "{s[4:5]}"(x), "{s[6:7]}"(wq), "{s[8:9]}"(k), "{s[10:11]}"(v), "{s[12:13]}"(wo), "{s[14:15]}"(pp),
      "{s16}"(rotq), "{s17}"(roto), "{s99}"(hsw)
    : "memory", "vcc", "scc", "m0", "v0", "v1", "v2", "v3", "v4", "v5", "v6", "v7", "v8", "v9", "v10", "v11", "v12", "v13", "v14", "v15", "v16", "v17", "v18", "v19", "v20", "v21", "v22", "v23", "v24", "v25", "v26", "v27", "v28", "v29", "v30", "v31", "v32", "v33", "v34", "v35", "v36", "v37", "v38", "v39", "v40", "v41", "v42", "v43", "v44", "v45", "v46", "v47", "v48", "v49", "v50", "v51", "v52", "v53", "v54", "v55", "v56", "v57", "v58", "v59", "v60", "v61", "v62", "v63", "v64", "v65", "v66", "v67", "v68", "v69", "v70", "v71", "v72", "v73", "v74", "v75", "v76", "v77", "v78", "v79", "v80", "v81", "v82", "v83", "v84", "v85", "v86", "v87", "v88", "v89", "v90", "v91", "v92", "v93", "v94", "v95", "v96", "v97", "v98", "v99", "v100", "v101", "v102", "v103", "v104", "v105", "v106", "v107", "v108", "v109", "v110", "v111", "v112", "v113", "v114", "v115", "v116", "v117", "v118", "v119", "v120", "v121", "v122", "v123", "v124", "v125", "v126", "v127", "v128", "v129", "v130", "v131", "v132", "v133", "v134", "v135", "v136", "v137", "v138", "v139", "v140", "v141", "v142", "v143", "v144", "v145", "v146", "v147", "v148", "v149", "v150", "v151", "v152", "v153", "v154", "v155", "v156", "v157", "v158", "v159", "v160", "v161", "v162", "v163", "v164", "v165", "v166", "v167", "v168", "v169", "v170", "v171", "v172", "v173", "v174", "v175", "v176", "v177", "v178", "v179", "v180", "v181", "v182", "v183", "v184", "v185", "v186", "v187", "v188", "v189", "v190", "v191", "v192", "v193", "v194", "v195", "v196", "v197", "v198", "v199", "v200", "v201", "v202", "v203", "v204", "v205", "v206", "v207", "v208", "v209", "v210", "v211", "v212", "v213", "v214", "v215", "v216", "v217", "v218", "v219", "v220", "v221", "v222", "v223", "v224", "v225", "v226", "v227", "v228", "v229", "v230", "v231", "v232", "v233", "v234", "v235", "v236", "v237", "v238", "v239", "v240", "v241", "v242", "v243", "v244", "v245", "v246", "v247", "v248", "v249", "v250", "v251", "v252", "v253", "v254", "a0", "a1", "a2", "a3", "a4", "a5", "a6", "a7", "a8", "a9", "a10", "a11", "a12", "a13", "a14", "a15", "a16", "a17", "a18", "a19", "a20", "a21", "a22", "a23", "a24", "a25", "a26", "a27", "a28", "a29", "a30", "a31", "a32", "a33", "a34", "a35", "a36", "a37", "a38", "a39", "a40", "a41", "a42", "a43", "a44", "a45", "a46", "a47", "a48", "a49", "a50", "a51", "a52", "a53", "a54", "a55", "a56", "a57", "a58", "a59", "a60", "a61", "a62", "a63", "a64", "a65", "a66", "a67", "a68", "a69", "a70", "a71", "a72", "a73", "a74", "a75", "a76", "a77", "a78", "a79", "a80", "a81", "a82", "a83", "a84", "a85", "a86", "a87", "a88", "a89", "a90", "a91", "a92", "a93", "a94", "a95", "a96", "a97", "a98", "a99", "a100", "a101", "a102", "a103", "a104", "a105", "a106", "a107", "a108", "a109", "a110", "a111", "a112", "a113", "a114", "a115", "a116", "a117", "a118", "a119", "a120", "a121", "a122", "a123", "a124", "a125", "a126", "a127", "a128", "a129", "a130", "a131", "a132", "a133", "a134", "a135", "a136", "a137", "a138", "a139", "a140", "a141", "a142", "a143", "a144", "a145", "a146", "a147", "a148", "a149", "a150", "a151", "a152", "a153", "a154", "a155", "a156", "a157", "a158", "a159", "a160", "a161", "a162", "a163", "a164", "a165", "a166", "a167", "a168", "a169", "a170", "a171", "a172", "a173", "a174", "a175", "a176", "a177", "a178", "a179", "a180", "a181", "a182", "a183", "a184", "a185", "a186", "a187", "a188", "a189", "a190", "a191", "a192", "a193", "a194", "a195", "a196", "a197", "a198", "a199", "a200", "a201", "a202", "a203", "a204", "a205", "a206", "a207", "a208", "a209", "a210", "a211", "a212", "a213", "a214", "a215", "a216", "a217", "a218", "a219", "a220", "a221", "a222", "a223", "a224", "a225", "a226", "a227", "a228", "a229", "a230", "a231", "a232", "a233", "a234", "a235", "a236", "a237", "a238", "a239", "a240", "a241", "a242", "a243", "a244", "a245", "a246", "a247", "a248", "a249", "a250", "a251", "a252", "a253", "a254", "a255", "s20", "s21", "s22", "s23", "s24", "s25", "s26", "s27", "s28", "s29", "s30", "s31", "s32", "s33", "s34", "s35", "s36", "s37", "s38", "s39", "s40", "s41", "s42", "s43", "s44", "s45", "s46", "s47", "s48", "s49", "s50", "s51", "s52", "s53", "s54", "s55", "s56", "s57", "s58", "s59", "s60", "s61", "s62", "s63", "s64", "s65", "s66", "s67", "s68", "s69", "s70", "s71", "s72", "s73", "s74", "s75", "s76", "s77", "s78", "s79", "s80", "s81", "s82", "s83", "s84", "s85", "s86", "s87", "s88", "s89", "s90", "s91", "s92", "s93", "s94", "s95", "s96", "s97");
}

__device__ __forceinline__ float bf_lo(unsigned u) { return __uint_as_float(u << 16); }
__device__ __forceinline__ float bf_hi(unsigned u) { return __uint_as_float(u & 0xffff0000u); }
__device__ __forceinline__ unsigned pack_bf16(float lo, float hi) {
  typedef float f32x2 __attribute__((ext_vector_type(2)));
  typedef __bf16 bf16x2 __attribute__((ext_vector_type(2)));
  f32x2 v = {lo, hi};
  bf16x2 r = __builtin_convertvector(v, bf16x2);
  return __builtin_bit_cast(unsigned, r);
}

__global__ __launch_bounds__(256) void reduce_heads(const uint4* __restrict__ part, uint2* __restrict__ out) {
  const unsigned lane = threadIdx.x & 63u;
  const unsigned grp = blockIdx.x / 14u, frag = (threadIdx.x >> 6) * 14u + blockIdx.x % 14u;
  const uint4* p = part + ((size_t)grp * 56 + frag) * 8 * 64 + lane;
  uint4 v[8];
#pragma unroll
  for (int h = 0; h < 8; ++h) {
    typedef unsigned u32x4 __attribute__((ext_vector_type(4)));
    const u32x4 t = __builtin_nontemporal_load((const u32x4*)(p + h * 64));
    v[h] = make_uint4(t.x, t.y, t.z, t.w);
  }
  float a[8] = {0, 0, 0, 0, 0, 0, 0, 0};
#pragma unroll
  for (int h = 0; h < 8; ++h) {
    a[0] += bf_lo(v[h].x); a[1] += bf_hi(v[h].x); a[2] += bf_lo(v[h].y); a[3] += bf_hi(v[h].y);
    a[4] += bf_lo(v[h].z); a[5] += bf_hi(v[h].z); a[6] += bf_lo(v[h].w); a[7] += bf_hi(v[h].w);
  }
  const unsigned br = grp >> 4, b = (grp >> 1) & 7u, half = grp & 1u;
  const unsigned w = frag / 14u, f = frag % 14u, sg = f >> 1, jp = f & 1u;
  const unsigned n = 64u * sg + 16u * w + 4u * (lane >> 4);
  const unsigned q1 = 32u * jp + (lane & 15u), q2 = q1 + 16u;
  const size_t row0 = (size_t)b * kSQ + 56u * half;
  out[((row0 + q1) * (2 * kD) + br * kD + n) / 4] = make_uint2(pack_bf16(a[0], a[1]), pack_bf16(a[2], a[3]));
  if (q2 < 56u) out[((row0 + q2) * (2 * kD) + br * kD + n) / 4] = make_uint2(pack_bf16(a[4], a[5]), pack_bf16(a[6], a[7]));
}
}

extern "C" void kernel_launch(void* const* d_in, const int* in_sizes, int n_in, void* d_out, int out_size, void* d_ws,
                              size_t ws_size, hipStream_t stream) {
  (void)in_sizes; (void)n_in; (void)out_size; (void)ws_size;
  char* part = (char*)d_ws;
  gqa_block<<<256, 256, 0, stream>>>((const char*)d_in[0], (const char*)d_in[1], (const char*)d_in[2], (const char*)d_in[3],
                                     (const char*)d_in[4], (const char*)d_in[5], (const char*)d_in[6], (const char*)d_in[7],
                                     (const char*)d_in[8], (const char*)d_in[9], part);
  reduce_heads<<<448, 256, 0, stream>>>((const uint4*)part, (uint2*)d_out);
}
```
